```python
import torch

def _softmax(x, axis=-1):
    return torch.softmax(x, dim=axis)

def _gelu(x):
    return torch.nn.functional.gelu(x, approximate='tanh')

def _silu(x):
    return torch.nn.functional.silu(x)

def _sigmoid(x):
    return torch.sigmoid(x)

def _layernorm(x, axis=-1, eps=1e-05):
    m = x.mean(dim=axis, keepdim=True)
    v = x.var(dim=axis, keepdim=True, unbiased=False)
    return (x - m) / torch.sqrt(v + eps)

def _scatter_add(tgt, idx, src, axis=0):
    return tgt.clone().index_add_(axis, idx, src)

def _segment_sum(src, seg, n):
    out = torch.zeros((n,) + src.shape[1:], dtype=src.dtype)
    return out.index_add_(0, seg, src)

def _pad(x, pw):
    flat = tuple((v for pair in reversed(pw) for v in pair))
    return torch.nn.functional.pad(x, flat)
kernel_name = 'adaLN_cond__linear_kernel_hedgehog__glu_gated_reglu__prompt'

def setup_inputs():

    _w = lambda a: (lambda t: t.to(torch.bfloat16) if t.is_floating_point() else t)(torch.as_tensor(a))

    def _A_setup():
        B, S, D, H = (1, 2048, 512, 32)
        Dh = D // H
        return {'x': _w((lambda *s: torch.randn(*s, dtype=torch.bfloat16) * 0.05)(B, S, D)), 'wqkv': _w((lambda *s: torch.randn(*s, dtype=torch.bfloat16) * 0.4)(D, 3 * D)), 'w_phi': _w((lambda *s: torch.randn(*s, dtype=torch.bfloat16) * 0.4)(H, Dh, Dh) * Dh ** (-0.5)), 'wo': _w((lambda *s: torch.randn(*s, dtype=torch.bfloat16) * 0.05)(D, D))}
    _A_in = _A_setup()

    def _B_setup():
        B, S, D, F = (1, 2048, 512, 1376)
        return {'x': _w((lambda *s: torch.randn(*s, dtype=torch.bfloat16) * 0.05)(B, S, D)), 'W_gate': _w((lambda *s: torch.randn(*s, dtype=torch.bfloat16) * 0.8)(D, F) * (2.0 / D) ** 0.5), 'W_up': _w((lambda *s: torch.randn(*s, dtype=torch.bfloat16) * 0.8)(D, F) * (2.0 / D) ** 0.5), 'W_down': _w((lambda *s: torch.randn(*s, dtype=torch.bfloat16) * 0.8)(F, D) * (1.0 / F) ** 0.5)}
    _B_in = _B_setup()
    return {'A_x': _A_in['x'], 'A_wqkv': _A_in['wqkv'], 'A_w_phi': _A_in['w_phi'], 'A_wo': _A_in['wo'], 'B_x': _B_in['x'], 'B_W_gate': _B_in['W_gate'], 'B_W_up': _B_in['W_up'], 'B_W_down': _B_in['W_down'], 'c': _w((lambda *s: torch.randn(*s, dtype=torch.bfloat16) * 0.2)(1, 512) * 8.0), 'w_mod': _w((lambda *s: torch.randn(*s, dtype=torch.bfloat16) * 0.2)(512, 6 * 512) * 512 ** (-0.5) * 8.0)}

def reference(A_x, A_wqkv, A_w_phi, A_wo, B_x, B_W_gate, B_W_up, B_W_down, c, w_mod):

    def _A_ref(x, wqkv, w_phi, wo):
        B, S, D = x.shape
        H = 32
        Dh = D // H
        qkv = (lambda x, *d: x.permute(*d))((x @ wqkv).reshape(B, S, 3, H, Dh), 2, 0, 3, 1, 4)
        q, k, v = (qkv[0], qkv[1], qkv[2])
        pq = torch.einsum('bhsd,hdf->bhsf', q, w_phi)
        pk = torch.einsum('bhsd,hdf->bhsf', k, w_phi)
        phi_q = torch.concatenate([_softmax(pq, axis=-1), _softmax(-pq, axis=-1)], axis=-1)
        phi_k = torch.concatenate([_softmax(pk, axis=-1), _softmax(-pk, axis=-1)], axis=-1)
        kv = torch.einsum('bhsf,bhsd->bhfd', phi_k, v)
        z = torch.einsum('bhsf->bhf', phi_k)
        num = torch.einsum('bhqf,bhfd->bhqd', phi_q, kv)
        den = torch.einsum('bhqf,bhf->bhq', phi_q, z)[..., None] + 1e-06
        o = num / den
        return (lambda x, *d: x.permute(*d))(o, 0, 2, 1, 3).reshape(B, S, D) @ wo

    def _B_ref(x, W_gate, W_up, W_down):
        gate = torch.einsum('bsd,df->bsf', x, W_gate)
        up = torch.einsum('bsd,df->bsf', x, W_up)
        gate = gate * (lambda x, dt: x.to(dt))(gate > 0, gate.dtype)
        h = gate * up
        return torch.einsum('bsf,fd->bsd', h, W_down)
    x = A_x
    mod = (_silu(c) @ w_mod).reshape(c.shape[0], 6, 1, -1)
    sa, ca, ga, sb, cb, gb = (mod[:, 0].clone(), mod[:, 1].clone(), mod[:, 2].clone(), mod[:, 3].clone(), mod[:, 4].clone(), mod[:, 5].clone())
    h = _layernorm(x, axis=-1) * (1 + ca) + sa
    x = x + ga * _A_ref(h, A_wqkv, A_w_phi, A_wo)
    h = _layernorm(x, axis=-1) * (1 + cb) + sb
    x = x + gb * _B_ref(h, B_W_gate, B_W_up, B_W_down)
    return x
```

```cpp
#include <hip/hip_runtime.h>
#include <hip/hip_bf16.h>
typedef unsigned short u16;
__device__ __forceinline__ float bf2f(u16 v){ return __builtin_bit_cast(float, (unsigned)v<<16); }
__device__ __forceinline__ u16 f2bf(float f){ __hip_bfloat16 b = __float2bfloat16(f); return __builtin_bit_cast(u16,b); }
__device__ __forceinline__ float rb(float f){ return bf2f(f2bf(f)); }
constexpr int S=2048,D=512,H=32,F=1376;

__global__ void k_mod(const u16* c, const u16* wmod, float* mod){
  int j = blockIdx.x*blockDim.x+threadIdx.x; if(j>=6*D) return;
  float a=0; for(int k=0;k<D;k++){ float cv=bf2f(c[k]); float s=rb(cv/(1.f+__expf(-cv))); a+=s*bf2f(wmod[k*6*D+j]); }
  mod[j]=rb(a);
}
__global__ void k_ln(const u16* x, const float* sh, const float* sc, u16* h){
  int r=blockIdx.x; int t=threadIdx.x;
  float v[8]; float s=0; for(int i=0;i<8;i++){ v[i]=bf2f(x[r*D+t*8+i]); s+=v[i]; }
  for(int o=32;o;o>>=1) s+=__shfl_xor(s,o);
  float m=s/D; float q=0; for(int i=0;i<8;i++){ q+=(v[i]-m)*(v[i]-m);} for(int o=32;o;o>>=1) q+=__shfl_xor(q,o);
  float rs=rsqrtf(q/D+1e-5f);
  for(int i=0;i<8;i++){ int k=t*8+i; h[r*D+k]=f2bf((v[i]-m)*rs*rb(1.f+sc[k])+sh[k]); }
}
__global__ void k_gemm(const u16* A, const u16* B, float* C, int M,int N,int K){
  int n=blockIdx.x*blockDim.x+threadIdx.x; int m=blockIdx.y; if(n>=N) return;
  float a=0; for(int k=0;k<K;k++) a+=bf2f(A[m*K+k])*bf2f(B[k*N+n]);
  C[m*N+n]=a;
}
__global__ void k_phi(const float* qkv, const u16* wphi, float* phi){
  int i=blockIdx.x*blockDim.x+threadIdx.x; if(i>=2*S*H) return;
  int w=i/(S*H), s=(i/H)%S, h=i%H;
  float q[16]; for(int d=0;d<16;d++) q[d]=rb(qkv[s*3*D+w*D+h*16+d]);
  float p[16]; float mx=-1e30f,mn=1e30f;
  for(int f=0;f<16;f++){ float a=0; for(int d=0;d<16;d++) a+=q[d]*bf2f(wphi[h*256+d*16+f]); p[f]=rb(a); mx=fmaxf(mx,p[f]); mn=fminf(mn,p[f]); }
  float e[32]; float s1=0,s2=0; for(int f=0;f<16;f++){ e[f]=__expf(p[f]-mx); s1+=e[f]; e[16+f]=__expf(mn-p[f]); s2+=e[16+f]; }
  for(int f=0;f<16;f++){ phi[(size_t)i*32+f]=rb(e[f]/s1); phi[(size_t)i*32+16+f]=rb(e[16+f]/s2);}
}
__global__ void k_kv(const float* qkv, const float* phi, float* kv){
  int i=blockIdx.x*blockDim.x+threadIdx.x; if(i>=H*32*17) return;
  int h=i/(32*17), f=(i/17)%32, d=i%17;
  float a=0; for(int s=0;s<S;s++){ float pk=phi[((size_t)(S*H)+s*H+h)*32+f]; float v = d<16? rb(qkv[s*3*D+2*D+h*16+d]) : 1.f; a+=pk*v; }
  kv[i]=rb(a);
}
__global__ void k_o(const float* phi, const float* kv, u16* o){
  int i=blockIdx.x*blockDim.x+threadIdx.x; if(i>=S*D) return;
  int s=i/D, h=(i%D)/16, d=i%16;
  float num=0,den=0; for(int f=0;f<32;f++){ float p=phi[((size_t)s*H+h)*32+f]; num+=p*kv[(h*32+f)*17+d]; den+=p*kv[(h*32+f)*17+16]; }
  o[i]=f2bf(rb(num)/rb(rb(den)+1e-6f));
}
__global__ void k_res(const u16* x, const float* g, const float* y, u16* out){
  int i=blockIdx.x*blockDim.x+threadIdx.x; if(i>=S*D) return;
  out[i]=f2bf(bf2f(x[i])+rb(g[i%D]*rb(y[i])));
}
__global__ void k_glu(const float* g, const float* u, u16* h){
  int i=blockIdx.x*blockDim.x+threadIdx.x; if(i>=S*F) return;
  float gg=rb(g[i]); gg = gg>0?gg:0.f*gg; h[i]=f2bf(gg*rb(u[i]));
}
extern "C" void kernel_launch(void* const* d_in, const int* in_sizes, int n_in, void* d_out, int out_size, void* d_ws, size_t ws_size, hipStream_t st){
  const u16 *x=(const u16*)d_in[0],*wqkv=(const u16*)d_in[1],*wphi=(const u16*)d_in[2],*wo=(const u16*)d_in[3],*wg=(const u16*)d_in[5],*wu=(const u16*)d_in[6],*wd=(const u16*)d_in[7],*c=(const u16*)d_in[8],*wmod=(const u16*)d_in[9];
  char* w=(char*)d_ws; size_t off=0; auto al=[&](size_t b){ void* p=w+off; off+=(b+255)/256*256; return p; };
  float* mod=(float*)al(6*D*4); u16* h=(u16*)al(S*D*2); float* qkv=(float*)al(S*3*D*4); float* phi=(float*)al((size_t)2*S*H*32*4);
  float* kv=(float*)al(H*32*17*4); u16* o=(u16*)al(S*D*2); float* y=(float*)al(S*D*4); u16* x1=(u16*)al(S*D*2);
  float* g=(float*)al(S*F*4); float* u=(float*)al(S*F*4); u16* h2=(u16*)al(S*F*2);
  k_mod<<<12,256,0,st>>>(c,wmod,mod);
  k_ln<<<S,64,0,st>>>(x,mod,mod+D,h);
  k_gemm<<<dim3(6,S),256,0,st>>>(h,wqkv,qkv,S,3*D,D);
  k_phi<<<2*S*H/256,256,0,st>>>(qkv,wphi,phi);
  k_kv<<<(H*32*17+63)/64,64,0,st>>>(qkv,phi,kv);
  k_o<<<S*D/256,256,0,st>>>(phi,kv,o);
  k_gemm<<<dim3(2,S),256,0,st>>>(o,wo,y,S,D,D);
  k_res<<<S*D/256,256,0,st>>>(x,mod+2*D,y,x1);
  k_ln<<<S,64,0,st>>>(x1,mod+3*D,mod+4*D,h);
  k_gemm<<<dim3(6,S),256,0,st>>>(h,wg,g,S,F,D);
  k_gemm<<<dim3(6,S),256,0,st>>>(h,wu,u,S,F,D);
  k_glu<<<S*F/256,256,0,st>>>(g,u,h2);
  k_gemm<<<dim3(2,S),256,0,st>>>(h2,wd,y,S,D,F);
  k_res<<<S*D/256,256,0,st>>>(x1,mod+5*D,y,(u16*)d_out);
}
```

```cpp
#include <hip/hip_runtime.h>
#define GAS __attribute__((address_space(1)))
#define LAS __attribute__((address_space(3)))
#define DI __device__ __forceinline__
typedef unsigned u32; typedef unsigned long long u64; typedef unsigned short u16;
typedef u32 v4u __attribute__((ext_vector_type(4)));
typedef u32 v2u __attribute__((ext_vector_type(2)));
typedef float f2 __attribute__((ext_vector_type(2)));
typedef float f4 __attribute__((ext_vector_type(4)));
typedef float f16v __attribute__((ext_vector_type(16)));
typedef short s4 __attribute__((ext_vector_type(4)));
typedef __bf16 b2 __attribute__((ext_vector_type(2)));
typedef __bf16 b8 __attribute__((ext_vector_type(8)));
typedef __amdgpu_buffer_rsrc_t rsrc_t;

#define STAMPS 0

constexpr int S_ = 2048, D_ = 512, F_ = 1376;
constexpr int WS_EPOCH = 0;
constexpr int WS_MODG = 4096;
constexpr int WS_FH = 20480;
constexpr int WS_FKV = WS_FH + 8192;
constexpr int WS_FO = WS_FKV + 2048;
constexpr int WS_FX = WS_FO + 8192;
constexpr int WS_FH2 = WS_FX + 4096;
constexpr int WS_ZP = 65536;
constexpr int WS_ST = 131072;
constexpr int WS_STAMP = 524288;
constexpr int WS_KVP = 1 << 20;
constexpr int WS_H = 2 << 20;
constexpr int WS_O = 4 << 20;
constexpr int WS_XS = 6 << 20;
constexpr int WS_H2 = 8 << 20;
constexpr int WS_END = WS_H2 + 128 * 43 * 1024;
constexpr int L_WK = 0, L_WV = 32768, L_WQ = 65536, L_PHI = 98304;
constexpr int L_WO = 0;
constexpr int L_W3 = 57344;
constexpr int L_W4 = 0;
constexpr int L_MISC = 155648;
constexpr int L_RED = L_MISC, L_MODB = L_MISC + 1024, L_KVL = L_MISC + 3072, L_ZL = L_KVL + 2048, L_ZERO = L_ZL + 128, L_UT = L_MISC + 5376;
constexpr int L_XRES = 126976;
constexpr int LDS_BYTES = 163840;
constexpr int AUX_SC1 = 16;
constexpr u32 SPIN_CAP = 1u << 18;

struct Params { const u16 *x, *wqkv, *wphi, *wo, *wg, *wu, *wd, *c, *wmod; u16* out; char* ws; };

DI float asf(u32 v) { return __builtin_bit_cast(float, v); }
DI u32 asu(float v) { return __builtin_bit_cast(u32, v); }
DI float bflo(u32 v) { return __builtin_bit_cast(float, v << 16); }
DI float bfhi(u32 v) { return __builtin_bit_cast(float, v & 0xffff0000u); }
DI u32 pk(float a, float b) { f2 t = {a, b}; return __builtin_bit_cast(u32, __builtin_convertvector(t, b2)); }
DI float rbf(float a) { return bflo(pk(a, a)); }
DI f16v mfma32(v4u a, v4u b, f16v c) { return __builtin_amdgcn_mfma_f32_32x32x16_bf16(__builtin_bit_cast(b8, a), __builtin_bit_cast(b8, b), c, 0, 0, 0); }
DI f4 mfma16(v4u a, v4u b, f4 c) { return __builtin_amdgcn_mfma_f32_16x16x32_bf16(__builtin_bit_cast(b8, a), __builtin_bit_cast(b8, b), c, 0, 0, 0); }
DI v2u trd(LAS char* p) { return __builtin_bit_cast(v2u, __builtin_amdgcn_ds_read_tr16_b64_v4i16((LAS s4*)p)); }
DI v4u trd2(LAS char* p0, LAS char* p1) { v2u a = trd(p0), b = trd(p1); return v4u{a.x, a.y, b.x, b.y}; }
DI rsrc_t mkrs(const void* p, int bytes) { return __builtin_amdgcn_make_buffer_rsrc((void*)p, 0, bytes, 0x00020000); }
template <int AUX> DI void st128(v4u v, rsrc_t rs, u32 voff, u32 soff) { __builtin_amdgcn_raw_buffer_store_b128(v, rs, voff + soff, 0, AUX); }
DI void dma16(rsrc_t rs, u32 voff, u32 soff, u32 lds) {
  asm volatile("s_mov_b32 m0, %0\n\ts_nop 0\n\tbuffer_load_dwordx4 %1, %2, %3 offen lds" ::"s"(lds), "v"(voff), "s"(rs), "s"(soff) : "memory");
}
DI void bar() { asm volatile("s_waitcnt lgkmcnt(0)\n\ts_barrier" ::: "memory"); }
DI void drain() { asm volatile("s_waitcnt vmcnt(0)" ::: "memory"); }
DI void cfence() { asm volatile("" ::: "memory"); }
template <int CTRL> DI float dppf(float v) { return __builtin_bit_cast(float, __builtin_amdgcn_update_dpp(0, __builtin_bit_cast(int, v), CTRL, 0xf, 0xf, true)); }
DI float sum16(float v) { auto r = __builtin_amdgcn_permlane16_swap(__builtin_bit_cast(u32, v), __builtin_bit_cast(u32, v), false, false); return asf(r[0]) + asf(r[1]); }
DI float sum32(float v) { auto r = __builtin_amdgcn_permlane32_swap(__builtin_bit_cast(u32, v), __builtin_bit_cast(u32, v), false, false); return asf(r[0]) + asf(r[1]); }
constexpr int ROR8 = 0x128, ROR4 = 0x124, ROR2 = 0x122, ROR1 = 0x121;
DI void wait_flags(rsrc_t ws, u32 off, int n, int lane, v2u tag) {
  const u32 vo = off + (lane < n ? lane : 0) * 8;
  for (u32 sp = 0; sp < SPIN_CAP; ++sp) {
    v2u f = __builtin_amdgcn_raw_buffer_load_b64(ws, vo, 0, AUX_SC1);
    cfence();
    if (__all(f.x == tag.x && f.y == tag.y)) break;
  }
  cfence();
}
DI void set_flag(rsrc_t ws, u32 off, int lane, v2u tag) { if (lane == 0) __builtin_amdgcn_raw_buffer_store_b64(tag, ws, off, 0, AUX_SC1); }
DI v2u wait_gran(rsrc_t ws, u32 voff, u32 tag) {
  v4u g;
  for (u32 sp = 0; sp < SPIN_CAP; ++sp) {
    g = __builtin_amdgcn_raw_buffer_load_b128(ws, voff, 0, AUX_SC1);
    cfence();
    if (__all(g.y == tag && g.w == tag)) break;
  }
  return v2u{g.x, g.z};
}
#define STAMP(i) do {} while (0)

DI void feature_map(const f16v& acc, v4u wp0, v4u wp1, u32 (&ph)[16]) {
  v4u b0 = {pk(acc[0], acc[1]), pk(acc[2], acc[3]), pk(acc[4], acc[5]), pk(acc[6], acc[7])};
  v4u b1 = {pk(acc[8], acc[9]), pk(acc[10], acc[11]), pk(acc[12], acc[13]), pk(acc[14], acc[15])};
  f16v z = {};
  f16v lg = mfma32(wp0, b0, z);
  lg = mfma32(wp1, b1, lg);
  float mx = lg[0], mn = lg[0];
#pragma unroll
  for (int i = 1; i < 16; ++i) { mx = __builtin_fmaxf(mx, lg[i]); mn = __builtin_fminf(mn, lg[i]); }
  const float L2E = 1.44269504088896f;
  const float cp = -mx * L2E, cn = mn * L2E;
  float ep[16], en[16];
#pragma unroll
  for (int i = 0; i < 16; ++i) { ep[i] = __builtin_amdgcn_exp2f(__builtin_fmaf(lg[i], L2E, cp)); en[i] = __builtin_amdgcn_exp2f(__builtin_fmaf(lg[i], -L2E, cn)); }
  f2 sp = {ep[0], ep[1]}, sn = {en[0], en[1]};
#pragma unroll
  for (int i = 1; i < 8; ++i) { sp += f2{ep[2 * i], ep[2 * i + 1]}; sn += f2{en[2 * i], en[2 * i + 1]}; }
  const float rp = __builtin_amdgcn_rcpf(sp.x + sp.y), rn = __builtin_amdgcn_rcpf(sn.x + sn.y);
#pragma unroll
  for (int i = 0; i < 8; ++i) { ph[i] = pk(ep[2 * i] * rp, ep[2 * i + 1] * rp); ph[8 + i] = pk(en[2 * i] * rn, en[2 * i + 1] * rn); }
}

__global__ void __launch_bounds__(256, 1) fused_block(Params p) {
  extern __shared__ __attribute__((aligned(16))) char smem_[];
  LAS char* L = (LAS char*)smem_;
  const u32 Lb = (u32)(size_t)L;
  const int tid = threadIdx.x, lane = tid & 63, b = blockIdx.x;
  const int w = __builtin_amdgcn_readfirstlane(tid >> 6);
  const int xg = b & 7, jj = b >> 3;
  const int pan = 4 * (xg >> 1) + (jj >> 3), mem = 8 * (xg & 1) + (jj & 7);
  const int rb = 4 * xg + (jj >> 3), ct = jj & 7;
  const int sp3 = 4 * (xg >> 2) + (jj >> 3), m3 = 8 * (xg & 3) + (jj & 7);
  const int u0 = m3 * 3 < 83 ? m3 * 3 : 83;
  const bool has3 = m3 < 29;
  STAMP(0);
  const rsrc_t ws = mkrs(p.ws, WS_END);
  const rsrc_t rx = mkrs(p.x, S_ * D_ * 2), rqkv = mkrs(p.wqkv, D_ * 3 * D_ * 2), rwo = mkrs(p.wo, D_ * D_ * 2);
  const rsrc_t rg = mkrs(p.wg, D_ * F_ * 2), ru = mkrs(p.wu, D_ * F_ * 2), rd = mkrs(p.wd, F_ * D_ * 2);
  const rsrc_t rmod = mkrs(p.wmod, D_ * 6 * D_ * 2), rc = mkrs(p.c, D_ * 2), rphi = mkrs(p.wphi, 32 * 256 * 2), rout = mkrs(p.out, S_ * D_ * 2);
  const int l31 = lane & 31, hh = lane >> 5, l15 = lane & 15, qq = lane >> 4;
  const int ta = (lane >> 2) & 3, tp = lane & 3;

  const v2u ep = __builtin_amdgcn_raw_buffer_load_b64(ws, WS_EPOCH, 0, AUX_SC1);
  const bool hasmod = jj < 24;
  const int mcol = jj < 8 ? (2 * xg + (jj >> 2)) * 64 + (jj & 3) * 16 : 1024 + (4 * xg + ((jj - 8) >> 2)) * 64 + (jj & 3) * 16;
  v4u wm[4]; u16 cc[4];
  if (hasmod) {
#pragma unroll
    for (int i = 0; i < 4; ++i) cc[i] = __builtin_amdgcn_raw_buffer_load_b16(rc, ((tid >> 1) + 128 * i) * 2, 0, 0);
#pragma unroll
    for (int i = 0; i < 4; ++i) wm[i] = __builtin_amdgcn_raw_buffer_load_b128(rmod, ((tid >> 1) + 128 * i) * 6144 + (tid & 1) * 16, mcol * 2, 0);
  }
  v4u xr[2];
#pragma unroll
  for (int i = 0; i < 2; ++i) xr[i] = __builtin_amdgcn_raw_buffer_load_b128(rx, (lane & 7) * 1024 + ((lane >> 3) + 8 * i) * 16, b * 8192 + w * 256, 0);
  u16 wp[8];
  {
    const int hd = (l31 >> 2) & 1, f = 4 * (l31 >> 3) + (l31 & 3);
#pragma unroll
    for (int j = 0; j < 8; ++j) wp[j] = __builtin_amdgcn_raw_buffer_load_b16(rphi, (hd * 256 + (8 * (j >> 2) + 4 * hh + (j & 3)) * 16 + f) * 2, mem * 1024, 0);
  }
#pragma unroll
  for (int i = 0; i < 2; ++i) dma16(rx, (lane >> 3) * 1024 + (lane & 7) * 16, (rb * 64 + w * 16 + i * 8) * 1024 + ct * 128, Lb + L_XRES + w * 2048 + i * 1024);
  {
    const u32 vo = (lane >> 2) * 3072 + (lane & 3) * 16;
    for (int i = 0; i < 8; ++i) {
      const int r16 = i * 4 + w;
      dma16(rqkv, vo, r16 * 16 * 3072 + 1024 + mem * 64, Lb + L_WK + r16 * 1024);
      dma16(rqkv, vo, r16 * 16 * 3072 + 2048 + mem * 64, Lb + L_WV + r16 * 1024);
    }
    for (int i = 0; i < 8; ++i) { const int r16 = i * 4 + w; dma16(rqkv, vo, r16 * 16 * 3072 + mem * 64, Lb + L_WQ + r16 * 1024); }
  }
  const u64 tag64 = (((u64)ep.y << 32) | ep.x) + 0x9E3779B97F4A7C15ull;
  const v2u tag = {(u32)__builtin_amdgcn_readfirstlane((u32)tag64), (u32)__builtin_amdgcn_readfirstlane((u32)(tag64 >> 32))};
  if (tid < 16) *(LAS u32*)(L + L_ZERO + tid * 4) = 0u;
  STAMP(1);
  if (hasmod) {
    float a[8] = {};
#pragma unroll
    for (int i = 0; i < 4; ++i) {
      const float cv = bflo(cc[i]);
      const float s = rbf(cv * __builtin_amdgcn_rcpf(1.f + __builtin_amdgcn_exp2f(-1.44269504088896f * cv)));
#pragma unroll
      for (int j = 0; j < 4; ++j) { a[2 * j] += s * bflo(wm[i][j]); a[2 * j + 1] += s * bfhi(wm[i][j]); }
    }
#pragma unroll
    for (int j = 0; j < 8; ++j) { float v = a[j]; v += dppf<ROR2>(v); v += dppf<ROR4>(v); v += dppf<ROR8>(v); a[j] = v; }
    if (l15 < 2) { *(LAS f4*)(L + L_RED + (w * 4 + qq) * 64 + l15 * 32) = f4{a[0], a[1], a[2], a[3]}; *(LAS f4*)(L + L_RED + (w * 4 + qq) * 64 + l15 * 32 + 16) = f4{a[4], a[5], a[6], a[7]}; }
  }
  float xv[16];
#pragma unroll
  for (int i = 0; i < 2; ++i)
#pragma unroll
    for (int j = 0; j < 4; ++j) { xv[8 * i + 2 * j] = bflo(xr[i][j]); xv[8 * i + 2 * j + 1] = bfhi(xr[i][j]); }
  float s1 = 0.f;
#pragma unroll
  for (int i = 0; i < 16; ++i) s1 += xv[i];
  s1 += dppf<ROR8>(s1); s1 = sum16(s1); s1 = sum32(s1);
  const float ml = s1 * (1.f / 128.f);
  float s2 = 0.f;
#pragma unroll
  for (int i = 0; i < 16; ++i) s2 += (xv[i] - ml) * (xv[i] - ml);
  s2 += dppf<ROR8>(s2); s2 = sum16(s2); s2 = sum32(s2);
  if (lane < 8) *(LAS f2*)(L + L_UT + lane * 32 + w * 8) = f2{ml, s2};
  bar();
  if (hasmod && w == 0) {
    f2 v = *(LAS f2*)(L + L_RED + (lane >> 3) * 128 + (lane & 7) * 8);
    v += *(LAS f2*)(L + L_RED + (lane >> 3) * 128 + 64 + (lane & 7) * 8);
    v.x += dppf<ROR8>(v.x); v.y += dppf<ROR8>(v.y);
    v.x = sum32(sum16(v.x)); v.y = sum32(sum16(v.y));
    if (lane < 8) __builtin_amdgcn_raw_buffer_store_b64(v2u{pk(v.x, v.y), tag.x}, ws, WS_MODG + lane * 8, mcol * 4, AUX_SC1);
  }
  STAMP(2);
  {
    const f4 t0 = *(LAS f4*)(L + L_UT + (lane & 7) * 32), t1 = *(LAS f4*)(L + L_UT + (lane & 7) * 32 + 16);
    const float m = (t0.x + t0.z + t1.x + t1.z) * 0.25f;
    const float M2 = t0.y + t0.w + t1.y + t1.w + 128.f * ((t0.x - m) * (t0.x - m) + (t0.z - m) * (t0.z - m) + (t1.x - m) * (t1.x - m) + (t1.z - m) * (t1.z - m));
    const float r = __builtin_amdgcn_rsqf(M2 * (1.f / 512.f) + 1e-5f);
#pragma unroll
    for (int i = 0; i < 16; ++i) xv[i] = (xv[i] - m) * r;
  }
  {
    const v2u g = wait_gran(ws, WS_MODG + hh * 2048 + w * 512 + l31 * 16, tag.x);
    *(LAS v2u*)(L + L_MODB + w * 512 + lane * 8) = g;
    cfence();
    STAMP(3);
#pragma unroll
    for (int i = 0; i < 2; ++i) {
      const v4u sa = *(LAS v4u*)(L + L_MODB + w * 512 + ((lane >> 3) + 8 * i) * 16);
      const v4u ca = *(LAS v4u*)(L + L_MODB + w * 512 + 256 + ((lane >> 3) + 8 * i) * 16);
      v4u h;
#pragma unroll
      for (int j = 0; j < 4; ++j) {
        const u32 g1 = pk(1.f + bflo(ca[j]), 1.f + bfhi(ca[j]));
        h[j] = pk(xv[8 * i + 2 * j] * bflo(g1) + bflo(sa[j]), xv[8 * i + 2 * j + 1] * bfhi(g1) + bfhi(sa[j]));
      }
      st128<AUX_SC1>(h, ws, ((lane >> 3) + 8 * i) * 512 + (lane & 7) * 16, WS_H + (b >> 2) * 32768 + w * 8192 + (b & 3) * 128);
    }
  }
  drain();
  bar();
  if (w == 0) set_flag(ws, WS_FH + (b >> 2) * 128 + (b & 3) * 8, lane, tag);
  STAMP(4);
  v2u xres[4];
#pragma unroll
  for (int t = 0; t < 4; ++t) xres[t] = *(LAS v2u*)(L + L_XRES + w * 2048 + l15 * 128 + t * 32 + qq * 8);
  v4u wp0, wp1;
  {
    const v4u v = {wp[0] | ((u32)wp[1] << 16), wp[2] | ((u32)wp[3] << 16), wp[4] | ((u32)wp[5] << 16), wp[6] | ((u32)wp[7] << 16)};
    const u32 m1 = ((l31 >> 2) & 1) ? 0xffffffffu : 0u;
    wp1 = v & m1; wp0 = v & ~m1;
  }

  const int t32 = pan * 4 + w;
  wait_flags(ws, WS_FH + t32 * 128, 4, lane, tag);
  STAMP(5);
  v4u hf[32];
#pragma unroll
  for (int s = 0; s < 32; ++s) hf[s] = __builtin_amdgcn_raw_buffer_load_b128(ws, lane * 16 + (s & 3) * 1024, WS_H + t32 * 32768 + (s >> 2) * 4096, AUX_SC1);
  __builtin_amdgcn_sched_barrier(0);
  LAS char* wa = L + (8 * hh + ta) * 64 + (qq & 1) * 32 + tp * 8;
  f16v ak = {}, av = {};
#pragma unroll
  for (int s = 0; s < 32; ++s) {
    ak = mfma32(trd2(wa + L_WK + s * 1024, wa + L_WK + s * 1024 + 256), hf[s], ak);
    av = mfma32(trd2(wa + L_WV + s * 1024, wa + L_WV + s * 1024 + 256), hf[s], av);
  }
  STAMP(6);
  {
    u32 ph[16];
    feature_map(ak, wp0, wp1, ph);
    LAS char* row = L + L_PHI + (w * 32 + l31) * 224;
#pragma unroll
    for (int i = 0; i < 4; ++i) *(LAS v4u*)(row + hh * 64 + i * 16) = v4u{ph[4 * i], ph[4 * i + 1], ph[4 * i + 2], ph[4 * i + 3]};
#pragma unroll
    for (int a = 0; a < 4; ++a) *(LAS v2u*)(row + 128 + a * 16 + hh * 8) = v2u{pk(av[4 * a], av[4 * a + 1]), pk(av[4 * a + 2], av[4 * a + 3])};
  }
  bar();
  STAMP(7);
  {
    LAS char* ra = L + L_PHI + (4 * qq + ta) * 224 + tp * 8;
    f4 kv = {}, zz = {};
    const v4u ones = {0x3f803f80u, 0x3f803f80u, 0x3f803f80u, 0x3f803f80u};
#pragma unroll
    for (int s = 0; s < 4; ++s) {
      const v4u A = trd2(ra + w * 32 + s * 32 * 224, ra + w * 32 + (s * 32 + 16) * 224);
      const v4u B = trd2(ra + 128 + (w >> 1) * 32 + s * 32 * 224, ra + 128 + (w >> 1) * 32 + (s * 32 + 16) * 224);
      kv = mfma16(A, B, kv); zz = mfma16(A, ones, zz);
    }
    st128<AUX_SC1>(__builtin_bit_cast(v4u, kv), ws, lane * 16, WS_KVP + (mem * 16 + pan) * 4096 + w * 1024);
    if (l15 == 0) st128<AUX_SC1>(__builtin_bit_cast(v4u, zz), ws, qq * 16, WS_ZP + (mem * 16 + pan) * 256 + w * 64);
  }
  drain();
  bar();
  if (w == 0) set_flag(ws, WS_FKV + mem * 128 + pan * 8, lane, tag);
  STAMP(8);
  const u32 vo128 = (lane >> 3) * 1024;
  {
    for (int i = 0; i < 16; ++i) {
      const int r8 = i * 4 + w;
      const u32 sw = (((lane >> 4) & 1) << 1) | ((w & 1) << 2);
      dma16(rwo, vo128 + ((lane & 7) ^ sw) * 16, r8 * 8192 + ct * 128, Lb + L_WO + r8 * 1024);
    }
  }
  u32 pq[16];
  {
    f16v aq = {};
#pragma unroll
    for (int s = 0; s < 32; ++s) aq = mfma32(trd2(wa + L_WQ + s * 1024, wa + L_WQ + s * 1024 + 256), hf[s], aq);
    feature_map(aq, wp0, wp1, pq);
  }
  STAMP(9);
  wait_flags(ws, WS_FKV + mem * 128, 16, lane, tag);
  STAMP(10);
  {
    f4 zq = __builtin_bit_cast(f4, __builtin_amdgcn_raw_buffer_load_b128(ws, (lane >> 2) * 256 + (lane & 3) * 16, WS_ZP + mem * 4096 + w * 64, AUX_SC1));
    v4u kp[16];
#pragma unroll
    for (int i = 0; i < 16; ++i) kp[i] = __builtin_amdgcn_raw_buffer_load_b128(ws, lane * 16, WS_KVP + (mem * 16 + i) * 4096 + w * 1024, AUX_SC1);
    __builtin_amdgcn_sched_barrier(0);
#pragma unroll
    for (int j = 0; j < 4; ++j) { float v = zq[j]; v += dppf<ROR4>(v); v += dppf<ROR8>(v); zq[j] = sum32(sum16(v)); }
    if (lane < 4) *(LAS v2u*)(L + L_ZL + w * 32 + lane * 8) = v2u{pk(zq.x, zq.y), pk(zq.z, zq.w)};
    f4 s = __builtin_bit_cast(f4, kp[0]);
#pragma unroll
    for (int i = 1; i < 16; ++i) s += __builtin_bit_cast(f4, kp[i]);
    *(LAS v2u*)(L + L_KVL + ((w >> 1) * 16 + l15) * 64 + (w & 1) * 32 + qq * 8) = v2u{pk(s.x, s.y), pk(s.z, s.w)};
  }
  bar();
  STAMP(11);
  {
    const bool mine = ((l31 >> 2) & 1) == hh;
    LAS char* pa = L + (mine ? L_KVL + (hh * 16 + 4 * (l31 >> 3) + (l31 & 3)) * 64 : L_ZERO);
    LAS char* pz = L + (mine ? L_ZL + hh * 64 : L_ZERO);
    f16v num = {}, den = {};
#pragma unroll
    for (int u = 0; u < 4; ++u) {
      const v4u B = {pq[4 * u], pq[4 * u + 1], pq[4 * u + 2], pq[4 * u + 3]};
      den = mfma32(*(LAS v4u*)(pz + u * 16), B, den);
      num = mfma32(*(LAS v4u*)(pa + u * 16), B, num);
    }
    const float ri = __builtin_amdgcn_rcpf(den[0] + 1e-6f);
#pragma unroll
    for (int e = 0; e < 2; ++e) {
      v4u o;
#pragma unroll
      for (int j = 0; j < 4; ++j) o[j] = pk(num[8 * e + 2 * j] * ri, num[8 * e + 2 * j + 1] * ri);
      st128<AUX_SC1>(o, ws, (l31 >> 4) * 16384 + (2 * hh + e) * 256 + l15 * 16, WS_O + t32 * 32768 + mem * 1024);
    }
  }
  drain();
  set_flag(ws, WS_FO + t32 * 128 + mem * 8, lane, tag);
  STAMP(12);
  u32 v3[3], v3u[3];
#pragma unroll
  for (int i = 0; i < 3; ++i) { const int id = i * 64 + lane, r = id / 6, c = id - 6 * r; v3[i] = r * 2752 + c * 16; v3u[i] = (r ^ 4) * 2752 + c * 16; }
  if (has3) {
    for (int g = 2 + (w >> 1); g < 16; g += 2) {
#pragma unroll
      for (int i = 0; i < 3; ++i) {
        if (w & 1) dma16(ru, v3u[i], g * 32 * 2752 + u0 * 32, Lb + L_W3 + g * 6144 + 3072 + i * 1024);
        else dma16(rg, v3[i], g * 32 * 2752 + u0 * 32, Lb + L_W3 + g * 6144 + i * 1024);
      }
    }
  }
  v2u gam[4], cbm[4], gbm[4];
#pragma unroll
  for (int t = 0; t < 4; ++t) {
    gam[t] = wait_gran(ws, WS_MODG + (1024 + ct * 64 + t * 16 + qq * 4) * 4, tag.x);
    cbm[t] = wait_gran(ws, WS_MODG + (2048 + ct * 64 + t * 16 + qq * 4) * 4, tag.x);
    gbm[t] = wait_gran(ws, WS_MODG + (2560 + ct * 64 + t * 16 + qq * 4) * 4, tag.x);
  }
  {
    v2u g = wait_gran(ws, WS_MODG + (1536 + tid * 4) * 4, tag.x);
    if (w >= 2) g = v2u{pk(1.f + bflo(g.x), 1.f + bfhi(g.x)), pk(1.f + bflo(g.y), 1.f + bfhi(g.y))};
    const int c4 = tid & 127;
    *(LAS v2u*)(L + L_MODB + (w >= 2 ? 0 : 1024) + (c4 >> 3) * 64 + (c4 & 3) * 16 + ((c4 >> 2) & 1) * 8) = g;
  }
  v4u wf[64];
  {
    const int sw = (((ta >> 1) & 1) << 1) | ((qq & 1) << 2);
#pragma unroll
    for (int t = 0; t < 4; ++t) {
      LAS char* a = L + L_WO + (8 * qq + ta) * 128 + (((2 * t + (tp >> 1)) ^ sw) * 16) + (tp & 1) * 8;
#pragma unroll
      for (int s = 0; s < 16; ++s) wf[s * 4 + t] = trd2(a + s * 4096, a + s * 4096 + 512);
    }
  }
  bar();
  STAMP(13);
  wait_flags(ws, WS_FO + (rb * 2 + (w >> 1)) * 128, 16, lane, tag);
  if (b == 0 && w == 0 && lane == 0) __builtin_amdgcn_raw_buffer_store_b64(tag, ws, WS_EPOCH, 0, AUX_SC1);
  STAMP(14);
  f4 x1[4];
  {
    v4u of[16];
#pragma unroll
    for (int s = 0; s < 16; ++s) of[s] = __builtin_amdgcn_raw_buffer_load_b128(ws, lane * 16 + (s & 3) * 1024, WS_O + (rb * 4 + w) * 16384 + (s >> 2) * 4096, AUX_SC1);
    __builtin_amdgcn_sched_barrier(0);
    f4 acc[4] = {};
#pragma unroll
    for (int s = 0; s < 16; ++s)
#pragma unroll
      for (int t = 0; t < 4; ++t) acc[t] = mfma16(wf[s * 4 + t], of[s], acc[t]);
    float sm = 0.f;
#pragma unroll
    for (int t = 0; t < 4; ++t) {
      x1[t] = f4{bflo(xres[t].x) + bflo(gam[t].x) * acc[t].x, bfhi(xres[t].x) + bfhi(gam[t].x) * acc[t].y, bflo(xres[t].y) + bflo(gam[t].y) * acc[t].z, bfhi(xres[t].y) + bfhi(gam[t].y) * acc[t].w};
      sm += (x1[t].x + x1[t].y) + (x1[t].z + x1[t].w);
    }
#pragma unroll
    for (int T = 0; T < 2; ++T) {
      v4u xs;
#pragma unroll
      for (int e = 0; e < 2; ++e) {
        const int t = 2 * T + e;
        const u32 g0 = pk(1.f + bflo(cbm[t].x), 1.f + bfhi(cbm[t].x)), g1 = pk(1.f + bflo(cbm[t].y), 1.f + bfhi(cbm[t].y));
        xs[2 * e] = pk(x1[t].x * bflo(g0), x1[t].y * bfhi(g0));
        xs[2 * e + 1] = pk(x1[t].z * bflo(g1), x1[t].w * bfhi(g1));
      }
      st128<AUX_SC1>(xs, ws, qq * 512 + l15 * 16 + T * 2048, WS_XS + (rb * 2 + (w >> 1)) * 32768 + ct * 4096 + (w & 1) * 256);
    }
    const float m = sum32(sum16(sm)) * (1.f / 64.f);
    float q2 = 0.f;
#pragma unroll
    for (int t = 0; t < 4; ++t)
#pragma unroll
      for (int r = 0; r < 4; ++r) q2 += (x1[t][r] - m) * (x1[t][r] - m);
    q2 = sum32(sum16(q2));
    if (lane < 16) __builtin_amdgcn_raw_buffer_store_b64(v2u{__builtin_bit_cast(u32, m), __builtin_bit_cast(u32, q2)}, ws, lane * 8, WS_ST + (rb * 2 + (w >> 1)) * 2048 + ct * 256 + (w & 1) * 128, AUX_SC1);
  }
  drain();
  bar();
  if (w == 0) set_flag(ws, WS_FX + rb * 128 + ct * 8, lane, tag);
  STAMP(15);
  const u32 vo4 = vo128 + ((lane & 7) ^ (((lane >> 4) & 3) << 1)) * 16;
  if (has3) {
    const int g = w >> 1;
#pragma unroll
    for (int i = 0; i < 3; ++i) {
      if (w & 1) dma16(ru, v3u[i], g * 32 * 2752 + u0 * 32, Lb + L_W3 + g * 6144 + 3072 + i * 1024);
      else dma16(rg, v3[i], g * 32 * 2752 + u0 * 32, Lb + L_W3 + g * 6144 + i * 1024);
    }
    drain();
  }
  for (int i = 0; i < 14; ++i) { const int r8 = i * 4 + w; dma16(rd, vo4, r8 * 8192 + ct * 128, Lb + L_W4 + r8 * 1024); }
  bar();
  if (has3) {
    LAS char* w3 = L + L_W3 + (qq & 1) * 3072 + ((4 * hh + ta) ^ ((qq & 1) * 4)) * 96 + tp * 8;
    {
      f16v ut[3] = {};
      LAS char* mb = L + L_MODB + ((l31 >> 2) & 1) * 1024 + hh * 16 + w * 256;
      LAS char* wk = w3 + w * 4 * 6144;
#pragma unroll
      for (int s = 0; s < 8; ++s) {
        const v4u A = *(LAS v4u*)(mb + s * 32);
#pragma unroll
        for (int t = 0; t < 3; ++t) { LAS char* a = wk + (s >> 1) * 6144 + (s & 1) * 768 + t * 32; ut[t] = mfma32(A, trd2(a, a + 1536), ut[t]); }
      }
#pragma unroll
      for (int t = 0; t < 3; ++t) *(LAS float*)(L + L_UT - 2304 + t * 1024 + lane * 16 + w * 4) = ut[t][0];
    }
    bar();
    u32 utp[3];
#pragma unroll
    for (int t = 0; t < 3; ++t) {
      const f4 v = *(LAS f4*)(L + L_UT - 2304 + t * 1024 + lane * 16);
      const float s = (v.x + v.y) + (v.z + v.w), hi = rbf(s);
      utp[t] = pk(hi, s - hi);
    }
    STAMP(16);
    const int rb3 = sp3 * 4 + w;
    wait_flags(ws, WS_FX + rb3 * 128, 8, lane, tag);
    STAMP(17);
    constexpr int RING = 12;
    v4u xf[2][RING];
    const u32 xo = WS_XS + rb3 * 65536;
#pragma unroll
    for (int s = 0; s < RING; ++s)
#pragma unroll
      for (int mt = 0; mt < 2; ++mt) xf[mt][s] = __builtin_amdgcn_raw_buffer_load_b128(ws, lane * 16 + (s & 3) * 1024, xo + mt * 32768 + (s >> 2) * 4096, AUX_SC1);
    __builtin_amdgcn_sched_barrier(0);
    f16v acc[2][3] = {};
    v2u st[2][4];
#pragma unroll
    for (int s = 0; s < 32; ++s) {
      v4u A[3];
#pragma unroll
      for (int t = 0; t < 3; ++t) { LAS char* a = w3 + (s >> 1) * 6144 + (s & 1) * 768 + t * 32; A[t] = trd2(a, a + 1536); }
#pragma unroll
      for (int mt = 0; mt < 2; ++mt) {
        const v4u B = xf[mt][s % RING];
#pragma unroll
        for (int t = 0; t < 3; ++t) acc[mt][t] = mfma32(A[t], B, acc[mt][t]);
        if (s + RING < 32) xf[mt][s % RING] = __builtin_amdgcn_raw_buffer_load_b128(ws, lane * 16 + ((s + RING) & 3) * 1024, xo + mt * 32768 + ((s + RING) >> 2) * 4096, AUX_SC1);
        if (s + RING == 32) {
#pragma unroll
          for (int i = 0; i < 4; ++i) st[mt][i] = __builtin_amdgcn_raw_buffer_load_b64(ws, (hh * 4 + i) * 256 + l31 * 8, WS_ST + (rb3 * 2 + mt) * 2048, AUX_SC1);
        }
      }
    }
    STAMP(18);
#pragma unroll
    for (int mt = 0; mt < 2; ++mt) {
      float mi[4], ms = 0.f, M2 = 0.f;
#pragma unroll
      for (int i = 0; i < 4; ++i) { mi[i] = asf(st[mt][i].x); ms += mi[i]; M2 += asf(st[mt][i].y); }
      const float m = sum32(ms) * 0.125f;
#pragma unroll
      for (int i = 0; i < 4; ++i) M2 += 64.f * (mi[i] - m) * (mi[i] - m);
      const float var = sum32(M2) * (1.f / 512.f) + 1e-5f;
      const float c = hh ? __builtin_amdgcn_sqrtf(var) : -m, chi = rbf(c);
      const float r2 = __builtin_amdgcn_rcpf(var);
      const v4u B = {pk(chi, chi), pk(c - chi, c - chi), 0u, 0u};
#pragma unroll
      for (int t = 0; t < 3; ++t) {
        const f16v a = mfma32(v4u{utp[t], utp[t], 0u, 0u}, B, acc[mt][t]);
        v4u h2;
#pragma unroll
        for (int j = 0; j < 4; ++j) h2[j] = pk(__builtin_fmaxf(a[2 * j], 0.f) * (a[8 + 2 * j] * r2), __builtin_fmaxf(a[2 * j + 1], 0.f) * (a[9 + 2 * j] * r2));
        const int u = u0 + t;
        st128<AUX_SC1>(h2, ws, (l31 >> 4) * 44032 + hh * 256 + l15 * 16, WS_H2 + (rb3 * 4 + mt * 2) * 44032 + u * 512);
      }
    }
    drain();
    set_flag(ws, WS_FH2 + rb3 * 256 + m3 * 8, lane, tag);
  } else {
    bar();
    drain();
  }
  bar();
  STAMP(19);
  for (int i = 14; i < 38; ++i) { const int r8 = i * 4 + w; dma16(rd, vo4, r8 * 8192 + ct * 128, Lb + L_W4 + r8 * 1024); }
  {
    const int r0 = 16 * (qq >> 1) + 4 * (qq & 1) + ta;
    const int sw = ((r0 >> 1) & 3) << 1;
    LAS char* a4[4];
#pragma unroll
    for (int t = 0; t < 4; ++t) a4[t] = L + L_W4 + r0 * 128 + (((2 * t + (tp >> 1)) ^ sw) * 16) + (tp & 1) * 8;
    v4u wd[56];
#pragma unroll
    for (int s = 0; s < 14; ++s)
#pragma unroll
      for (int t = 0; t < 4; ++t) wd[s * 4 + t] = trd2(a4[t] + s * 4096, a4[t] + s * 4096 + 1024);
    bar();
    for (int i = 38; i < 43; ++i) { const int r8 = i * 4 + w; dma16(rd, vo4, r8 * 8192 + ct * 128, Lb + L_W4 + (r8 - 152) * 1024); }
    STAMP(20);
    wait_flags(ws, WS_FH2 + rb * 256, 29, lane, tag);
    bar();
    STAMP(21);
    v4u gf[43];
#pragma unroll
    for (int s = 0; s < 43; ++s) gf[s] = __builtin_amdgcn_raw_buffer_load_b128(ws, lane * 16 + (s & 3) * 1024, WS_H2 + (rb * 4 + w) * 44032 + (s >> 2) * 4096, AUX_SC1);
    __builtin_amdgcn_sched_barrier(0);
    f4 acc[4] = {};
#pragma unroll
    for (int s = 0; s < 43; ++s)
#pragma unroll
      for (int t = 0; t < 4; ++t) {
        const int so = (s < 38 ? s : s - 38) * 4096;
        acc[t] = mfma16(s < 14 ? wd[s * 4 + t] : trd2(a4[t] + so, a4[t] + so + 1024), gf[s], acc[t]);
      }
    STAMP(22);
    LAS char* ob = L + L_MISC + w * 2048;
#pragma unroll
    for (int t = 0; t < 4; ++t) {
      const v2u o = {pk(x1[t].x + bflo(gbm[t].x) * acc[t].x, x1[t].y + bfhi(gbm[t].x) * acc[t].y), pk(x1[t].z + bflo(gbm[t].y) * acc[t].z, x1[t].w + bfhi(gbm[t].y) * acc[t].w)};
      *(LAS v2u*)(ob + l15 * 128 + (((2 * t + (qq >> 1)) ^ (l15 & 7)) * 16) + (qq & 1) * 8) = o;
    }
    cfence();
#pragma unroll
    for (int i = 0; i < 2; ++i) {
      const v4u o = *(LAS v4u*)(ob + i * 1024 + (lane >> 3) * 128 + (((lane & 7) ^ (lane >> 3)) * 16));
      st128<0>(o, rout, (lane >> 3) * 1024 + (lane & 7) * 16, (rb * 64 + w * 16 + i * 8) * 1024 + ct * 128);
    }
  }
  STAMP(23);
}

extern "C" void kernel_launch(void* const* d_in, const int* in_sizes, int n_in, void* d_out, int out_size, void* d_ws, size_t ws_size, hipStream_t stream) {
  Params p;
  p.x = (const u16*)d_in[0]; p.wqkv = (const u16*)d_in[1]; p.wphi = (const u16*)d_in[2]; p.wo = (const u16*)d_in[3];
  p.wg = (const u16*)d_in[5]; p.wu = (const u16*)d_in[6]; p.wd = (const u16*)d_in[7]; p.c = (const u16*)d_in[8]; p.wmod = (const u16*)d_in[9];
  p.out = (u16*)d_out; p.ws = (char*)d_ws;
  (void)hipFuncSetAttribute((const void*)fused_block, hipFuncAttributeMaxDynamicSharedMemorySize, LDS_BYTES);
  fused_block<<<256, 256, LDS_BYTES, stream>>>(p);
}
```

```cpp
#include <hip/hip_runtime.h>
#define GAS __attribute__((address_space(1)))
#define LAS __attribute__((address_space(3)))
#define DI __device__ __forceinline__
typedef unsigned u32; typedef unsigned long long u64; typedef unsigned short u16;
typedef u32 v4u __attribute__((ext_vector_type(4)));
typedef u32 v2u __attribute__((ext_vector_type(2)));
typedef float f2 __attribute__((ext_vector_type(2)));
typedef float f4 __attribute__((ext_vector_type(4)));
typedef float f16v __attribute__((ext_vector_type(16)));
typedef short s4 __attribute__((ext_vector_type(4)));
typedef __bf16 b2 __attribute__((ext_vector_type(2)));
typedef __bf16 b8 __attribute__((ext_vector_type(8)));
typedef __amdgpu_buffer_rsrc_t rsrc_t;

#define STAMPS 0

constexpr int S_ = 2048, D_ = 512, F_ = 1376;
constexpr int WS_EPOCH = 0;
constexpr int WS_MODG = 4096;
constexpr int WS_FH = 20480;
constexpr int WS_FKV = WS_FH + 8192;
constexpr int WS_FO = WS_FKV + 2048;
constexpr int WS_FX = WS_FO + 8192;
constexpr int WS_FH2 = WS_FX + 4096;
constexpr int WS_ZP = 65536;
constexpr int WS_ST = 131072;
constexpr int WS_STAMP = 524288;
constexpr int WS_KVP = 1 << 20;
constexpr int WS_H = 2 << 20;
constexpr int WS_O = 4 << 20;
constexpr int WS_XS = 6 << 20;
constexpr int WS_H2 = 8 << 20;
constexpr int WS_END = WS_H2 + 128 * 43 * 1024;
constexpr int L_WK = 0, L_WV = 32768, L_WQ = 65536, L_PHI = 98304;
constexpr int L_WO = 0;
constexpr int L_W3 = 57344;
constexpr int L_W4 = 0;
constexpr int L_MISC = 155648;
constexpr int L_RED = L_MISC, L_MODB = L_MISC + 1024, L_KVL = L_MISC + 3072, L_ZL = L_KVL + 2048, L_ZERO = L_ZL + 128, L_UT = L_MISC + 5376;
constexpr int L_XRES = 126976;
constexpr int LDS_BYTES = 163840;
constexpr int AUX_SC1 = 16;
constexpr u32 SPIN_CAP = 1u << 18;

struct Params { const u16 *bx, *x, *wqkv, *wphi, *wo, *wg, *wu, *wd, *c, *wmod; u16* out; char* ws; };

DI float asf(u32 v) { return __builtin_bit_cast(float, v); }
DI u32 asu(float v) { return __builtin_bit_cast(u32, v); }
DI float bflo(u32 v) { return __builtin_bit_cast(float, v << 16); }
DI float bfhi(u32 v) { return __builtin_bit_cast(float, v & 0xffff0000u); }
DI u32 pk(float a, float b) { f2 t = {a, b}; return __builtin_bit_cast(u32, __builtin_convertvector(t, b2)); }
DI float rbf(float a) { return bflo(pk(a, a)); }
DI f16v mfma32(v4u a, v4u b, f16v c) { return __builtin_amdgcn_mfma_f32_32x32x16_bf16(__builtin_bit_cast(b8, a), __builtin_bit_cast(b8, b), c, 0, 0, 0); }
DI f4 mfma16(v4u a, v4u b, f4 c) { return __builtin_amdgcn_mfma_f32_16x16x32_bf16(__builtin_bit_cast(b8, a), __builtin_bit_cast(b8, b), c, 0, 0, 0); }
DI v2u trd(LAS char* p) { return __builtin_bit_cast(v2u, __builtin_amdgcn_ds_read_tr16_b64_v4i16((LAS s4*)p)); }
DI v4u trd2(LAS char* p0, LAS char* p1) { v2u a = trd(p0), b = trd(p1); return v4u{a.x, a.y, b.x, b.y}; }
DI rsrc_t mkrs(const void* p, int bytes) { return __builtin_amdgcn_make_buffer_rsrc((void*)p, 0, bytes, 0x00020000); }
template <int AUX> DI void st128(v4u v, rsrc_t rs, u32 voff, u32 soff) { __builtin_amdgcn_raw_buffer_store_b128(v, rs, voff + soff, 0, AUX); }
DI void dma16(rsrc_t rs, u32 voff, u32 soff, u32 lds) {
  asm volatile("s_mov_b32 m0, %0\n\ts_nop 0\n\tbuffer_load_dwordx4 %1, %2, %3 offen lds" ::"s"(lds), "v"(voff), "s"(rs), "s"(soff) : "memory");
}
DI void bar() { asm volatile("s_waitcnt lgkmcnt(0)\n\ts_barrier" ::: "memory"); }
DI void drain() { asm volatile("s_waitcnt vmcnt(0)" ::: "memory"); }
DI void cfence() { asm volatile("" ::: "memory"); }
template <int CTRL> DI float dppf(float v) { return __builtin_bit_cast(float, __builtin_amdgcn_update_dpp(0, __builtin_bit_cast(int, v), CTRL, 0xf, 0xf, true)); }
DI float sum16(float v) { auto r = __builtin_amdgcn_permlane16_swap(__builtin_bit_cast(u32, v), __builtin_bit_cast(u32, v), false, false); return asf(r[0]) + asf(r[1]); }
DI float sum32(float v) { auto r = __builtin_amdgcn_permlane32_swap(__builtin_bit_cast(u32, v), __builtin_bit_cast(u32, v), false, false); return asf(r[0]) + asf(r[1]); }
constexpr int ROR8 = 0x128, ROR4 = 0x124, ROR2 = 0x122, ROR1 = 0x121;
DI void wait_flags(rsrc_t ws, u32 off, int n, int lane, v2u tag) {
  const u32 vo = off + (lane < n ? lane : 0) * 8;
  for (u32 sp = 0; sp < SPIN_CAP; ++sp) {
    v2u f = __builtin_amdgcn_raw_buffer_load_b64(ws, vo, 0, AUX_SC1);
    cfence();
    if (__all(f.x == tag.x && f.y == tag.y)) break;
  }
  cfence();
}
DI void set_flag(rsrc_t ws, u32 off, int lane, v2u tag) { if (lane == 0) __builtin_amdgcn_raw_buffer_store_b64(tag, ws, off, 0, AUX_SC1); }
DI v2u wait_gran(rsrc_t ws, u32 voff, u32 tag) {
  v4u g;
  for (u32 sp = 0; sp < SPIN_CAP; ++sp) {
    g = __builtin_amdgcn_raw_buffer_load_b128(ws, voff, 0, AUX_SC1);
    cfence();
    if (__all(g.y == tag && g.w == tag)) break;
  }
  return v2u{g.x, g.z};
}
#define STAMP(i) do {} while (0)

DI void feature_map(const f16v& acc, v4u wp0, v4u wp1, u32 (&ph)[16]) {
  v4u b0 = {pk(acc[0], acc[1]), pk(acc[2], acc[3]), pk(acc[4], acc[5]), pk(acc[6], acc[7])};
  v4u b1 = {pk(acc[8], acc[9]), pk(acc[10], acc[11]), pk(acc[12], acc[13]), pk(acc[14], acc[15])};
  f16v z = {};
  f16v lg = mfma32(wp0, b0, z);
  lg = mfma32(wp1, b1, lg);
  float mx = lg[0], mn = lg[0];
#pragma unroll
  for (int i = 1; i < 16; ++i) { mx = __builtin_fmaxf(mx, lg[i]); mn = __builtin_fminf(mn, lg[i]); }
  const float L2E = 1.44269504088896f;
  const float cp = -mx * L2E, cn = mn * L2E;
  float ep[16], en[16];
#pragma unroll
  for (int i = 0; i < 16; ++i) { ep[i] = __builtin_amdgcn_exp2f(__builtin_fmaf(lg[i], L2E, cp)); en[i] = __builtin_amdgcn_exp2f(__builtin_fmaf(lg[i], -L2E, cn)); }
  f2 sp = {ep[0], ep[1]}, sn = {en[0], en[1]};
#pragma unroll
  for (int i = 1; i < 8; ++i) { sp += f2{ep[2 * i], ep[2 * i + 1]}; sn += f2{en[2 * i], en[2 * i + 1]}; }
  const float rp = __builtin_amdgcn_rcpf(sp.x + sp.y), rn = __builtin_amdgcn_rcpf(sn.x + sn.y);
#pragma unroll
  for (int i = 0; i < 8; ++i) { ph[i] = pk(ep[2 * i] * rp, ep[2 * i + 1] * rp); ph[8 + i] = pk(en[2 * i] * rn, en[2 * i + 1] * rn); }
}

__global__ void __launch_bounds__(256, 1) fused_block(Params p) {
  extern __shared__ __attribute__((aligned(16))) char smem_[];
  LAS char* L = (LAS char*)smem_;
  const u32 Lb = (u32)(size_t)L;
  const int tid = threadIdx.x, lane = tid & 63, b = blockIdx.x;
  const int w = __builtin_amdgcn_readfirstlane(tid >> 6);
  const int xg = b & 7, jj = b >> 3;
  const int pan = 4 * (xg >> 1) + (jj >> 3), mem = 8 * (xg & 1) + (jj & 7);
  const int rb = 4 * xg + (jj >> 3), ct = jj & 7;
  const int sp3 = 4 * (xg >> 2) + (jj >> 3), m3 = 8 * (xg & 3) + (jj & 7);
  const int u0 = m3 * 3 < 83 ? m3 * 3 : 83;
  const bool has3 = m3 < 29;
  STAMP(0);
  const rsrc_t ws = mkrs(p.ws, WS_END);
  const rsrc_t rx = mkrs(p.x, S_ * D_ * 2), rqkv = mkrs(p.wqkv, D_ * 3 * D_ * 2), rwo = mkrs(p.wo, D_ * D_ * 2);
  const rsrc_t rg = mkrs(p.wg, D_ * F_ * 2), ru = mkrs(p.wu, D_ * F_ * 2), rd = mkrs(p.wd, F_ * D_ * 2);
  const rsrc_t rmod = mkrs(p.wmod, D_ * 6 * D_ * 2), rc = mkrs(p.c, D_ * 2), rphi = mkrs(p.wphi, 32 * 256 * 2), rout = mkrs(p.out, S_ * D_ * 2);
  const int l31 = lane & 31, hh = lane >> 5, l15 = lane & 15, qq = lane >> 4;
  const int ta = (lane >> 2) & 3, tp = lane & 3;

  u32 cw_;
  { u64 pc; asm volatile("s_getpc_b64 %0" : "=s"(pc)); const GAS char* cp = (const GAS char*)(pc & ~127ull);
    int li = jj * 10 + (lane < 20 ? lane : 0); li = li < 304 ? li : 304;
    asm volatile("global_load_dword %0, %1, off" : "=v"(cw_) : "v"(cp + li * 128) : "memory"); }
  const v2u ep = __builtin_amdgcn_raw_buffer_load_b64(ws, WS_EPOCH, 0, AUX_SC1);
  const bool hasmod = jj < 24;
  const int mcol = jj < 8 ? (2 * xg + (jj >> 2)) * 64 + (jj & 3) * 16 : 1024 + (4 * xg + ((jj - 8) >> 2)) * 64 + (jj & 3) * 16;
  v4u wm[4]; u16 cc[4];
  if (hasmod) {
#pragma unroll
    for (int i = 0; i < 4; ++i) cc[i] = __builtin_amdgcn_raw_buffer_load_b16(rc, ((tid >> 1) + 128 * i) * 2, 0, 0);
#pragma unroll
    for (int i = 0; i < 4; ++i) wm[i] = __builtin_amdgcn_raw_buffer_load_b128(rmod, ((tid >> 1) + 128 * i) * 6144 + (tid & 1) * 16, mcol * 2, 0);
  }
  v4u xr[2];
#pragma unroll
  for (int i = 0; i < 2; ++i) xr[i] = __builtin_amdgcn_raw_buffer_load_b128(rx, (lane & 7) * 1024 + ((lane >> 3) + 8 * i) * 16, b * 8192 + w * 256, 0);
  u16 wp[8];
  {
    const int hd = (l31 >> 2) & 1, f = 4 * (l31 >> 3) + (l31 & 3);
#pragma unroll
    for (int j = 0; j < 8; ++j) wp[j] = __builtin_amdgcn_raw_buffer_load_b16(rphi, (hd * 256 + (8 * (j >> 2) + 4 * hh + (j & 3)) * 16 + f) * 2, mem * 1024, 0);
  }
#pragma unroll
  for (int i = 0; i < 2; ++i) dma16(rx, (lane >> 3) * 1024 + (lane & 7) * 16, (rb * 64 + w * 16 + i * 8) * 1024 + ct * 128, Lb + L_XRES + w * 2048 + i * 1024);
  {
    const u32 vo = (lane >> 2) * 3072 + (lane & 3) * 16;
    for (int i = 0; i < 8; ++i) {
      const int r16 = i * 4 + w;
      dma16(rqkv, vo, r16 * 16 * 3072 + 1024 + mem * 64, Lb + L_WK + r16 * 1024);
      dma16(rqkv, vo, r16 * 16 * 3072 + 2048 + mem * 64, Lb + L_WV + r16 * 1024);
    }
    for (int i = 0; i < 8; ++i) { const int r16 = i * 4 + w; dma16(rqkv, vo, r16 * 16 * 3072 + mem * 64, Lb + L_WQ + r16 * 1024); }
  }
  const u64 tag64 = (((u64)ep.y << 32) | ep.x) + 0x9E3779B97F4A7C15ull;
  const v2u tag = {(u32)__builtin_amdgcn_readfirstlane((u32)tag64), (u32)__builtin_amdgcn_readfirstlane((u32)(tag64 >> 32))};
  if (tid < 16) *(LAS u32*)(L + L_ZERO + tid * 4) = 0u;
  asm volatile("s_waitcnt vmcnt(40)" :: "v"(cw_) : "memory");
  STAMP(1);
  if (hasmod) {
    float a[8] = {};
#pragma unroll
    for (int i = 0; i < 4; ++i) {
      const float cv = bflo(cc[i]);
      const float s = rbf(cv * __builtin_amdgcn_rcpf(1.f + __builtin_amdgcn_exp2f(-1.44269504088896f * cv)));
#pragma unroll
      for (int j = 0; j < 4; ++j) { a[2 * j] += s * bflo(wm[i][j]); a[2 * j + 1] += s * bfhi(wm[i][j]); }
    }
#pragma unroll
    for (int j = 0; j < 8; ++j) { float v = a[j]; v += dppf<ROR2>(v); v += dppf<ROR4>(v); v += dppf<ROR8>(v); a[j] = v; }
    if (l15 < 2) { *(LAS f4*)(L + L_RED + (w * 4 + qq) * 64 + l15 * 32) = f4{a[0], a[1], a[2], a[3]}; *(LAS f4*)(L + L_RED + (w * 4 + qq) * 64 + l15 * 32 + 16) = f4{a[4], a[5], a[6], a[7]}; }
  }
  float xv[16];
#pragma unroll
  for (int i = 0; i < 2; ++i)
#pragma unroll
    for (int j = 0; j < 4; ++j) { xv[8 * i + 2 * j] = bflo(xr[i][j]); xv[8 * i + 2 * j + 1] = bfhi(xr[i][j]); }
  float s1 = 0.f;
#pragma unroll
  for (int i = 0; i < 16; ++i) s1 += xv[i];
  s1 += dppf<ROR8>(s1); s1 = sum16(s1); s1 = sum32(s1);
  const float ml = s1 * (1.f / 128.f);
  float s2 = 0.f;
#pragma unroll
  for (int i = 0; i < 16; ++i) s2 += (xv[i] - ml) * (xv[i] - ml);
  s2 += dppf<ROR8>(s2); s2 = sum16(s2); s2 = sum32(s2);
  if (lane < 8) *(LAS f2*)(L + L_UT + lane * 32 + w * 8) = f2{ml, s2};
  bar();
  if (hasmod && w == 0) {
    f2 v = *(LAS f2*)(L + L_RED + (lane >> 3) * 128 + (lane & 7) * 8);
    v += *(LAS f2*)(L + L_RED + (lane >> 3) * 128 + 64 + (lane & 7) * 8);
    v.x += dppf<ROR8>(v.x); v.y += dppf<ROR8>(v.y);
    v.x = sum32(sum16(v.x)); v.y = sum32(sum16(v.y));
    if (lane < 8) __builtin_amdgcn_raw_buffer_store_b64(v2u{pk(v.x, v.y), tag.x}, ws, WS_MODG + lane * 8, mcol * 4, AUX_SC1);
  }
  STAMP(2);
  {
    const f4 t0 = *(LAS f4*)(L + L_UT + (lane & 7) * 32), t1 = *(LAS f4*)(L + L_UT + (lane & 7) * 32 + 16);
    const float m = (t0.x + t0.z + t1.x + t1.z) * 0.25f;
    const float M2 = t0.y + t0.w + t1.y + t1.w + 128.f * ((t0.x - m) * (t0.x - m) + (t0.z - m) * (t0.z - m) + (t1.x - m) * (t1.x - m) + (t1.z - m) * (t1.z - m));
    const float r = __builtin_amdgcn_rsqf(M2 * (1.f / 512.f) + 1e-5f);
#pragma unroll
    for (int i = 0; i < 16; ++i) xv[i] = (xv[i] - m) * r;
  }
  {
    const v2u g = wait_gran(ws, WS_MODG + hh * 2048 + w * 512 + l31 * 16, tag.x);
    *(LAS v2u*)(L + L_MODB + w * 512 + lane * 8) = g;
    cfence();
    STAMP(3);
#pragma unroll
    for (int i = 0; i < 2; ++i) {
      const v4u sa = *(LAS v4u*)(L + L_MODB + w * 512 + ((lane >> 3) + 8 * i) * 16);
      const v4u ca = *(LAS v4u*)(L + L_MODB + w * 512 + 256 + ((lane >> 3) + 8 * i) * 16);
      v4u h;
#pragma unroll
      for (int j = 0; j < 4; ++j) {
        const u32 g1 = pk(1.f + bflo(ca[j]), 1.f + bfhi(ca[j]));
        h[j] = pk(xv[8 * i + 2 * j] * bflo(g1) + bflo(sa[j]), xv[8 * i + 2 * j + 1] * bfhi(g1) + bfhi(sa[j]));
      }
      st128<AUX_SC1>(h, ws, ((lane >> 3) + 8 * i) * 512 + (lane & 7) * 16, WS_H + (b >> 2) * 32768 + w * 8192 + (b & 3) * 128);
    }
  }
  drain();
  bar();
  if (w == 0) set_flag(ws, WS_FH + (b >> 2) * 128 + (b & 3) * 8, lane, tag);
  STAMP(4);
  v2u xres[4];
#pragma unroll
  for (int t = 0; t < 4; ++t) xres[t] = *(LAS v2u*)(L + L_XRES + w * 2048 + l15 * 128 + t * 32 + qq * 8);
  v4u wp0, wp1;
  {
    const v4u v = {wp[0] | ((u32)wp[1] << 16), wp[2] | ((u32)wp[3] << 16), wp[4] | ((u32)wp[5] << 16), wp[6] | ((u32)wp[7] << 16)};
    const u32 m1 = ((l31 >> 2) & 1) ? 0xffffffffu : 0u;
    wp1 = v & m1; wp0 = v & ~m1;
  }

  const int t32 = pan * 4 + w;
  wait_flags(ws, WS_FH + t32 * 128, 4, lane, tag);
  STAMP(5);
  v4u hf[32];
#pragma unroll
  for (int s = 0; s < 32; ++s) hf[s] = __builtin_amdgcn_raw_buffer_load_b128(ws, lane * 16 + (s & 3) * 1024, WS_H + t32 * 32768 + (s >> 2) * 4096, AUX_SC1);
  __builtin_amdgcn_sched_barrier(0);
  LAS char* wa = L + (8 * hh + ta) * 64 + (qq & 1) * 32 + tp * 8;
  f16v ak = {}, av = {};
#pragma unroll
  for (int s = 0; s < 32; ++s) {
    ak = mfma32(trd2(wa + L_WK + s * 1024, wa + L_WK + s * 1024 + 256), hf[s], ak);
    av = mfma32(trd2(wa + L_WV + s * 1024, wa + L_WV + s * 1024 + 256), hf[s], av);
  }
  STAMP(6);
  {
    u32 ph[16];
    feature_map(ak, wp0, wp1, ph);
    LAS char* row = L + L_PHI + (w * 32 + l31) * 224;
#pragma unroll
    for (int i = 0; i < 4; ++i) *(LAS v4u*)(row + hh * 64 + i * 16) = v4u{ph[4 * i], ph[4 * i + 1], ph[4 * i + 2], ph[4 * i + 3]};
#pragma unroll
    for (int a = 0; a < 4; ++a) *(LAS v2u*)(row + 128 + a * 16 + hh * 8) = v2u{pk(av[4 * a], av[4 * a + 1]), pk(av[4 * a + 2], av[4 * a + 3])};
  }
  bar();
  STAMP(7);
  {
    LAS char* ra = L + L_PHI + (4 * qq + ta) * 224 + tp * 8;
    f4 kv = {}, zz = {};
    const v4u ones = {0x3f803f80u, 0x3f803f80u, 0x3f803f80u, 0x3f803f80u};
#pragma unroll
    for (int s = 0; s < 4; ++s) {
      const v4u A = trd2(ra + w * 32 + s * 32 * 224, ra + w * 32 + (s * 32 + 16) * 224);
      const v4u B = trd2(ra + 128 + (w >> 1) * 32 + s * 32 * 224, ra + 128 + (w >> 1) * 32 + (s * 32 + 16) * 224);
      kv = mfma16(A, B, kv); zz = mfma16(A, ones, zz);
    }
    st128<AUX_SC1>(__builtin_bit_cast(v4u, kv), ws, lane * 16, WS_KVP + (mem * 16 + pan) * 4096 + w * 1024);
    if (l15 == 0) st128<AUX_SC1>(__builtin_bit_cast(v4u, zz), ws, qq * 16, WS_ZP + (mem * 16 + pan) * 256 + w * 64);
  }
  drain();
  bar();
  if (w == 0) set_flag(ws, WS_FKV + mem * 128 + pan * 8, lane, tag);
  STAMP(8);
  const u32 vo128 = (lane >> 3) * 1024;
  {
    for (int i = 0; i < 16; ++i) {
      const int r8 = i * 4 + w;
      const u32 sw = (((lane >> 4) & 1) << 1) | ((w & 1) << 2);
      dma16(rwo, vo128 + ((lane & 7) ^ sw) * 16, r8 * 8192 + ct * 128, Lb + L_WO + r8 * 1024);
    }
  }
  u32 pq[16];
  {
    f16v aq = {};
#pragma unroll
    for (int s = 0; s < 32; ++s) aq = mfma32(trd2(wa + L_WQ + s * 1024, wa + L_WQ + s * 1024 + 256), hf[s], aq);
    feature_map(aq, wp0, wp1, pq);
  }
  STAMP(9);
  wait_flags(ws, WS_FKV + mem * 128, 16, lane, tag);
  STAMP(10);
  {
    f4 zq = __builtin_bit_cast(f4, __builtin_amdgcn_raw_buffer_load_b128(ws, (lane >> 2) * 256 + (lane & 3) * 16, WS_ZP + mem * 4096 + w * 64, AUX_SC1));
    v4u kp[16];
#pragma unroll
    for (int i = 0; i < 16; ++i) kp[i] = __builtin_amdgcn_raw_buffer_load_b128(ws, lane * 16, WS_KVP + (mem * 16 + i) * 4096 + w * 1024, AUX_SC1);
    __builtin_amdgcn_sched_barrier(0);
#pragma unroll
    for (int j = 0; j < 4; ++j) { float v = zq[j]; v += dppf<ROR4>(v); v += dppf<ROR8>(v); zq[j] = sum32(sum16(v)); }
    if (lane < 4) *(LAS v2u*)(L + L_ZL + w * 32 + lane * 8) = v2u{pk(zq.x, zq.y), pk(zq.z, zq.w)};
    f4 s = __builtin_bit_cast(f4, kp[0]);
#pragma unroll
    for (int i = 1; i < 16; ++i) s += __builtin_bit_cast(f4, kp[i]);
    *(LAS v2u*)(L + L_KVL + ((w >> 1) * 16 + l15) * 64 + (w & 1) * 32 + qq * 8) = v2u{pk(s.x, s.y), pk(s.z, s.w)};
  }
  bar();
  STAMP(11);
  {
    const bool mine = ((l31 >> 2) & 1) == hh;
    LAS char* pa = L + (mine ? L_KVL + (hh * 16 + 4 * (l31 >> 3) + (l31 & 3)) * 64 : L_ZERO);
    LAS char* pz = L + (mine ? L_ZL + hh * 64 : L_ZERO);
    f16v num = {}, den = {};
#pragma unroll
    for (int u = 0; u < 4; ++u) {
      const v4u B = {pq[4 * u], pq[4 * u + 1], pq[4 * u + 2], pq[4 * u + 3]};
      den = mfma32(*(LAS v4u*)(pz + u * 16), B, den);
      num = mfma32(*(LAS v4u*)(pa + u * 16), B, num);
    }
    const float ri = __builtin_amdgcn_rcpf(den[0] + 1e-6f);
#pragma unroll
    for (int e = 0; e < 2; ++e) {
      v4u o;
#pragma unroll
      for (int j = 0; j < 4; ++j) o[j] = pk(num[8 * e + 2 * j] * ri, num[8 * e + 2 * j + 1] * ri);
      st128<AUX_SC1>(o, ws, (l31 >> 4) * 16384 + (2 * hh + e) * 256 + l15 * 16, WS_O + t32 * 32768 + mem * 1024);
    }
  }
  drain();
  set_flag(ws, WS_FO + t32 * 128 + mem * 8, lane, tag);
  STAMP(12);
  u32 v3[3], v3u[3];
#pragma unroll
  for (int i = 0; i < 3; ++i) { const int id = i * 64 + lane, r = id / 6, c = id - 6 * r; v3[i] = r * 2752 + c * 16; v3u[i] = (r ^ 4) * 2752 + c * 16; }
  if (has3) {
    for (int g = 2 + (w >> 1); g < 16; g += 2) {
#pragma unroll
      for (int i = 0; i < 3; ++i) {
        if (w & 1) dma16(ru, v3u[i], g * 32 * 2752 + u0 * 32, Lb + L_W3 + g * 6144 + 3072 + i * 1024);
        else dma16(rg, v3[i], g * 32 * 2752 + u0 * 32, Lb + L_W3 + g * 6144 + i * 1024);
      }
    }
  }
  v2u gam[4], cbm[4], gbm[4];
#pragma unroll
  for (int t = 0; t < 4; ++t) {
    gam[t] = wait_gran(ws, WS_MODG + (1024 + ct * 64 + t * 16 + qq * 4) * 4, tag.x);
    cbm[t] = wait_gran(ws, WS_MODG + (2048 + ct * 64 + t * 16 + qq * 4) * 4, tag.x);
    gbm[t] = wait_gran(ws, WS_MODG + (2560 + ct * 64 + t * 16 + qq * 4) * 4, tag.x);
  }
  {
    v2u g = wait_gran(ws, WS_MODG + (1536 + tid * 4) * 4, tag.x);
    if (w >= 2) g = v2u{pk(1.f + bflo(g.x), 1.f + bfhi(g.x)), pk(1.f + bflo(g.y), 1.f + bfhi(g.y))};
    const int c4 = tid & 127;
    *(LAS v2u*)(L + L_MODB + (w >= 2 ? 0 : 1024) + (c4 >> 3) * 64 + (c4 & 3) * 16 + ((c4 >> 2) & 1) * 8) = g;
  }
  v4u wf[64];
  {
    const int sw = (((ta >> 1) & 1) << 1) | ((qq & 1) << 2);
#pragma unroll
    for (int t = 0; t < 4; ++t) {
      LAS char* a = L + L_WO + (8 * qq + ta) * 128 + (((2 * t + (tp >> 1)) ^ sw) * 16) + (tp & 1) * 8;
#pragma unroll
      for (int s = 0; s < 16; ++s) wf[s * 4 + t] = trd2(a + s * 4096, a + s * 4096 + 512);
    }
  }
  bar();
  STAMP(13);
  wait_flags(ws, WS_FO + (rb * 2 + (w >> 1)) * 128, 16, lane, tag);
  if (b == 0 && w == 0 && lane == 0) __builtin_amdgcn_raw_buffer_store_b64(tag, ws, WS_EPOCH, 0, AUX_SC1);
  STAMP(14);
  f4 x1[4];
  {
    v4u of[16];
#pragma unroll
    for (int s = 0; s < 16; ++s) of[s] = __builtin_amdgcn_raw_buffer_load_b128(ws, lane * 16 + (s & 3) * 1024, WS_O + (rb * 4 + w) * 16384 + (s >> 2) * 4096, AUX_SC1);
    __builtin_amdgcn_sched_barrier(0);
    f4 acc[4] = {};
#pragma unroll
    for (int s = 0; s < 16; ++s)
#pragma unroll
      for (int t = 0; t < 4; ++t) acc[t] = mfma16(wf[s * 4 + t], of[s], acc[t]);
    float sm = 0.f;
#pragma unroll
    for (int t = 0; t < 4; ++t) {
      x1[t] = f4{bflo(xres[t].x) + bflo(gam[t].x) * acc[t].x, bfhi(xres[t].x) + bfhi(gam[t].x) * acc[t].y, bflo(xres[t].y) + bflo(gam[t].y) * acc[t].z, bfhi(xres[t].y) + bfhi(gam[t].y) * acc[t].w};
      sm += (x1[t].x + x1[t].y) + (x1[t].z + x1[t].w);
    }
#pragma unroll
    for (int T = 0; T < 2; ++T) {
      v4u xs;
#pragma unroll
      for (int e = 0; e < 2; ++e) {
        const int t = 2 * T + e;
        const u32 g0 = pk(1.f + bflo(cbm[t].x), 1.f + bfhi(cbm[t].x)), g1 = pk(1.f + bflo(cbm[t].y), 1.f + bfhi(cbm[t].y));
        xs[2 * e] = pk(x1[t].x * bflo(g0), x1[t].y * bfhi(g0));
        xs[2 * e + 1] = pk(x1[t].z * bflo(g1), x1[t].w * bfhi(g1));
      }
      st128<AUX_SC1>(xs, ws, qq * 512 + l15 * 16 + T * 2048, WS_XS + (rb * 2 + (w >> 1)) * 32768 + ct * 4096 + (w & 1) * 256);
    }
    const float m = sum32(sum16(sm)) * (1.f / 64.f);
    float q2 = 0.f;
#pragma unroll
    for (int t = 0; t < 4; ++t)
#pragma unroll
      for (int r = 0; r < 4; ++r) q2 += (x1[t][r] - m) * (x1[t][r] - m);
    q2 = sum32(sum16(q2));
    if (lane < 16) __builtin_amdgcn_raw_buffer_store_b64(v2u{__builtin_bit_cast(u32, m), __builtin_bit_cast(u32, q2)}, ws, lane * 8, WS_ST + (rb * 2 + (w >> 1)) * 2048 + ct * 256 + (w & 1) * 128, AUX_SC1);
  }
  drain();
  bar();
  if (w == 0) set_flag(ws, WS_FX + rb * 128 + ct * 8, lane, tag);
  STAMP(15);
  const u32 vo4 = vo128 + ((lane & 7) ^ (((lane >> 4) & 3) << 1)) * 16;
  if (has3) {
    const int g = w >> 1;
#pragma unroll
    for (int i = 0; i < 3; ++i) {
      if (w & 1) dma16(ru, v3u[i], g * 32 * 2752 + u0 * 32, Lb + L_W3 + g * 6144 + 3072 + i * 1024);
      else dma16(rg, v3[i], g * 32 * 2752 + u0 * 32, Lb + L_W3 + g * 6144 + i * 1024);
    }
    drain();
  }
  for (int i = 0; i < 14; ++i) { const int r8 = i * 4 + w; dma16(rd, vo4, r8 * 8192 + ct * 128, Lb + L_W4 + r8 * 1024); }
  bar();
  if (has3) {
    LAS char* w3 = L + L_W3 + (qq & 1) * 3072 + ((4 * hh + ta) ^ ((qq & 1) * 4)) * 96 + tp * 8;
    {
      f16v ut[3] = {};
      LAS char* mb = L + L_MODB + ((l31 >> 2) & 1) * 1024 + hh * 16 + w * 256;
      LAS char* wk = w3 + w * 4 * 6144;
#pragma unroll
      for (int s = 0; s < 8; ++s) {
        const v4u A = *(LAS v4u*)(mb + s * 32);
#pragma unroll
        for (int t = 0; t < 3; ++t) { LAS char* a = wk + (s >> 1) * 6144 + (s & 1) * 768 + t * 32; ut[t] = mfma32(A, trd2(a, a + 1536), ut[t]); }
      }
#pragma unroll
      for (int t = 0; t < 3; ++t) *(LAS float*)(L + L_UT - 2304 + t * 1024 + lane * 16 + w * 4) = ut[t][0];
    }
    bar();
    u32 utp[3];
#pragma unroll
    for (int t = 0; t < 3; ++t) {
      const f4 v = *(LAS f4*)(L + L_UT - 2304 + t * 1024 + lane * 16);
      const float s = (v.x + v.y) + (v.z + v.w), hi = rbf(s);
      utp[t] = pk(hi, s - hi);
    }
    STAMP(16);
    const int rb3 = sp3 * 4 + w;
    wait_flags(ws, WS_FX + rb3 * 128, 8, lane, tag);
    STAMP(17);
    constexpr int RING = 12;
    v4u xf[2][RING];
    const u32 xo = WS_XS + rb3 * 65536;
#pragma unroll
    for (int s = 0; s < RING; ++s)
#pragma unroll
      for (int mt = 0; mt < 2; ++mt) xf[mt][s] = __builtin_amdgcn_raw_buffer_load_b128(ws, lane * 16 + (s & 3) * 1024, xo + mt * 32768 + (s >> 2) * 4096, AUX_SC1);
    __builtin_amdgcn_sched_barrier(0);
    f16v acc[2][3] = {};
    v2u st[2][4];
#pragma unroll
    for (int s = 0; s < 32; ++s) {
      v4u A[3];
#pragma unroll
      for (int t = 0; t < 3; ++t) { LAS char* a = w3 + (s >> 1) * 6144 + (s & 1) * 768 + t * 32; A[t] = trd2(a, a + 1536); }
#pragma unroll
      for (int mt = 0; mt < 2; ++mt) {
        const v4u B = xf[mt][s % RING];
#pragma unroll
        for (int t = 0; t < 3; ++t) acc[mt][t] = mfma32(A[t], B, acc[mt][t]);
        if (s + RING < 32) xf[mt][s % RING] = __builtin_amdgcn_raw_buffer_load_b128(ws, lane * 16 + ((s + RING) & 3) * 1024, xo + mt * 32768 + ((s + RING) >> 2) * 4096, AUX_SC1);
        if (s + RING == 32) {
#pragma unroll
          for (int i = 0; i < 4; ++i) st[mt][i] = __builtin_amdgcn_raw_buffer_load_b64(ws, (hh * 4 + i) * 256 + l31 * 8, WS_ST + (rb3 * 2 + mt) * 2048, AUX_SC1);
        }
      }
    }
    STAMP(18);
#pragma unroll
    for (int mt = 0; mt < 2; ++mt) {
      float mi[4], ms = 0.f, M2 = 0.f;
#pragma unroll
      for (int i = 0; i < 4; ++i) { mi[i] = asf(st[mt][i].x); ms += mi[i]; M2 += asf(st[mt][i].y); }
      const float m = sum32(ms) * 0.125f;
#pragma unroll
      for (int i = 0; i < 4; ++i) M2 += 64.f * (mi[i] - m) * (mi[i] - m);
      const float var = sum32(M2) * (1.f / 512.f) + 1e-5f;
      const float c = hh ? __builtin_amdgcn_sqrtf(var) : -m, chi = rbf(c);
      const float r2 = __builtin_amdgcn_rcpf(var);
      const v4u B = {pk(chi, chi), pk(c - chi, c - chi), 0u, 0u};
#pragma unroll
      for (int t = 0; t < 3; ++t) {
        const f16v a = mfma32(v4u{utp[t], utp[t], 0u, 0u}, B, acc[mt][t]);
        v4u h2;
#pragma unroll
        for (int j = 0; j < 4; ++j) h2[j] = pk(__builtin_fmaxf(a[2 * j], 0.f) * (a[8 + 2 * j] * r2), __builtin_fmaxf(a[2 * j + 1], 0.f) * (a[9 + 2 * j] * r2));
        const int u = u0 + t;
        st128<AUX_SC1>(h2, ws, (l31 >> 4) * 44032 + hh * 256 + l15 * 16, WS_H2 + (rb3 * 4 + mt * 2) * 44032 + u * 512);
      }
    }
    drain();
    set_flag(ws, WS_FH2 + rb3 * 256 + m3 * 8, lane, tag);
  } else {
    bar();
    drain();
  }
  bar();
  STAMP(19);
  for (int i = 14; i < 38; ++i) { const int r8 = i * 4 + w; dma16(rd, vo4, r8 * 8192 + ct * 128, Lb + L_W4 + r8 * 1024); }
  {
    const int r0 = 16 * (qq >> 1) + 4 * (qq & 1) + ta;
    const int sw = ((r0 >> 1) & 3) << 1;
    LAS char* a4[4];
#pragma unroll
    for (int t = 0; t < 4; ++t) a4[t] = L + L_W4 + r0 * 128 + (((2 * t + (tp >> 1)) ^ sw) * 16) + (tp & 1) * 8;
    v4u wd[56];
#pragma unroll
    for (int s = 0; s < 14; ++s)
#pragma unroll
      for (int t = 0; t < 4; ++t) wd[s * 4 + t] = trd2(a4[t] + s * 4096, a4[t] + s * 4096 + 1024);
    bar();
    for (int i = 38; i < 43; ++i) { const int r8 = i * 4 + w; dma16(rd, vo4, r8 * 8192 + ct * 128, Lb + L_W4 + (r8 - 152) * 1024); }
    STAMP(20);
    wait_flags(ws, WS_FH2 + rb * 256, 29, lane, tag);
    bar();
    STAMP(21);
    v4u gf[43];
#pragma unroll
    for (int s = 0; s < 43; ++s) gf[s] = __builtin_amdgcn_raw_buffer_load_b128(ws, lane * 16 + (s & 3) * 1024, WS_H2 + (rb * 4 + w) * 44032 + (s >> 2) * 4096, AUX_SC1);
    __builtin_amdgcn_sched_barrier(0);
    f4 acc[4] = {};
#pragma unroll
    for (int s = 0; s < 43; ++s)
#pragma unroll
      for (int t = 0; t < 4; ++t) {
        const int so = (s < 38 ? s : s - 38) * 4096;
        acc[t] = mfma16(s < 14 ? wd[s * 4 + t] : trd2(a4[t] + so, a4[t] + so + 1024), gf[s], acc[t]);
      }
    STAMP(22);
    LAS char* ob = L + L_MISC + w * 2048;
#pragma unroll
    for (int t = 0; t < 4; ++t) {
      const v2u o = {pk(x1[t].x + bflo(gbm[t].x) * acc[t].x, x1[t].y + bfhi(gbm[t].x) * acc[t].y), pk(x1[t].z + bflo(gbm[t].y) * acc[t].z, x1[t].w + bfhi(gbm[t].y) * acc[t].w)};
      *(LAS v2u*)(ob + l15 * 128 + (((2 * t + (qq >> 1)) ^ (l15 & 7)) * 16) + (qq & 1) * 8) = o;
    }
    cfence();
#pragma unroll
    for (int i = 0; i < 2; ++i) {
      const v4u o = *(LAS v4u*)(ob + i * 1024 + (lane >> 3) * 128 + (((lane & 7) ^ (lane >> 3)) * 16));
      st128<0>(o, rout, (lane >> 3) * 1024 + (lane & 7) * 16, (rb * 64 + w * 16 + i * 8) * 1024 + ct * 128);
    }
  }
  STAMP(23);
}

extern "C" void kernel_launch(void* const* d_in, const int* in_sizes, int n_in, void* d_out, int out_size, void* d_ws, size_t ws_size, hipStream_t stream) {
  Params p;
  p.bx = (const u16*)d_in[4]; p.x = (const u16*)d_in[0]; p.wqkv = (const u16*)d_in[1]; p.wphi = (const u16*)d_in[2]; p.wo = (const u16*)d_in[3];
  p.wg = (const u16*)d_in[5]; p.wu = (const u16*)d_in[6]; p.wd = (const u16*)d_in[7]; p.c = (const u16*)d_in[8]; p.wmod = (const u16*)d_in[9];
  p.out = (u16*)d_out; p.ws = (char*)d_ws;
  (void)hipFuncSetAttribute((const void*)fused_block, hipFuncAttributeMaxDynamicSharedMemorySize, LDS_BYTES);
  fused_block<<<256, 256, LDS_BYTES, stream>>>(p);
}
```

```cpp
#include <hip/hip_runtime.h>
#define GAS __attribute__((address_space(1)))
#define LAS __attribute__((address_space(3)))
#define DI __device__ __forceinline__
typedef unsigned u32; typedef unsigned long long u64; typedef unsigned short u16;
typedef u32 v4u __attribute__((ext_vector_type(4)));
typedef u32 v2u __attribute__((ext_vector_type(2)));
typedef float f2 __attribute__((ext_vector_type(2)));
typedef float f4 __attribute__((ext_vector_type(4)));
typedef float f16v __attribute__((ext_vector_type(16)));
typedef short s4 __attribute__((ext_vector_type(4)));
typedef __bf16 b2 __attribute__((ext_vector_type(2)));
typedef __bf16 b8 __attribute__((ext_vector_type(8)));
typedef __amdgpu_buffer_rsrc_t rsrc_t;

#define STAMPS 0

constexpr int S_ = 2048, D_ = 512, F_ = 1376;
constexpr int WS_EPOCH = 0;
constexpr int WS_MODG = 4096;
constexpr int WS_FH = 20480;
constexpr int WS_FKV = WS_FH + 8192;
constexpr int WS_FO = WS_FKV + 2048;
constexpr int WS_FX = WS_FO + 8192;
constexpr int WS_FH2 = WS_FX + 4096;
constexpr int WS_ZP = 65536;
constexpr int WS_ST = 131072;
constexpr int WS_STAMP = 524288;
constexpr int WS_KVP = 1 << 20;
constexpr int WS_H = 2 << 20;
constexpr int WS_O = 4 << 20;
constexpr int WS_XS = 6 << 20;
constexpr int WS_H2 = 8 << 20;
constexpr int WS_END = WS_H2 + 128 * 43 * 1024;
constexpr int L_WK = 0, L_WV = 32768, L_WQ = 65536, L_PHI = 98304;
constexpr int L_WO = 0;
constexpr int L_W3 = 57344;
constexpr int L_W4 = 0;
constexpr int L_MISC = 155648;
constexpr int L_RED = L_MISC, L_MODB = L_MISC + 1024, L_KVL = L_MISC + 3072, L_ZL = L_KVL + 2048, L_ZERO = L_ZL + 128, L_UT = L_MISC + 5376;
constexpr int L_XRES = 126976;
constexpr int LDS_BYTES = 163840;
constexpr int AUX_SC1 = 16;
constexpr u32 SPIN_CAP = 1u << 18;

struct Params { const u16 *x, *wqkv, *wphi, *wo, *wg, *wu, *wd, *c, *wmod; u16* out; char* ws; };

DI float asf(u32 v) { return __builtin_bit_cast(float, v); }
DI u32 asu(float v) { return __builtin_bit_cast(u32, v); }
DI float bflo(u32 v) { return __builtin_bit_cast(float, v << 16); }
DI float bfhi(u32 v) { return __builtin_bit_cast(float, v & 0xffff0000u); }
DI u32 pk(float a, float b) { f2 t = {a, b}; return __builtin_bit_cast(u32, __builtin_convertvector(t, b2)); }
DI float rbf(float a) { return bflo(pk(a, a)); }
DI f16v mfma32(v4u a, v4u b, f16v c) { return __builtin_amdgcn_mfma_f32_32x32x16_bf16(__builtin_bit_cast(b8, a), __builtin_bit_cast(b8, b), c, 0, 0, 0); }
DI f4 mfma16(v4u a, v4u b, f4 c) { return __builtin_amdgcn_mfma_f32_16x16x32_bf16(__builtin_bit_cast(b8, a), __builtin_bit_cast(b8, b), c, 0, 0, 0); }
DI v2u trd(LAS char* p) { return __builtin_bit_cast(v2u, __builtin_amdgcn_ds_read_tr16_b64_v4i16((LAS s4*)p)); }
DI v4u trd2(LAS char* p0, LAS char* p1) { v2u a = trd(p0), b = trd(p1); return v4u{a.x, a.y, b.x, b.y}; }
DI rsrc_t mkrs(const void* p, int bytes) { return __builtin_amdgcn_make_buffer_rsrc((void*)p, 0, bytes, 0x00020000); }
template <int AUX> DI void st128(v4u v, rsrc_t rs, u32 voff, u32 soff) { __builtin_amdgcn_raw_buffer_store_b128(v, rs, voff + soff, 0, AUX); }
DI void dma16(rsrc_t rs, u32 voff, u32 soff, u32 lds) {
  asm volatile("s_mov_b32 m0, %0\n\ts_nop 0\n\tbuffer_load_dwordx4 %1, %2, %3 offen lds" ::"s"(lds), "v"(voff), "s"(rs), "s"(soff) : "memory");
}
DI void bar() { asm volatile("s_waitcnt lgkmcnt(0)\n\ts_barrier" ::: "memory"); }
DI void drain() { asm volatile("s_waitcnt vmcnt(0)" ::: "memory"); }
DI void cfence() { asm volatile("" ::: "memory"); }
template <int CTRL> DI float dppf(float v) { return __builtin_bit_cast(float, __builtin_amdgcn_update_dpp(0, __builtin_bit_cast(int, v), CTRL, 0xf, 0xf, true)); }
DI float sum16(float v) { auto r = __builtin_amdgcn_permlane16_swap(__builtin_bit_cast(u32, v), __builtin_bit_cast(u32, v), false, false); return asf(r[0]) + asf(r[1]); }
DI float sum32(float v) { auto r = __builtin_amdgcn_permlane32_swap(__builtin_bit_cast(u32, v), __builtin_bit_cast(u32, v), false, false); return asf(r[0]) + asf(r[1]); }
constexpr int ROR8 = 0x128, ROR4 = 0x124, ROR2 = 0x122, ROR1 = 0x121;
DI void wait_flags(rsrc_t ws, u32 off, int n, int lane, v2u tag) {
  const u32 vo = off + (lane < n ? lane : 0) * 8;
  for (u32 sp = 0; sp < SPIN_CAP; ++sp) {
    v2u f = __builtin_amdgcn_raw_buffer_load_b64(ws, vo, 0, AUX_SC1);
    cfence();
    if (__all(f.x == tag.x && f.y == tag.y)) break;
  }
  cfence();
}
DI void set_flag(rsrc_t ws, u32 off, int lane, v2u tag) { if (lane == 0) __builtin_amdgcn_raw_buffer_store_b64(tag, ws, off, 0, AUX_SC1); }
DI v2u wait_gran(rsrc_t ws, u32 voff, u32 tag) {
  v4u g;
  for (u32 sp = 0; sp < SPIN_CAP; ++sp) {
    g = __builtin_amdgcn_raw_buffer_load_b128(ws, voff, 0, AUX_SC1);
    cfence();
    if (__all(g.y == tag && g.w == tag)) break;
  }
  return v2u{g.x, g.z};
}
#define STAMP(i) do {} while (0)

DI void feature_map(const f16v& acc, v4u wp0, v4u wp1, u32 (&ph)[16]) {
  v4u b0 = {pk(acc[0], acc[1]), pk(acc[2], acc[3]), pk(acc[4], acc[5]), pk(acc[6], acc[7])};
  v4u b1 = {pk(acc[8], acc[9]), pk(acc[10], acc[11]), pk(acc[12], acc[13]), pk(acc[14], acc[15])};
  f16v z = {};
  f16v lg = mfma32(wp0, b0, z);
  lg = mfma32(wp1, b1, lg);
  float mx = lg[0], mn = lg[0];
#pragma unroll
  for (int i = 1; i < 16; ++i) { mx = __builtin_fmaxf(mx, lg[i]); mn = __builtin_fminf(mn, lg[i]); }
  const float L2E = 1.44269504088896f;
  const float cp = -mx * L2E, cn = mn * L2E;
  float ep[16], en[16];
#pragma unroll
  for (int i = 0; i < 16; ++i) { ep[i] = __builtin_amdgcn_exp2f(__builtin_fmaf(lg[i], L2E, cp)); en[i] = __builtin_amdgcn_exp2f(__builtin_fmaf(lg[i], -L2E, cn)); }
  f2 sp = {ep[0], ep[1]}, sn = {en[0], en[1]};
#pragma unroll
  for (int i = 1; i < 8; ++i) { sp += f2{ep[2 * i], ep[2 * i + 1]}; sn += f2{en[2 * i], en[2 * i + 1]}; }
  const float rp = __builtin_amdgcn_rcpf(sp.x + sp.y), rn = __builtin_amdgcn_rcpf(sn.x + sn.y);
#pragma unroll
  for (int i = 0; i < 8; ++i) { ph[i] = pk(ep[2 * i] * rp, ep[2 * i + 1] * rp); ph[8 + i] = pk(en[2 * i] * rn, en[2 * i + 1] * rn); }
}

__global__ void __launch_bounds__(256, 1) fused_block(Params p) {
  extern __shared__ __attribute__((aligned(16))) char smem_[];
  LAS char* L = (LAS char*)smem_;
  const u32 Lb = (u32)(size_t)L;
  const int tid = threadIdx.x, lane = tid & 63, b = blockIdx.x;
  const int w = __builtin_amdgcn_readfirstlane(tid >> 6);
  const int xg = b & 7, jj = b >> 3;
  const int pan = 4 * (xg >> 1) + (jj >> 3), mem = 8 * (xg & 1) + (jj & 7);
  const int rb = 4 * xg + (jj >> 3), ct = jj & 7;
  const int sp3 = 4 * (xg >> 2) + (jj >> 3), m3 = 8 * (xg & 3) + (jj & 7);
  const int u0 = m3 * 3 < 83 ? m3 * 3 : 83;
  const bool has3 = m3 < 29;
  STAMP(0);
  const u64 rt0_ = __builtin_amdgcn_s_memrealtime(); u64 rtx_ = rt0_;
  const rsrc_t ws = mkrs(p.ws, WS_END);
  const rsrc_t rx = mkrs(p.x, S_ * D_ * 2), rqkv = mkrs(p.wqkv, D_ * 3 * D_ * 2), rwo = mkrs(p.wo, D_ * D_ * 2);
  const rsrc_t rg = mkrs(p.wg, D_ * F_ * 2), ru = mkrs(p.wu, D_ * F_ * 2), rd = mkrs(p.wd, F_ * D_ * 2);
  const rsrc_t rmod = mkrs(p.wmod, D_ * 6 * D_ * 2), rc = mkrs(p.c, D_ * 2), rphi = mkrs(p.wphi, 32 * 256 * 2), rout = mkrs(p.out, S_ * D_ * 2);
  const int l31 = lane & 31, hh = lane >> 5, l15 = lane & 15, qq = lane >> 4;
  const int ta = (lane >> 2) & 3, tp = lane & 3;

  const v2u ep = __builtin_amdgcn_raw_buffer_load_b64(ws, WS_EPOCH, 0, AUX_SC1);
  const bool hasmod = jj < 24;
  const int mcol = jj < 8 ? (2 * xg + (jj >> 2)) * 64 + (jj & 3) * 16 : 1024 + (4 * xg + ((jj - 8) >> 2)) * 64 + (jj & 3) * 16;
  v4u wm[4]; u16 cc[4];
  if (hasmod) {
#pragma unroll
    for (int i = 0; i < 4; ++i) cc[i] = __builtin_amdgcn_raw_buffer_load_b16(rc, ((tid >> 1) + 128 * i) * 2, 0, 0);
#pragma unroll
    for (int i = 0; i < 4; ++i) wm[i] = __builtin_amdgcn_raw_buffer_load_b128(rmod, ((tid >> 1) + 128 * i) * 6144 + (tid & 1) * 16, mcol * 2, 0);
  }
  v4u xr[2];
#pragma unroll
  for (int i = 0; i < 2; ++i) xr[i] = __builtin_amdgcn_raw_buffer_load_b128(rx, (lane & 7) * 1024 + ((lane >> 3) + 8 * i) * 16, b * 8192 + w * 256, 0);
  u16 wp[8];
  {
    const int hd = (l31 >> 2) & 1, f = 4 * (l31 >> 3) + (l31 & 3);
#pragma unroll
    for (int j = 0; j < 8; ++j) wp[j] = __builtin_amdgcn_raw_buffer_load_b16(rphi, (hd * 256 + (8 * (j >> 2) + 4 * hh + (j & 3)) * 16 + f) * 2, mem * 1024, 0);
  }
#pragma unroll
  for (int i = 0; i < 2; ++i) dma16(rx, (lane >> 3) * 1024 + (lane & 7) * 16, (rb * 64 + w * 16 + i * 8) * 1024 + ct * 128, Lb + L_XRES + w * 2048 + i * 1024);
  {
    const u32 vo = (lane >> 2) * 3072 + (lane & 3) * 16;
    for (int i = 0; i < 8; ++i) {
      const int r16 = i * 4 + w;
      dma16(rqkv, vo, r16 * 16 * 3072 + 1024 + mem * 64, Lb + L_WK + r16 * 1024);
      dma16(rqkv, vo, r16 * 16 * 3072 + 2048 + mem * 64, Lb + L_WV + r16 * 1024);
    }
    for (int i = 0; i < 8; ++i) { const int r16 = i * 4 + w; dma16(rqkv, vo, r16 * 16 * 3072 + mem * 64, Lb + L_WQ + r16 * 1024); }
  }
  const u64 tag64 = (((u64)ep.y << 32) | ep.x) + 0x9E3779B97F4A7C15ull;
  const v2u tag = {(u32)__builtin_amdgcn_readfirstlane((u32)tag64), (u32)__builtin_amdgcn_readfirstlane((u32)(tag64 >> 32))};
  if (tid < 16) *(LAS u32*)(L + L_ZERO + tid * 4) = 0u;
  STAMP(1);
  if (hasmod) {
    float a[8] = {};
#pragma unroll
    for (int i = 0; i < 4; ++i) {
      const float cv = bflo(cc[i]);
      const float s = rbf(cv * __builtin_amdgcn_rcpf(1.f + __builtin_amdgcn_exp2f(-1.44269504088896f * cv)));
#pragma unroll
      for (int j = 0; j < 4; ++j) { a[2 * j] += s * bflo(wm[i][j]); a[2 * j + 1] += s * bfhi(wm[i][j]); }
    }
#pragma unroll
    for (int j = 0; j < 8; ++j) { float v = a[j]; v += dppf<ROR2>(v); v += dppf<ROR4>(v); v += dppf<ROR8>(v); a[j] = v; }
    if (l15 < 2) { *(LAS f4*)(L + L_RED + (w * 4 + qq) * 64 + l15 * 32) = f4{a[0], a[1], a[2], a[3]}; *(LAS f4*)(L + L_RED + (w * 4 + qq) * 64 + l15 * 32 + 16) = f4{a[4], a[5], a[6], a[7]}; }
  }
  float xv[16];
#pragma unroll
  for (int i = 0; i < 2; ++i)
#pragma unroll
    for (int j = 0; j < 4; ++j) { xv[8 * i + 2 * j] = bflo(xr[i][j]); xv[8 * i + 2 * j + 1] = bfhi(xr[i][j]); }
  float s1 = 0.f;
#pragma unroll
  for (int i = 0; i < 16; ++i) s1 += xv[i];
  s1 += dppf<ROR8>(s1); s1 = sum16(s1); s1 = sum32(s1);
  const float ml = s1 * (1.f / 128.f);
  float s2 = 0.f;
#pragma unroll
  for (int i = 0; i < 16; ++i) s2 += (xv[i] - ml) * (xv[i] - ml);
  s2 += dppf<ROR8>(s2); s2 = sum16(s2); s2 = sum32(s2);
  if (lane < 8) *(LAS f2*)(L + L_UT + lane * 32 + w * 8) = f2{ml, s2};
  bar();
  if (hasmod && w == 0) {
    f2 v = *(LAS f2*)(L + L_RED + (lane >> 3) * 128 + (lane & 7) * 8);
    v += *(LAS f2*)(L + L_RED + (lane >> 3) * 128 + 64 + (lane & 7) * 8);
    v.x += dppf<ROR8>(v.x); v.y += dppf<ROR8>(v.y);
    v.x = sum32(sum16(v.x)); v.y = sum32(sum16(v.y));
    if (lane < 8) __builtin_amdgcn_raw_buffer_store_b64(v2u{pk(v.x, v.y), tag.x}, ws, WS_MODG + lane * 8, mcol * 4, AUX_SC1);
  }
  STAMP(2);
  {
    const f4 t0 = *(LAS f4*)(L + L_UT + (lane & 7) * 32), t1 = *(LAS f4*)(L + L_UT + (lane & 7) * 32 + 16);
    const float m = (t0.x + t0.z + t1.x + t1.z) * 0.25f;
    const float M2 = t0.y + t0.w + t1.y + t1.w + 128.f * ((t0.x - m) * (t0.x - m) + (t0.z - m) * (t0.z - m) + (t1.x - m) * (t1.x - m) + (t1.z - m) * (t1.z - m));
    const float r = __builtin_amdgcn_rsqf(M2 * (1.f / 512.f) + 1e-5f);
#pragma unroll
    for (int i = 0; i < 16; ++i) xv[i] = (xv[i] - m) * r;
  }
  {
    const v2u g = wait_gran(ws, WS_MODG + hh * 2048 + w * 512 + l31 * 16, tag.x);
    *(LAS v2u*)(L + L_MODB + w * 512 + lane * 8) = g;
    cfence();
    STAMP(3);
#pragma unroll
    for (int i = 0; i < 2; ++i) {
      const v4u sa = *(LAS v4u*)(L + L_MODB + w * 512 + ((lane >> 3) + 8 * i) * 16);
      const v4u ca = *(LAS v4u*)(L + L_MODB + w * 512 + 256 + ((lane >> 3) + 8 * i) * 16);
      v4u h;
#pragma unroll
      for (int j = 0; j < 4; ++j) {
        const u32 g1 = pk(1.f + bflo(ca[j]), 1.f + bfhi(ca[j]));
        h[j] = pk(xv[8 * i + 2 * j] * bflo(g1) + bflo(sa[j]), xv[8 * i + 2 * j + 1] * bfhi(g1) + bfhi(sa[j]));
      }
      st128<AUX_SC1>(h, ws, ((lane >> 3) + 8 * i) * 512 + (lane & 7) * 16, WS_H + (b >> 2) * 32768 + w * 8192 + (b & 3) * 128);
    }
  }
  drain();
  bar();
  if (w == 0) set_flag(ws, WS_FH + (b >> 2) * 128 + (b & 3) * 8, lane, tag);
  STAMP(4);
  v2u xres[4];
#pragma unroll
  for (int t = 0; t < 4; ++t) xres[t] = *(LAS v2u*)(L + L_XRES + w * 2048 + l15 * 128 + t * 32 + qq * 8);
  v4u wp0, wp1;
  {
    const v4u v = {wp[0] | ((u32)wp[1] << 16), wp[2] | ((u32)wp[3] << 16), wp[4] | ((u32)wp[5] << 16), wp[6] | ((u32)wp[7] << 16)};
    const u32 m1 = ((l31 >> 2) & 1) ? 0xffffffffu : 0u;
    wp1 = v & m1; wp0 = v & ~m1;
  }

  const int t32 = pan * 4 + w;
  wait_flags(ws, WS_FH + t32 * 128, 4, lane, tag);
  STAMP(5);
  v4u hf[32];
#pragma unroll
  for (int s = 0; s < 32; ++s) hf[s] = __builtin_amdgcn_raw_buffer_load_b128(ws, lane * 16 + (s & 3) * 1024, WS_H + t32 * 32768 + (s >> 2) * 4096, AUX_SC1);
  __builtin_amdgcn_sched_barrier(0);
  LAS char* wa = L + (8 * hh + ta) * 64 + (qq & 1) * 32 + tp * 8;
  f16v ak = {}, av = {};
#pragma unroll
  for (int s = 0; s < 32; ++s) {
    ak = mfma32(trd2(wa + L_WK + s * 1024, wa + L_WK + s * 1024 + 256), hf[s], ak);
    av = mfma32(trd2(wa + L_WV + s * 1024, wa + L_WV + s * 1024 + 256), hf[s], av);
  }
  STAMP(6);
  {
    u32 ph[16];
    feature_map(ak, wp0, wp1, ph);
    LAS char* row = L + L_PHI + (w * 32 + l31) * 224;
#pragma unroll
    for (int i = 0; i < 4; ++i) *(LAS v4u*)(row + hh * 64 + i * 16) = v4u{ph[4 * i], ph[4 * i + 1], ph[4 * i + 2], ph[4 * i + 3]};
#pragma unroll
    for (int a = 0; a < 4; ++a) *(LAS v2u*)(row + 128 + a * 16 + hh * 8) = v2u{pk(av[4 * a], av[4 * a + 1]), pk(av[4 * a + 2], av[4 * a + 3])};
  }
  bar();
  STAMP(7);
  {
    LAS char* ra = L + L_PHI + (4 * qq + ta) * 224 + tp * 8;
    f4 kv = {}, zz = {};
    const v4u ones = {0x3f803f80u, 0x3f803f80u, 0x3f803f80u, 0x3f803f80u};
#pragma unroll
    for (int s = 0; s < 4; ++s) {
      const v4u A = trd2(ra + w * 32 + s * 32 * 224, ra + w * 32 + (s * 32 + 16) * 224);
      const v4u B = trd2(ra + 128 + (w >> 1) * 32 + s * 32 * 224, ra + 128 + (w >> 1) * 32 + (s * 32 + 16) * 224);
      kv = mfma16(A, B, kv); zz = mfma16(A, ones, zz);
    }
    st128<AUX_SC1>(__builtin_bit_cast(v4u, kv), ws, lane * 16, WS_KVP + (mem * 16 + pan) * 4096 + w * 1024);
    if (l15 == 0) st128<AUX_SC1>(__builtin_bit_cast(v4u, zz), ws, qq * 16, WS_ZP + (mem * 16 + pan) * 256 + w * 64);
  }
  drain();
  bar();
  if (w == 0) set_flag(ws, WS_FKV + mem * 128 + pan * 8, lane, tag);
  STAMP(8);
  const u32 vo128 = (lane >> 3) * 1024;
  {
    for (int i = 0; i < 16; ++i) {
      const int r8 = i * 4 + w;
      const u32 sw = (((lane >> 4) & 1) << 1) | ((w & 1) << 2);
      dma16(rwo, vo128 + ((lane & 7) ^ sw) * 16, r8 * 8192 + ct * 128, Lb + L_WO + r8 * 1024);
    }
  }
  u32 pq[16];
  {
    f16v aq = {};
#pragma unroll
    for (int s = 0; s < 32; ++s) aq = mfma32(trd2(wa + L_WQ + s * 1024, wa + L_WQ + s * 1024 + 256), hf[s], aq);
    feature_map(aq, wp0, wp1, pq);
  }
  STAMP(9);
  wait_flags(ws, WS_FKV + mem * 128, 16, lane, tag);
  STAMP(10);
  {
    f4 zq = __builtin_bit_cast(f4, __builtin_amdgcn_raw_buffer_load_b128(ws, (lane >> 2) * 256 + (lane & 3) * 16, WS_ZP + mem * 4096 + w * 64, AUX_SC1));
    v4u kp[16];
#pragma unroll
    for (int i = 0; i < 16; ++i) kp[i] = __builtin_amdgcn_raw_buffer_load_b128(ws, lane * 16, WS_KVP + (mem * 16 + i) * 4096 + w * 1024, AUX_SC1);
    __builtin_amdgcn_sched_barrier(0);
#pragma unroll
    for (int j = 0; j < 4; ++j) { float v = zq[j]; v += dppf<ROR4>(v); v += dppf<ROR8>(v); zq[j] = sum32(sum16(v)); }
    if (lane < 4) *(LAS v2u*)(L + L_ZL + w * 32 + lane * 8) = v2u{pk(zq.x, zq.y), pk(zq.z, zq.w)};
    f4 s = __builtin_bit_cast(f4, kp[0]);
#pragma unroll
    for (int i = 1; i < 16; ++i) s += __builtin_bit_cast(f4, kp[i]);
    *(LAS v2u*)(L + L_KVL + ((w >> 1) * 16 + l15) * 64 + (w & 1) * 32 + qq * 8) = v2u{pk(s.x, s.y), pk(s.z, s.w)};
  }
  bar();
  STAMP(11);
  {
    const bool mine = ((l31 >> 2) & 1) == hh;
    LAS char* pa = L + (mine ? L_KVL + (hh * 16 + 4 * (l31 >> 3) + (l31 & 3)) * 64 : L_ZERO);
    LAS char* pz = L + (mine ? L_ZL + hh * 64 : L_ZERO);
    f16v num = {}, den = {};
#pragma unroll
    for (int u = 0; u < 4; ++u) {
      const v4u B = {pq[4 * u], pq[4 * u + 1], pq[4 * u + 2], pq[4 * u + 3]};
      den = mfma32(*(LAS v4u*)(pz + u * 16), B, den);
      num = mfma32(*(LAS v4u*)(pa + u * 16), B, num);
    }
    const float ri = __builtin_amdgcn_rcpf(den[0] + 1e-6f);
#pragma unroll
    for (int e = 0; e < 2; ++e) {
      v4u o;
#pragma unroll
      for (int j = 0; j < 4; ++j) o[j] = pk(num[8 * e + 2 * j] * ri, num[8 * e + 2 * j + 1] * ri);
      st128<AUX_SC1>(o, ws, (l31 >> 4) * 16384 + (2 * hh + e) * 256 + l15 * 16, WS_O + t32 * 32768 + mem * 1024);
    }
  }
  drain();
  set_flag(ws, WS_FO + t32 * 128 + mem * 8, lane, tag);
  STAMP(12);
  u32 v3[3], v3u[3];
#pragma unroll
  for (int i = 0; i < 3; ++i) { const int id = i * 64 + lane, r = id / 6, c = id - 6 * r; v3[i] = r * 2752 + c * 16; v3u[i] = (r ^ 4) * 2752 + c * 16; }
  if (has3) {
    for (int g = 2 + (w >> 1); g < 16; g += 2) {
#pragma unroll
      for (int i = 0; i < 3; ++i) {
        if (w & 1) dma16(ru, v3u[i], g * 32 * 2752 + u0 * 32, Lb + L_W3 + g * 6144 + 3072 + i * 1024);
        else dma16(rg, v3[i], g * 32 * 2752 + u0 * 32, Lb + L_W3 + g * 6144 + i * 1024);
      }
    }
  }
  v2u gam[4], cbm[4], gbm[4];
#pragma unroll
  for (int t = 0; t < 4; ++t) {
    gam[t] = wait_gran(ws, WS_MODG + (1024 + ct * 64 + t * 16 + qq * 4) * 4, tag.x);
    cbm[t] = wait_gran(ws, WS_MODG + (2048 + ct * 64 + t * 16 + qq * 4) * 4, tag.x);
    gbm[t] = wait_gran(ws, WS_MODG + (2560 + ct * 64 + t * 16 + qq * 4) * 4, tag.x);
  }
  {
    v2u g = wait_gran(ws, WS_MODG + (1536 + tid * 4) * 4, tag.x);
    if (w >= 2) g = v2u{pk(1.f + bflo(g.x), 1.f + bfhi(g.x)), pk(1.f + bflo(g.y), 1.f + bfhi(g.y))};
    const int c4 = tid & 127;
    *(LAS v2u*)(L + L_MODB + (w >= 2 ? 0 : 1024) + (c4 >> 3) * 64 + (c4 & 3) * 16 + ((c4 >> 2) & 1) * 8) = g;
  }
  v4u wf[64];
  {
    const int sw = (((ta >> 1) & 1) << 1) | ((qq & 1) << 2);
#pragma unroll
    for (int t = 0; t < 4; ++t) {
      LAS char* a = L + L_WO + (8 * qq + ta) * 128 + (((2 * t + (tp >> 1)) ^ sw) * 16) + (tp & 1) * 8;
#pragma unroll
      for (int s = 0; s < 16; ++s) wf[s * 4 + t] = trd2(a + s * 4096, a + s * 4096 + 512);
    }
  }
  bar();
  STAMP(13);
  wait_flags(ws, WS_FO + (rb * 2 + (w >> 1)) * 128, 16, lane, tag);
  if (b == 0 && w == 0 && lane == 0) __builtin_amdgcn_raw_buffer_store_b64(tag, ws, WS_EPOCH, 0, AUX_SC1);
  STAMP(14);
  f4 x1[4];
  {
    v4u of[16];
#pragma unroll
    for (int s = 0; s < 16; ++s) of[s] = __builtin_amdgcn_raw_buffer_load_b128(ws, lane * 16 + (s & 3) * 1024, WS_O + (rb * 4 + w) * 16384 + (s >> 2) * 4096, AUX_SC1);
    __builtin_amdgcn_sched_barrier(0);
    f4 acc[4] = {};
#pragma unroll
    for (int s = 0; s < 16; ++s)
#pragma unroll
      for (int t = 0; t < 4; ++t) acc[t] = mfma16(wf[s * 4 + t], of[s], acc[t]);
    float sm = 0.f;
#pragma unroll
    for (int t = 0; t < 4; ++t) {
      x1[t] = f4{bflo(xres[t].x) + bflo(gam[t].x) * acc[t].x, bfhi(xres[t].x) + bfhi(gam[t].x) * acc[t].y, bflo(xres[t].y) + bflo(gam[t].y) * acc[t].z, bfhi(xres[t].y) + bfhi(gam[t].y) * acc[t].w};
      sm += (x1[t].x + x1[t].y) + (x1[t].z + x1[t].w);
    }
#pragma unroll
    for (int T = 0; T < 2; ++T) {
      v4u xs;
#pragma unroll
      for (int e = 0; e < 2; ++e) {
        const int t = 2 * T + e;
        const u32 g0 = pk(1.f + bflo(cbm[t].x), 1.f + bfhi(cbm[t].x)), g1 = pk(1.f + bflo(cbm[t].y), 1.f + bfhi(cbm[t].y));
        xs[2 * e] = pk(x1[t].x * bflo(g0), x1[t].y * bfhi(g0));
        xs[2 * e + 1] = pk(x1[t].z * bflo(g1), x1[t].w * bfhi(g1));
      }
      st128<AUX_SC1>(xs, ws, qq * 512 + l15 * 16 + T * 2048, WS_XS + (rb * 2 + (w >> 1)) * 32768 + ct * 4096 + (w & 1) * 256);
    }
    const float m = sum32(sum16(sm)) * (1.f / 64.f);
    float q2 = 0.f;
#pragma unroll
    for (int t = 0; t < 4; ++t)
#pragma unroll
      for (int r = 0; r < 4; ++r) q2 += (x1[t][r] - m) * (x1[t][r] - m);
    q2 = sum32(sum16(q2));
    if (lane < 16) __builtin_amdgcn_raw_buffer_store_b64(v2u{__builtin_bit_cast(u32, m), __builtin_bit_cast(u32, q2)}, ws, lane * 8, WS_ST + (rb * 2 + (w >> 1)) * 2048 + ct * 256 + (w & 1) * 128, AUX_SC1);
  }
  drain();
  bar();
  if (w == 0) set_flag(ws, WS_FX + rb * 128 + ct * 8, lane, tag);
  STAMP(15);
  const u32 vo4 = vo128 + ((lane & 7) ^ (((lane >> 4) & 3) << 1)) * 16;
  if (has3) {
    const int g = w >> 1;
#pragma unroll
    for (int i = 0; i < 3; ++i) {
      if (w & 1) dma16(ru, v3u[i], g * 32 * 2752 + u0 * 32, Lb + L_W3 + g * 6144 + 3072 + i * 1024);
      else dma16(rg, v3[i], g * 32 * 2752 + u0 * 32, Lb + L_W3 + g * 6144 + i * 1024);
    }
    drain();
  }
  for (int i = 0; i < 14; ++i) { const int r8 = i * 4 + w; dma16(rd, vo4, r8 * 8192 + ct * 128, Lb + L_W4 + r8 * 1024); }
  bar();
  if (has3) {
    LAS char* w3 = L + L_W3 + (qq & 1) * 3072 + ((4 * hh + ta) ^ ((qq & 1) * 4)) * 96 + tp * 8;
    {
      f16v ut[3] = {};
      LAS char* mb = L + L_MODB + ((l31 >> 2) & 1) * 1024 + hh * 16 + w * 256;
      LAS char* wk = w3 + w * 4 * 6144;
#pragma unroll
      for (int s = 0; s < 8; ++s) {
        const v4u A = *(LAS v4u*)(mb + s * 32);
#pragma unroll
        for (int t = 0; t < 3; ++t) { LAS char* a = wk + (s >> 1) * 6144 + (s & 1) * 768 + t * 32; ut[t] = mfma32(A, trd2(a, a + 1536), ut[t]); }
      }
#pragma unroll
      for (int t = 0; t < 3; ++t) *(LAS float*)(L + L_UT - 2304 + t * 1024 + lane * 16 + w * 4) = ut[t][0];
    }
    bar();
    u32 utp[3];
#pragma unroll
    for (int t = 0; t < 3; ++t) {
      const f4 v = *(LAS f4*)(L + L_UT - 2304 + t * 1024 + lane * 16);
      const float s = (v.x + v.y) + (v.z + v.w), hi = rbf(s);
      utp[t] = pk(hi, s - hi);
    }
    STAMP(16);
    const int rb3 = sp3 * 4 + w;
    wait_flags(ws, WS_FX + rb3 * 128, 8, lane, tag);
    STAMP(17);
    constexpr int RING = 12;
    v4u xf[2][RING];
    const u32 xo = WS_XS + rb3 * 65536;
#pragma unroll
    for (int s = 0; s < RING; ++s)
#pragma unroll
      for (int mt = 0; mt < 2; ++mt) xf[mt][s] = __builtin_amdgcn_raw_buffer_load_b128(ws, lane * 16 + (s & 3) * 1024, xo + mt * 32768 + (s >> 2) * 4096, AUX_SC1);
    __builtin_amdgcn_sched_barrier(0);
    f16v acc[2][3] = {};
    v2u st[2][4];
#pragma unroll
    for (int s = 0; s < 32; ++s) {
      v4u A[3];
#pragma unroll
      for (int t = 0; t < 3; ++t) { LAS char* a = w3 + (s >> 1) * 6144 + (s & 1) * 768 + t * 32; A[t] = trd2(a, a + 1536); }
#pragma unroll
      for (int mt = 0; mt < 2; ++mt) {
        const v4u B = xf[mt][s % RING];
#pragma unroll
        for (int t = 0; t < 3; ++t) acc[mt][t] = mfma32(A[t], B, acc[mt][t]);
        if (s + RING < 32) xf[mt][s % RING] = __builtin_amdgcn_raw_buffer_load_b128(ws, lane * 16 + ((s + RING) & 3) * 1024, xo + mt * 32768 + ((s + RING) >> 2) * 4096, AUX_SC1);
        if (s + RING == 32) {
#pragma unroll
          for (int i = 0; i < 4; ++i) st[mt][i] = __builtin_amdgcn_raw_buffer_load_b64(ws, (hh * 4 + i) * 256 + l31 * 8, WS_ST + (rb3 * 2 + mt) * 2048, AUX_SC1);
        }
      }
    }
    STAMP(18);
#pragma unroll
    for (int mt = 0; mt < 2; ++mt) {
      float mi[4], ms = 0.f, M2 = 0.f;
#pragma unroll
      for (int i = 0; i < 4; ++i) { mi[i] = asf(st[mt][i].x); ms += mi[i]; M2 += asf(st[mt][i].y); }
      const float m = sum32(ms) * 0.125f;
#pragma unroll
      for (int i = 0; i < 4; ++i) M2 += 64.f * (mi[i] - m) * (mi[i] - m);
      const float var = sum32(M2) * (1.f / 512.f) + 1e-5f;
      const float c = hh ? __builtin_amdgcn_sqrtf(var) : -m, chi = rbf(c);
      const float r2 = __builtin_amdgcn_rcpf(var);
      const v4u B = {pk(chi, chi), pk(c - chi, c - chi), 0u, 0u};
#pragma unroll
      for (int t = 0; t < 3; ++t) {
        const f16v a = mfma32(v4u{utp[t], utp[t], 0u, 0u}, B, acc[mt][t]);
        v4u h2;
#pragma unroll
        for (int j = 0; j < 4; ++j) h2[j] = pk(__builtin_fmaxf(a[2 * j], 0.f) * (a[8 + 2 * j] * r2), __builtin_fmaxf(a[2 * j + 1], 0.f) * (a[9 + 2 * j] * r2));
        const int u = u0 + t;
        st128<AUX_SC1>(h2, ws, (l31 >> 4) * 44032 + hh * 256 + l15 * 16, WS_H2 + (rb3 * 4 + mt * 2) * 44032 + u * 512);
      }
    }
    drain();
    set_flag(ws, WS_FH2 + rb3 * 256 + m3 * 8, lane, tag);
  } else {
    bar();
    drain();
  }
  bar();
  STAMP(19);
  for (int i = 14; i < 38; ++i) { const int r8 = i * 4 + w; dma16(rd, vo4, r8 * 8192 + ct * 128, Lb + L_W4 + r8 * 1024); }
  {
    const int r0 = 16 * (qq >> 1) + 4 * (qq & 1) + ta;
    const int sw = ((r0 >> 1) & 3) << 1;
    LAS char* a4[4];
#pragma unroll
    for (int t = 0; t < 4; ++t) a4[t] = L + L_W4 + r0 * 128 + (((2 * t + (tp >> 1)) ^ sw) * 16) + (tp & 1) * 8;
    v4u wd[56];
#pragma unroll
    for (int s = 0; s < 14; ++s)
#pragma unroll
      for (int t = 0; t < 4; ++t) wd[s * 4 + t] = trd2(a4[t] + s * 4096, a4[t] + s * 4096 + 1024);
    bar();
    for (int i = 38; i < 43; ++i) { const int r8 = i * 4 + w; dma16(rd, vo4, r8 * 8192 + ct * 128, Lb + L_W4 + (r8 - 152) * 1024); }
    STAMP(20);
    wait_flags(ws, WS_FH2 + rb * 256, 29, lane, tag);
    bar();
    STAMP(21);
    v4u gf[43];
#pragma unroll
    for (int s = 0; s < 43; ++s) gf[s] = __builtin_amdgcn_raw_buffer_load_b128(ws, lane * 16 + (s & 3) * 1024, WS_H2 + (rb * 4 + w) * 44032 + (s >> 2) * 4096, AUX_SC1);
    __builtin_amdgcn_sched_barrier(0);
    f4 acc[4] = {};
#pragma unroll
    for (int s = 0; s < 43; ++s)
#pragma unroll
      for (int t = 0; t < 4; ++t) {
        const int so = (s < 38 ? s : s - 38) * 4096;
        acc[t] = mfma16(s < 14 ? wd[s * 4 + t] : trd2(a4[t] + so, a4[t] + so + 1024), gf[s], acc[t]);
      }
    STAMP(22);
    LAS char* ob = L + L_MISC + w * 2048;
#pragma unroll
    for (int t = 0; t < 4; ++t) {
      const v2u o = {pk(x1[t].x + bflo(gbm[t].x) * acc[t].x, x1[t].y + bfhi(gbm[t].x) * acc[t].y), pk(x1[t].z + bflo(gbm[t].y) * acc[t].z, x1[t].w + bfhi(gbm[t].y) * acc[t].w)};
      *(LAS v2u*)(ob + l15 * 128 + (((2 * t + (qq >> 1)) ^ (l15 & 7)) * 16) + (qq & 1) * 8) = o;
    }
    cfence();
#pragma unroll
    for (int i = 0; i < 2; ++i) {
      const v4u o = *(LAS v4u*)(ob + i * 1024 + (lane >> 3) * 128 + (((lane & 7) ^ (lane >> 3)) * 16));
      st128<0>(o, rout, (lane >> 3) * 1024 + (lane & 7) * 16, (rb * 64 + w * 16 + i * 8) * 1024 + ct * 128);
    }
  }
  STAMP(23);
  { while (__builtin_amdgcn_s_memrealtime() < rt0_ + 6000) {} const u64 c0 = __builtin_amdgcn_s_memtime(); while (__builtin_amdgcn_s_memtime() < c0 + 96000) {} }
}

extern "C" void kernel_launch(void* const* d_in, const int* in_sizes, int n_in, void* d_out, int out_size, void* d_ws, size_t ws_size, hipStream_t stream) {
  Params p;
  p.x = (const u16*)d_in[0]; p.wqkv = (const u16*)d_in[1]; p.wphi = (const u16*)d_in[2]; p.wo = (const u16*)d_in[3];
  p.wg = (const u16*)d_in[5]; p.wu = (const u16*)d_in[6]; p.wd = (const u16*)d_in[7]; p.c = (const u16*)d_in[8]; p.wmod = (const u16*)d_in[9];
  p.out = (u16*)d_out; p.ws = (char*)d_ws;
  (void)hipFuncSetAttribute((const void*)fused_block, hipFuncAttributeMaxDynamicSharedMemorySize, LDS_BYTES);
  fused_block<<<256, 256, LDS_BYTES, stream>>>(p);
}
```

```cpp
#include <hip/hip_runtime.h>
#define GAS __attribute__((address_space(1)))
#define LAS __attribute__((address_space(3)))
#define DI __device__ __forceinline__
typedef unsigned u32; typedef unsigned long long u64; typedef unsigned short u16;
typedef u32 v4u __attribute__((ext_vector_type(4)));
typedef u32 v2u __attribute__((ext_vector_type(2)));
typedef float f2 __attribute__((ext_vector_type(2)));
typedef float f4 __attribute__((ext_vector_type(4)));
typedef float f16v __attribute__((ext_vector_type(16)));
typedef short s4 __attribute__((ext_vector_type(4)));
typedef __bf16 b2 __attribute__((ext_vector_type(2)));
typedef __bf16 b8 __attribute__((ext_vector_type(8)));
typedef __amdgpu_buffer_rsrc_t rsrc_t;

#define STAMPS 0

constexpr int S_ = 2048, D_ = 512, F_ = 1376;
constexpr int WS_EPOCH = 0;
constexpr int WS_MODG = 4096;
constexpr int WS_FH = 20480;
constexpr int WS_FKV = WS_FH + 8192;
constexpr int WS_FO = WS_FKV + 2048;
constexpr int WS_FX = WS_FO + 8192;
constexpr int WS_FH2 = WS_FX + 4096;
constexpr int WS_ZP = 65536;
constexpr int WS_ST = 131072;
constexpr int WS_STAMP = 524288;
constexpr int WS_KVP = 1 << 20;
constexpr int WS_H = 2 << 20;
constexpr int WS_O = 4 << 20;
constexpr int WS_XS = 6 << 20;
constexpr int WS_H2 = 8 << 20;
constexpr int WS_END = WS_H2 + 128 * 43 * 1024;
constexpr int L_WK = 0, L_WV = 32768, L_WQ = 65536, L_PHI = 98304;
constexpr int L_WO = 0;
constexpr int L_W3 = 57344;
constexpr int L_W4 = 0;
constexpr int L_MISC = 155648;
constexpr int L_RED = L_MISC, L_MODB = L_MISC + 1024, L_KVL = L_MISC + 3072, L_ZL = L_KVL + 2048, L_ZERO = L_ZL + 128, L_UT = L_MISC + 5376;
constexpr int L_XRES = 126976;
constexpr int LDS_BYTES = 163840;
constexpr int AUX_SC1 = 16;
constexpr u32 SPIN_CAP = 1u << 18;

struct Params { const u16 *x, *wqkv, *wphi, *wo, *wg, *wu, *wd, *c, *wmod; u16* out; char* ws; };

DI float asf(u32 v) { return __builtin_bit_cast(float, v); }
DI u32 asu(float v) { return __builtin_bit_cast(u32, v); }
DI float bflo(u32 v) { return __builtin_bit_cast(float, v << 16); }
DI float bfhi(u32 v) { return __builtin_bit_cast(float, v & 0xffff0000u); }
DI u32 pk(float a, float b) { f2 t = {a, b}; return __builtin_bit_cast(u32, __builtin_convertvector(t, b2)); }
DI float rbf(float a) { return bflo(pk(a, a)); }
DI f16v mfma32(v4u a, v4u b, f16v c) { return __builtin_amdgcn_mfma_f32_32x32x16_bf16(__builtin_bit_cast(b8, a), __builtin_bit_cast(b8, b), c, 0, 0, 0); }
DI f4 mfma16(v4u a, v4u b, f4 c) { return __builtin_amdgcn_mfma_f32_16x16x32_bf16(__builtin_bit_cast(b8, a), __builtin_bit_cast(b8, b), c, 0, 0, 0); }
DI v2u trd(LAS char* p) { return __builtin_bit_cast(v2u, __builtin_amdgcn_ds_read_tr16_b64_v4i16((LAS s4*)p)); }
DI v4u trd2(LAS char* p0, LAS char* p1) { v2u a = trd(p0), b = trd(p1); return v4u{a.x, a.y, b.x, b.y}; }
DI rsrc_t mkrs(const void* p, int bytes) { return __builtin_amdgcn_make_buffer_rsrc((void*)p, 0, bytes, 0x00020000); }
template <int AUX> DI void st128(v4u v, rsrc_t rs, u32 voff, u32 soff) { __builtin_amdgcn_raw_buffer_store_b128(v, rs, voff + soff, 0, AUX); }
DI void dma16(rsrc_t rs, u32 voff, u32 soff, u32 lds) {
  asm volatile("s_mov_b32 m0, %0\n\ts_nop 0\n\tbuffer_load_dwordx4 %1, %2, %3 offen lds" ::"s"(lds), "v"(voff), "s"(rs), "s"(soff) : "memory");
}
DI void bar() { asm volatile("s_waitcnt lgkmcnt(0)\n\ts_barrier" ::: "memory"); }
DI void drain() { asm volatile("s_waitcnt vmcnt(0)" ::: "memory"); }
DI void cfence() { asm volatile("" ::: "memory"); }
template <int CTRL> DI float dppf(float v) { return __builtin_bit_cast(float, __builtin_amdgcn_update_dpp(0, __builtin_bit_cast(int, v), CTRL, 0xf, 0xf, true)); }
DI float sum16(float v) { auto r = __builtin_amdgcn_permlane16_swap(__builtin_bit_cast(u32, v), __builtin_bit_cast(u32, v), false, false); return asf(r[0]) + asf(r[1]); }
DI float sum32(float v) { auto r = __builtin_amdgcn_permlane32_swap(__builtin_bit_cast(u32, v), __builtin_bit_cast(u32, v), false, false); return asf(r[0]) + asf(r[1]); }
constexpr int ROR8 = 0x128, ROR4 = 0x124, ROR2 = 0x122, ROR1 = 0x121;
DI void wait_flags(rsrc_t ws, u32 off, int n, int lane, v2u tag) {
  const u32 vo = off + (lane < n ? lane : 0) * 8;
  for (u32 sp = 0; sp < SPIN_CAP; ++sp) {
    v2u f = __builtin_amdgcn_raw_buffer_load_b64(ws, vo, 0, AUX_SC1);
    cfence();
    if (__all(f.x == tag.x && f.y == tag.y)) break;
  }
  cfence();
}
DI void set_flag(rsrc_t ws, u32 off, int lane, v2u tag) { if (lane == 0) __builtin_amdgcn_raw_buffer_store_b64(tag, ws, off, 0, AUX_SC1); }
DI v2u wait_gran(rsrc_t ws, u32 voff, u32 tag) {
  v4u g;
  for (u32 sp = 0; sp < SPIN_CAP; ++sp) {
    g = __builtin_amdgcn_raw_buffer_load_b128(ws, voff, 0, AUX_SC1);
    cfence();
    if (__all(g.y == tag && g.w == tag)) break;
  }
  return v2u{g.x, g.z};
}
#define STAMP(i) do {} while (0)

DI void feature_map(const f16v& acc, v4u wp0, v4u wp1, u32 (&ph)[16]) {
  v4u b0 = {pk(acc[0], acc[1]), pk(acc[2], acc[3]), pk(acc[4], acc[5]), pk(acc[6], acc[7])};
  v4u b1 = {pk(acc[8], acc[9]), pk(acc[10], acc[11]), pk(acc[12], acc[13]), pk(acc[14], acc[15])};
  f16v z = {};
  f16v lg = mfma32(wp0, b0, z);
  lg = mfma32(wp1, b1, lg);
  float mx = lg[0], mn = lg[0];
#pragma unroll
  for (int i = 1; i < 16; ++i) { mx = __builtin_fmaxf(mx, lg[i]); mn = __builtin_fminf(mn, lg[i]); }
  const float L2E = 1.44269504088896f;
  const float cp = -mx * L2E, cn = mn * L2E;
  float ep[16], en[16];
#pragma unroll
  for (int i = 0; i < 16; ++i) { ep[i] = __builtin_amdgcn_exp2f(__builtin_fmaf(lg[i], L2E, cp)); en[i] = __builtin_amdgcn_exp2f(__builtin_fmaf(lg[i], -L2E, cn)); }
  f2 sp = {ep[0], ep[1]}, sn = {en[0], en[1]};
#pragma unroll
  for (int i = 1; i < 8; ++i) { sp += f2{ep[2 * i], ep[2 * i + 1]}; sn += f2{en[2 * i], en[2 * i + 1]}; }
  const float rp = __builtin_amdgcn_rcpf(sp.x + sp.y), rn = __builtin_amdgcn_rcpf(sn.x + sn.y);
#pragma unroll
  for (int i = 0; i < 8; ++i) { ph[i] = pk(ep[2 * i] * rp, ep[2 * i + 1] * rp); ph[8 + i] = pk(en[2 * i] * rn, en[2 * i + 1] * rn); }
}

__global__ void __launch_bounds__(256, 1) fused_block(Params p) {
  extern __shared__ __attribute__((aligned(16))) char smem_[];
  LAS char* L = (LAS char*)smem_;
  const u32 Lb = (u32)(size_t)L;
  const int tid = threadIdx.x, lane = tid & 63, b = blockIdx.x;
  const int w = __builtin_amdgcn_readfirstlane(tid >> 6);
  const int xg = b & 7, jj = b >> 3;
  const int pan = 4 * (xg >> 1) + (jj >> 3), mem = 8 * (xg & 1) + (jj & 7);
  const int rb = 4 * xg + (jj >> 3), ct = jj & 7;
  const int sp3 = 4 * (xg >> 2) + (jj >> 3), m3 = 8 * (xg & 3) + (jj & 7);
  const int u0 = m3 * 3 < 83 ? m3 * 3 : 83;
  const bool has3 = m3 < 29;
  STAMP(0);
  const u64 rt0_ = __builtin_amdgcn_s_memrealtime();
  const rsrc_t ws = mkrs(p.ws, WS_END);
  const rsrc_t rx = mkrs(p.x, S_ * D_ * 2), rqkv = mkrs(p.wqkv, D_ * 3 * D_ * 2), rwo = mkrs(p.wo, D_ * D_ * 2);
  const rsrc_t rg = mkrs(p.wg, D_ * F_ * 2), ru = mkrs(p.wu, D_ * F_ * 2), rd = mkrs(p.wd, F_ * D_ * 2);
  const rsrc_t rmod = mkrs(p.wmod, D_ * 6 * D_ * 2), rc = mkrs(p.c, D_ * 2), rphi = mkrs(p.wphi, 32 * 256 * 2), rout = mkrs(p.out, S_ * D_ * 2);
  const int l31 = lane & 31, hh = lane >> 5, l15 = lane & 15, qq = lane >> 4;
  const int ta = (lane >> 2) & 3, tp = lane & 3;

  if (tid == 0) __builtin_amdgcn_raw_buffer_store_b64(v2u{(u32)rt0_, (u32)(__builtin_amdgcn_s_getreg((3 << 11) | 20) & 0xF)}, ws, WS_STAMP + b * 8, 0, AUX_SC1);
  const v2u ep = __builtin_amdgcn_raw_buffer_load_b64(ws, WS_EPOCH, 0, AUX_SC1);
  const bool hasmod = jj < 24;
  const int mcol = jj < 8 ? (2 * xg + (jj >> 2)) * 64 + (jj & 3) * 16 : 1024 + (4 * xg + ((jj - 8) >> 2)) * 64 + (jj & 3) * 16;
  v4u wm[4]; u16 cc[4];
  if (hasmod) {
#pragma unroll
    for (int i = 0; i < 4; ++i) cc[i] = __builtin_amdgcn_raw_buffer_load_b16(rc, ((tid >> 1) + 128 * i) * 2, 0, 0);
#pragma unroll
    for (int i = 0; i < 4; ++i) wm[i] = __builtin_amdgcn_raw_buffer_load_b128(rmod, ((tid >> 1) + 128 * i) * 6144 + (tid & 1) * 16, mcol * 2, 0);
  }
  v4u xr[2];
#pragma unroll
  for (int i = 0; i < 2; ++i) xr[i] = __builtin_amdgcn_raw_buffer_load_b128(rx, (lane & 7) * 1024 + ((lane >> 3) + 8 * i) * 16, b * 8192 + w * 256, 0);
  u16 wp[8];
  {
    const int hd = (l31 >> 2) & 1, f = 4 * (l31 >> 3) + (l31 & 3);
#pragma unroll
    for (int j = 0; j < 8; ++j) wp[j] = __builtin_amdgcn_raw_buffer_load_b16(rphi, (hd * 256 + (8 * (j >> 2) + 4 * hh + (j & 3)) * 16 + f) * 2, mem * 1024, 0);
  }
#pragma unroll
  for (int i = 0; i < 2; ++i) dma16(rx, (lane >> 3) * 1024 + (lane & 7) * 16, (rb * 64 + w * 16 + i * 8) * 1024 + ct * 128, Lb + L_XRES + w * 2048 + i * 1024);
  {
    const u32 vo = (lane >> 2) * 3072 + (lane & 3) * 16;
    for (int i = 0; i < 8; ++i) {
      const int r16 = i * 4 + w;
      dma16(rqkv, vo, r16 * 16 * 3072 + 1024 + mem * 64, Lb + L_WK + r16 * 1024);
      dma16(rqkv, vo, r16 * 16 * 3072 + 2048 + mem * 64, Lb + L_WV + r16 * 1024);
    }
    for (int i = 0; i < 8; ++i) { const int r16 = i * 4 + w; dma16(rqkv, vo, r16 * 16 * 3072 + mem * 64, Lb + L_WQ + r16 * 1024); }
  }
  const u64 tag64 = (((u64)ep.y << 32) | ep.x) + 0x9E3779B97F4A7C15ull;
  const v2u tag = {(u32)__builtin_amdgcn_readfirstlane((u32)tag64), (u32)__builtin_amdgcn_readfirstlane((u32)(tag64 >> 32))};
  if (tid < 16) *(LAS u32*)(L + L_ZERO + tid * 4) = 0u;
  STAMP(1);
  if (hasmod) {
    float a[8] = {};
#pragma unroll
    for (int i = 0; i < 4; ++i) {
      const float cv = bflo(cc[i]);
      const float s = rbf(cv * __builtin_amdgcn_rcpf(1.f + __builtin_amdgcn_exp2f(-1.44269504088896f * cv)));
#pragma unroll
      for (int j = 0; j < 4; ++j) { a[2 * j] += s * bflo(wm[i][j]); a[2 * j + 1] += s * bfhi(wm[i][j]); }
    }
#pragma unroll
    for (int j = 0; j < 8; ++j) { float v = a[j]; v += dppf<ROR2>(v); v += dppf<ROR4>(v); v += dppf<ROR8>(v); a[j] = v; }
    if (l15 < 2) { *(LAS f4*)(L + L_RED + (w * 4 + qq) * 64 + l15 * 32) = f4{a[0], a[1], a[2], a[3]}; *(LAS f4*)(L + L_RED + (w * 4 + qq) * 64 + l15 * 32 + 16) = f4{a[4], a[5], a[6], a[7]}; }
  }
  float xv[16];
#pragma unroll
  for (int i = 0; i < 2; ++i)
#pragma unroll
    for (int j = 0; j < 4; ++j) { xv[8 * i + 2 * j] = bflo(xr[i][j]); xv[8 * i + 2 * j + 1] = bfhi(xr[i][j]); }
  float s1 = 0.f;
#pragma unroll
  for (int i = 0; i < 16; ++i) s1 += xv[i];
  s1 += dppf<ROR8>(s1); s1 = sum16(s1); s1 = sum32(s1);
  const float ml = s1 * (1.f / 128.f);
  float s2 = 0.f;
#pragma unroll
  for (int i = 0; i < 16; ++i) s2 += (xv[i] - ml) * (xv[i] - ml);
  s2 += dppf<ROR8>(s2); s2 = sum16(s2); s2 = sum32(s2);
  if (lane < 8) *(LAS f2*)(L + L_UT + lane * 32 + w * 8) = f2{ml, s2};
  bar();
  if (hasmod && w == 0) {
    f2 v = *(LAS f2*)(L + L_RED + (lane >> 3) * 128 + (lane & 7) * 8);
    v += *(LAS f2*)(L + L_RED + (lane >> 3) * 128 + 64 + (lane & 7) * 8);
    v.x += dppf<ROR8>(v.x); v.y += dppf<ROR8>(v.y);
    v.x = sum32(sum16(v.x)); v.y = sum32(sum16(v.y));
    if (lane < 8) __builtin_amdgcn_raw_buffer_store_b64(v2u{pk(v.x, v.y), tag.x}, ws, WS_MODG + lane * 8, mcol * 4, AUX_SC1);
  }
  STAMP(2);
  {
    const f4 t0 = *(LAS f4*)(L + L_UT + (lane & 7) * 32), t1 = *(LAS f4*)(L + L_UT + (lane & 7) * 32 + 16);
    const float m = (t0.x + t0.z + t1.x + t1.z) * 0.25f;
    const float M2 = t0.y + t0.w + t1.y + t1.w + 128.f * ((t0.x - m) * (t0.x - m) + (t0.z - m) * (t0.z - m) + (t1.x - m) * (t1.x - m) + (t1.z - m) * (t1.z - m));
    const float r = __builtin_amdgcn_rsqf(M2 * (1.f / 512.f) + 1e-5f);
#pragma unroll
    for (int i = 0; i < 16; ++i) xv[i] = (xv[i] - m) * r;
  }
  {
    const v2u g = wait_gran(ws, WS_MODG + hh * 2048 + w * 512 + l31 * 16, tag.x);
    *(LAS v2u*)(L + L_MODB + w * 512 + lane * 8) = g;
    cfence();
    STAMP(3);
#pragma unroll
    for (int i = 0; i < 2; ++i) {
      const v4u sa = *(LAS v4u*)(L + L_MODB + w * 512 + ((lane >> 3) + 8 * i) * 16);
      const v4u ca = *(LAS v4u*)(L + L_MODB + w * 512 + 256 + ((lane >> 3) + 8 * i) * 16);
      v4u h;
#pragma unroll
      for (int j = 0; j < 4; ++j) {
        const u32 g1 = pk(1.f + bflo(ca[j]), 1.f + bfhi(ca[j]));
        h[j] = pk(xv[8 * i + 2 * j] * bflo(g1) + bflo(sa[j]), xv[8 * i + 2 * j + 1] * bfhi(g1) + bfhi(sa[j]));
      }
      st128<AUX_SC1>(h, ws, ((lane >> 3) + 8 * i) * 512 + (lane & 7) * 16, WS_H + (b >> 2) * 32768 + w * 8192 + (b & 3) * 128);
    }
  }
  drain();
  bar();
  if (w == 0) set_flag(ws, WS_FH + (b >> 2) * 128 + (b & 3) * 8, lane, tag);
  STAMP(4);
  v2u xres[4];
#pragma unroll
  for (int t = 0; t < 4; ++t) xres[t] = *(LAS v2u*)(L + L_XRES + w * 2048 + l15 * 128 + t * 32 + qq * 8);
  v4u wp0, wp1;
  {
    const v4u v = {wp[0] | ((u32)wp[1] << 16), wp[2] | ((u32)wp[3] << 16), wp[4] | ((u32)wp[5] << 16), wp[6] | ((u32)wp[7] << 16)};
    const u32 m1 = ((l31 >> 2) & 1) ? 0xffffffffu : 0u;
    wp1 = v & m1; wp0 = v & ~m1;
  }

  const int t32 = pan * 4 + w;
  wait_flags(ws, WS_FH + t32 * 128, 4, lane, tag);
  STAMP(5);
  v4u hf[32];
#pragma unroll
  for (int s = 0; s < 32; ++s) hf[s] = __builtin_amdgcn_raw_buffer_load_b128(ws, lane * 16 + (s & 3) * 1024, WS_H + t32 * 32768 + (s >> 2) * 4096, AUX_SC1);
  __builtin_amdgcn_sched_barrier(0);
  LAS char* wa = L + (8 * hh + ta) * 64 + (qq & 1) * 32 + tp * 8;
  f16v ak = {}, av = {};
#pragma unroll
  for (int s = 0; s < 32; ++s) {
    ak = mfma32(trd2(wa + L_WK + s * 1024, wa + L_WK + s * 1024 + 256), hf[s], ak);
    av = mfma32(trd2(wa + L_WV + s * 1024, wa + L_WV + s * 1024 + 256), hf[s], av);
  }
  STAMP(6);
  {
    u32 ph[16];
    feature_map(ak, wp0, wp1, ph);
    LAS char* row = L + L_PHI + (w * 32 + l31) * 224;
#pragma unroll
    for (int i = 0; i < 4; ++i) *(LAS v4u*)(row + hh * 64 + i * 16) = v4u{ph[4 * i], ph[4 * i + 1], ph[4 * i + 2], ph[4 * i + 3]};
#pragma unroll
    for (int a = 0; a < 4; ++a) *(LAS v2u*)(row + 128 + a * 16 + hh * 8) = v2u{pk(av[4 * a], av[4 * a + 1]), pk(av[4 * a + 2], av[4 * a + 3])};
  }
  bar();
  STAMP(7);
  {
    LAS char* ra = L + L_PHI + (4 * qq + ta) * 224 + tp * 8;
    f4 kv = {}, zz = {};
    const v4u ones = {0x3f803f80u, 0x3f803f80u, 0x3f803f80u, 0x3f803f80u};
#pragma unroll
    for (int s = 0; s < 4; ++s) {
      const v4u A = trd2(ra + w * 32 + s * 32 * 224, ra + w * 32 + (s * 32 + 16) * 224);
      const v4u B = trd2(ra + 128 + (w >> 1) * 32 + s * 32 * 224, ra + 128 + (w >> 1) * 32 + (s * 32 + 16) * 224);
      kv = mfma16(A, B, kv); zz = mfma16(A, ones, zz);
    }
    st128<AUX_SC1>(__builtin_bit_cast(v4u, kv), ws, lane * 16, WS_KVP + (mem * 16 + pan) * 4096 + w * 1024);
    if (l15 == 0) st128<AUX_SC1>(__builtin_bit_cast(v4u, zz), ws, qq * 16, WS_ZP + (mem * 16 + pan) * 256 + w * 64);
  }
  drain();
  bar();
  if (w == 0) set_flag(ws, WS_FKV + mem * 128 + pan * 8, lane, tag);
  STAMP(8);
  const u32 vo128 = (lane >> 3) * 1024;
  {
    for (int i = 0; i < 16; ++i) {
      const int r8 = i * 4 + w;
      const u32 sw = (((lane >> 4) & 1) << 1) | ((w & 1) << 2);
      dma16(rwo, vo128 + ((lane & 7) ^ sw) * 16, r8 * 8192 + ct * 128, Lb + L_WO + r8 * 1024);
    }
  }
  u32 pq[16];
  {
    f16v aq = {};
#pragma unroll
    for (int s = 0; s < 32; ++s) aq = mfma32(trd2(wa + L_WQ + s * 1024, wa + L_WQ + s * 1024 + 256), hf[s], aq);
    feature_map(aq, wp0, wp1, pq);
  }
  STAMP(9);
  wait_flags(ws, WS_FKV + mem * 128, 16, lane, tag);
  STAMP(10);
  {
    f4 zq = __builtin_bit_cast(f4, __builtin_amdgcn_raw_buffer_load_b128(ws, (lane >> 2) * 256 + (lane & 3) * 16, WS_ZP + mem * 4096 + w * 64, AUX_SC1));
    v4u kp[16];
#pragma unroll
    for (int i = 0; i < 16; ++i) kp[i] = __builtin_amdgcn_raw_buffer_load_b128(ws, lane * 16, WS_KVP + (mem * 16 + i) * 4096 + w * 1024, AUX_SC1);
    __builtin_amdgcn_sched_barrier(0);
#pragma unroll
    for (int j = 0; j < 4; ++j) { float v = zq[j]; v += dppf<ROR4>(v); v += dppf<ROR8>(v); zq[j] = sum32(sum16(v)); }
    if (lane < 4) *(LAS v2u*)(L + L_ZL + w * 32 + lane * 8) = v2u{pk(zq.x, zq.y), pk(zq.z, zq.w)};
    f4 s = __builtin_bit_cast(f4, kp[0]);
#pragma unroll
    for (int i = 1; i < 16; ++i) s += __builtin_bit_cast(f4, kp[i]);
    *(LAS v2u*)(L + L_KVL + ((w >> 1) * 16 + l15) * 64 + (w & 1) * 32 + qq * 8) = v2u{pk(s.x, s.y), pk(s.z, s.w)};
  }
  bar();
  STAMP(11);
  {
    const bool mine = ((l31 >> 2) & 1) == hh;
    LAS char* pa = L + (mine ? L_KVL + (hh * 16 + 4 * (l31 >> 3) + (l31 & 3)) * 64 : L_ZERO);
    LAS char* pz = L + (mine ? L_ZL + hh * 64 : L_ZERO);
    f16v num = {}, den = {};
#pragma unroll
    for (int u = 0; u < 4; ++u) {
      const v4u B = {pq[4 * u], pq[4 * u + 1], pq[4 * u + 2], pq[4 * u + 3]};
      den = mfma32(*(LAS v4u*)(pz + u * 16), B, den);
      num = mfma32(*(LAS v4u*)(pa + u * 16), B, num);
    }
    const float ri = __builtin_amdgcn_rcpf(den[0] + 1e-6f);
#pragma unroll
    for (int e = 0; e < 2; ++e) {
      v4u o;
#pragma unroll
      for (int j = 0; j < 4; ++j) o[j] = pk(num[8 * e + 2 * j] * ri, num[8 * e + 2 * j + 1] * ri);
      st128<AUX_SC1>(o, ws, (l31 >> 4) * 16384 + (2 * hh + e) * 256 + l15 * 16, WS_O + t32 * 32768 + mem * 1024);
    }
  }
  drain();
  set_flag(ws, WS_FO + t32 * 128 + mem * 8, lane, tag);
  STAMP(12);
  u32 v3[3], v3u[3];
#pragma unroll
  for (int i = 0; i < 3; ++i) { const int id = i * 64 + lane, r = id / 6, c = id - 6 * r; v3[i] = r * 2752 + c * 16; v3u[i] = (r ^ 4) * 2752 + c * 16; }
  if (has3) {
    for (int g = 2 + (w >> 1); g < 16; g += 2) {
#pragma unroll
      for (int i = 0; i < 3; ++i) {
        if (w & 1) dma16(ru, v3u[i], g * 32 * 2752 + u0 * 32, Lb + L_W3 + g * 6144 + 3072 + i * 1024);
        else dma16(rg, v3[i], g * 32 * 2752 + u0 * 32, Lb + L_W3 + g * 6144 + i * 1024);
      }
    }
  }
  v2u gam[4], cbm[4], gbm[4];
#pragma unroll
  for (int t = 0; t < 4; ++t) {
    gam[t] = wait_gran(ws, WS_MODG + (1024 + ct * 64 + t * 16 + qq * 4) * 4, tag.x);
    cbm[t] = wait_gran(ws, WS_MODG + (2048 + ct * 64 + t * 16 + qq * 4) * 4, tag.x);
    gbm[t] = wait_gran(ws, WS_MODG + (2560 + ct * 64 + t * 16 + qq * 4) * 4, tag.x);
  }
  {
    v2u g = wait_gran(ws, WS_MODG + (1536 + tid * 4) * 4, tag.x);
    if (w >= 2) g = v2u{pk(1.f + bflo(g.x), 1.f + bfhi(g.x)), pk(1.f + bflo(g.y), 1.f + bfhi(g.y))};
    const int c4 = tid & 127;
    *(LAS v2u*)(L + L_MODB + (w >= 2 ? 0 : 1024) + (c4 >> 3) * 64 + (c4 & 3) * 16 + ((c4 >> 2) & 1) * 8) = g;
  }
  v4u wf[64];
  {
    const int sw = (((ta >> 1) & 1) << 1) | ((qq & 1) << 2);
#pragma unroll
    for (int t = 0; t < 4; ++t) {
      LAS char* a = L + L_WO + (8 * qq + ta) * 128 + (((2 * t + (tp >> 1)) ^ sw) * 16) + (tp & 1) * 8;
#pragma unroll
      for (int s = 0; s < 16; ++s) wf[s * 4 + t] = trd2(a + s * 4096, a + s * 4096 + 512);
    }
  }
  bar();
  STAMP(13);
  wait_flags(ws, WS_FO + (rb * 2 + (w >> 1)) * 128, 16, lane, tag);
  if (b == 0 && w == 0 && lane == 0) __builtin_amdgcn_raw_buffer_store_b64(tag, ws, WS_EPOCH, 0, AUX_SC1);
  STAMP(14);
  f4 x1[4];
  {
    v4u of[16];
#pragma unroll
    for (int s = 0; s < 16; ++s) of[s] = __builtin_amdgcn_raw_buffer_load_b128(ws, lane * 16 + (s & 3) * 1024, WS_O + (rb * 4 + w) * 16384 + (s >> 2) * 4096, AUX_SC1);
    __builtin_amdgcn_sched_barrier(0);
    f4 acc[4] = {};
#pragma unroll
    for (int s = 0; s < 16; ++s)
#pragma unroll
      for (int t = 0; t < 4; ++t) acc[t] = mfma16(wf[s * 4 + t], of[s], acc[t]);
    float sm = 0.f;
#pragma unroll
    for (int t = 0; t < 4; ++t) {
      x1[t] = f4{bflo(xres[t].x) + bflo(gam[t].x) * acc[t].x, bfhi(xres[t].x) + bfhi(gam[t].x) * acc[t].y, bflo(xres[t].y) + bflo(gam[t].y) * acc[t].z, bfhi(xres[t].y) + bfhi(gam[t].y) * acc[t].w};
      sm += (x1[t].x + x1[t].y) + (x1[t].z + x1[t].w);
    }
#pragma unroll
    for (int T = 0; T < 2; ++T) {
      v4u xs;
#pragma unroll
      for (int e = 0; e < 2; ++e) {
        const int t = 2 * T + e;
        const u32 g0 = pk(1.f + bflo(cbm[t].x), 1.f + bfhi(cbm[t].x)), g1 = pk(1.f + bflo(cbm[t].y), 1.f + bfhi(cbm[t].y));
        xs[2 * e] = pk(x1[t].x * bflo(g0), x1[t].y * bfhi(g0));
        xs[2 * e + 1] = pk(x1[t].z * bflo(g1), x1[t].w * bfhi(g1));
      }
      st128<AUX_SC1>(xs, ws, qq * 512 + l15 * 16 + T * 2048, WS_XS + (rb * 2 + (w >> 1)) * 32768 + ct * 4096 + (w & 1) * 256);
    }
    const float m = sum32(sum16(sm)) * (1.f / 64.f);
    float q2 = 0.f;
#pragma unroll
    for (int t = 0; t < 4; ++t)
#pragma unroll
      for (int r = 0; r < 4; ++r) q2 += (x1[t][r] - m) * (x1[t][r] - m);
    q2 = sum32(sum16(q2));
    if (lane < 16) __builtin_amdgcn_raw_buffer_store_b64(v2u{__builtin_bit_cast(u32, m), __builtin_bit_cast(u32, q2)}, ws, lane * 8, WS_ST + (rb * 2 + (w >> 1)) * 2048 + ct * 256 + (w & 1) * 128, AUX_SC1);
  }
  drain();
  bar();
  if (w == 0) set_flag(ws, WS_FX + rb * 128 + ct * 8, lane, tag);
  STAMP(15);
  const u32 vo4 = vo128 + ((lane & 7) ^ (((lane >> 4) & 3) << 1)) * 16;
  if (has3) {
    const int g = w >> 1;
#pragma unroll
    for (int i = 0; i < 3; ++i) {
      if (w & 1) dma16(ru, v3u[i], g * 32 * 2752 + u0 * 32, Lb + L_W3 + g * 6144 + 3072 + i * 1024);
      else dma16(rg, v3[i], g * 32 * 2752 + u0 * 32, Lb + L_W3 + g * 6144 + i * 1024);
    }
    drain();
  }
  for (int i = 0; i < 14; ++i) { const int r8 = i * 4 + w; dma16(rd, vo4, r8 * 8192 + ct * 128, Lb + L_W4 + r8 * 1024); }
  bar();
  if (has3) {
    LAS char* w3 = L + L_W3 + (qq & 1) * 3072 + ((4 * hh + ta) ^ ((qq & 1) * 4)) * 96 + tp * 8;
    {
      f16v ut[3] = {};
      LAS char* mb = L + L_MODB + ((l31 >> 2) & 1) * 1024 + hh * 16 + w * 256;
      LAS char* wk = w3 + w * 4 * 6144;
#pragma unroll
      for (int s = 0; s < 8; ++s) {
        const v4u A = *(LAS v4u*)(mb + s * 32);
#pragma unroll
        for (int t = 0; t < 3; ++t) { LAS char* a = wk + (s >> 1) * 6144 + (s & 1) * 768 + t * 32; ut[t] = mfma32(A, trd2(a, a + 1536), ut[t]); }
      }
#pragma unroll
      for (int t = 0; t < 3; ++t) *(LAS float*)(L + L_UT - 2304 + t * 1024 + lane * 16 + w * 4) = ut[t][0];
    }
    bar();
    u32 utp[3];
#pragma unroll
    for (int t = 0; t < 3; ++t) {
      const f4 v = *(LAS f4*)(L + L_UT - 2304 + t * 1024 + lane * 16);
      const float s = (v.x + v.y) + (v.z + v.w), hi = rbf(s);
      utp[t] = pk(hi, s - hi);
    }
    STAMP(16);
    const int rb3 = sp3 * 4 + w;
    wait_flags(ws, WS_FX + rb3 * 128, 8, lane, tag);
    STAMP(17);
    constexpr int RING = 12;
    v4u xf[2][RING];
    const u32 xo = WS_XS + rb3 * 65536;
#pragma unroll
    for (int s = 0; s < RING; ++s)
#pragma unroll
      for (int mt = 0; mt < 2; ++mt) xf[mt][s] = __builtin_amdgcn_raw_buffer_load_b128(ws, lane * 16 + (s & 3) * 1024, xo + mt * 32768 + (s >> 2) * 4096, AUX_SC1);
    __builtin_amdgcn_sched_barrier(0);
    f16v acc[2][3] = {};
    v2u st[2][4];
#pragma unroll
    for (int s = 0; s < 32; ++s) {
      v4u A[3];
#pragma unroll
      for (int t = 0; t < 3; ++t) { LAS char* a = w3 + (s >> 1) * 6144 + (s & 1) * 768 + t * 32; A[t] = trd2(a, a + 1536); }
#pragma unroll
      for (int mt = 0; mt < 2; ++mt) {
        const v4u B = xf[mt][s % RING];
#pragma unroll
        for (int t = 0; t < 3; ++t) acc[mt][t] = mfma32(A[t], B, acc[mt][t]);
        if (s + RING < 32) xf[mt][s % RING] = __builtin_amdgcn_raw_buffer_load_b128(ws, lane * 16 + ((s + RING) & 3) * 1024, xo + mt * 32768 + ((s + RING) >> 2) * 4096, AUX_SC1);
        if (s + RING == 32) {
#pragma unroll
          for (int i = 0; i < 4; ++i) st[mt][i] = __builtin_amdgcn_raw_buffer_load_b64(ws, (hh * 4 + i) * 256 + l31 * 8, WS_ST + (rb3 * 2 + mt) * 2048, AUX_SC1);
        }
      }
    }
    STAMP(18);
#pragma unroll
    for (int mt = 0; mt < 2; ++mt) {
      float mi[4], ms = 0.f, M2 = 0.f;
#pragma unroll
      for (int i = 0; i < 4; ++i) { mi[i] = asf(st[mt][i].x); ms += mi[i]; M2 += asf(st[mt][i].y); }
      const float m = sum32(ms) * 0.125f;
#pragma unroll
      for (int i = 0; i < 4; ++i) M2 += 64.f * (mi[i] - m) * (mi[i] - m);
      const float var = sum32(M2) * (1.f / 512.f) + 1e-5f;
      const float c = hh ? __builtin_amdgcn_sqrtf(var) : -m, chi = rbf(c);
      const float r2 = __builtin_amdgcn_rcpf(var);
      const v4u B = {pk(chi, chi), pk(c - chi, c - chi), 0u, 0u};
#pragma unroll
      for (int t = 0; t < 3; ++t) {
        const f16v a = mfma32(v4u{utp[t], utp[t], 0u, 0u}, B, acc[mt][t]);
        v4u h2;
#pragma unroll
        for (int j = 0; j < 4; ++j) h2[j] = pk(__builtin_fmaxf(a[2 * j], 0.f) * (a[8 + 2 * j] * r2), __builtin_fmaxf(a[2 * j + 1], 0.f) * (a[9 + 2 * j] * r2));
        const int u = u0 + t;
        st128<AUX_SC1>(h2, ws, (l31 >> 4) * 44032 + hh * 256 + l15 * 16, WS_H2 + (rb3 * 4 + mt * 2) * 44032 + u * 512);
      }
    }
    drain();
    set_flag(ws, WS_FH2 + rb3 * 256 + m3 * 8, lane, tag);
  } else {
    bar();
    drain();
  }
  bar();
  STAMP(19);
  for (int i = 14; i < 38; ++i) { const int r8 = i * 4 + w; dma16(rd, vo4, r8 * 8192 + ct * 128, Lb + L_W4 + r8 * 1024); }
  {
    const int r0 = 16 * (qq >> 1) + 4 * (qq & 1) + ta;
    const int sw = ((r0 >> 1) & 3) << 1;
    LAS char* a4[4];
#pragma unroll
    for (int t = 0; t < 4; ++t) a4[t] = L + L_W4 + r0 * 128 + (((2 * t + (tp >> 1)) ^ sw) * 16) + (tp & 1) * 8;
    v4u wd[56];
#pragma unroll
    for (int s = 0; s < 14; ++s)
#pragma unroll
      for (int t = 0; t < 4; ++t) wd[s * 4 + t] = trd2(a4[t] + s * 4096, a4[t] + s * 4096 + 1024);
    bar();
    for (int i = 38; i < 43; ++i) { const int r8 = i * 4 + w; dma16(rd, vo4, r8 * 8192 + ct * 128, Lb + L_W4 + (r8 - 152) * 1024); }
    STAMP(20);
    wait_flags(ws, WS_FH2 + rb * 256, 29, lane, tag);
    bar();
    STAMP(21);
    v4u gf[43];
#pragma unroll
    for (int s = 0; s < 43; ++s) gf[s] = __builtin_amdgcn_raw_buffer_load_b128(ws, lane * 16 + (s & 3) * 1024, WS_H2 + (rb * 4 + w) * 44032 + (s >> 2) * 4096, AUX_SC1);
    __builtin_amdgcn_sched_barrier(0);
    f4 acc[4] = {};
#pragma unroll
    for (int s = 0; s < 43; ++s)
#pragma unroll
      for (int t = 0; t < 4; ++t) {
        const int so = (s < 38 ? s : s - 38) * 4096;
        acc[t] = mfma16(s < 14 ? wd[s * 4 + t] : trd2(a4[t] + so, a4[t] + so + 1024), gf[s], acc[t]);
      }
    STAMP(22);
    LAS char* ob = L + L_MISC + w * 2048;
#pragma unroll
    for (int t = 0; t < 4; ++t) {
      const v2u o = {pk(x1[t].x + bflo(gbm[t].x) * acc[t].x, x1[t].y + bfhi(gbm[t].x) * acc[t].y), pk(x1[t].z + bflo(gbm[t].y) * acc[t].z, x1[t].w + bfhi(gbm[t].y) * acc[t].w)};
      *(LAS v2u*)(ob + l15 * 128 + (((2 * t + (qq >> 1)) ^ (l15 & 7)) * 16) + (qq & 1) * 8) = o;
    }
    cfence();
#pragma unroll
    for (int i = 0; i < 2; ++i) {
      const v4u o = *(LAS v4u*)(ob + i * 1024 + (lane >> 3) * 128 + (((lane & 7) ^ (lane >> 3)) * 16));
      st128<0>(o, rout, (lane >> 3) * 1024 + (lane & 7) * 16, (rb * 64 + w * 16 + i * 8) * 1024 + ct * 128);
    }
  }
  STAMP(23);
  { int mn = 1 << 30;
    for (int i = 0; i < 4; ++i) { v2u e = __builtin_amdgcn_raw_buffer_load_b64(ws, WS_STAMP + (lane + 64 * i) * 8, 0, AUX_SC1); int d = (int)(e.x - (u32)rt0_); u32 xc = e.y; if (xc == 2) mn = d < mn ? d : mn; }
    for (int o = 32; o; o >>= 1) { int t = __shfl_xor(mn, o); mn = t < mn ? t : mn; }
    mn = __builtin_amdgcn_readfirstlane(mn); if (mn > 5000 || mn < -5000) mn = 5000;
    const u64 tgt = rt0_ + 10000 + (long long)mn; while (__builtin_amdgcn_s_memrealtime() < tgt) {} }
}

extern "C" void kernel_launch(void* const* d_in, const int* in_sizes, int n_in, void* d_out, int out_size, void* d_ws, size_t ws_size, hipStream_t stream) {
  Params p;
  p.x = (const u16*)d_in[0]; p.wqkv = (const u16*)d_in[1]; p.wphi = (const u16*)d_in[2]; p.wo = (const u16*)d_in[3];
  p.wg = (const u16*)d_in[5]; p.wu = (const u16*)d_in[6]; p.wd = (const u16*)d_in[7]; p.c = (const u16*)d_in[8]; p.wmod = (const u16*)d_in[9];
  p.out = (u16*)d_out; p.ws = (char*)d_ws;
  (void)hipFuncSetAttribute((const void*)fused_block, hipFuncAttributeMaxDynamicSharedMemorySize, LDS_BYTES);
  fused_block<<<256, 256, LDS_BYTES, stream>>>(p);
}
```

```cpp
#include <hip/hip_runtime.h>
#define GAS __attribute__((address_space(1)))
#define LAS __attribute__((address_space(3)))
#define DI __device__ __forceinline__
typedef unsigned u32; typedef unsigned long long u64; typedef unsigned short u16;
typedef u32 v4u __attribute__((ext_vector_type(4)));
typedef u32 v2u __attribute__((ext_vector_type(2)));
typedef float f2 __attribute__((ext_vector_type(2)));
typedef float f4 __attribute__((ext_vector_type(4)));
typedef float f16v __attribute__((ext_vector_type(16)));
typedef short s4 __attribute__((ext_vector_type(4)));
typedef __bf16 b2 __attribute__((ext_vector_type(2)));
typedef __bf16 b8 __attribute__((ext_vector_type(8)));
typedef __amdgpu_buffer_rsrc_t rsrc_t;

#define STAMPS 0

constexpr int S_ = 2048, D_ = 512, F_ = 1376;
constexpr int WS_EPOCH = 0;
constexpr int WS_MODG = 4096;
constexpr int WS_FH = 20480;
constexpr int WS_FKV = WS_FH + 8192;
constexpr int WS_FO = WS_FKV + 2048;
constexpr int WS_FX = WS_FO + 8192;
constexpr int WS_FH2 = WS_FX + 4096;
constexpr int WS_ZP = 65536;
constexpr int WS_ST = 131072;
constexpr int WS_STAMP = 524288;
constexpr int WS_KVP = 1 << 20;
constexpr int WS_H = 2 << 20;
constexpr int WS_O = 4 << 20;
constexpr int WS_XS = 6 << 20;
constexpr int WS_H2 = 8 << 20;
constexpr int WS_END = WS_H2 + 128 * 43 * 1024;
constexpr int L_WK = 0, L_WV = 32768, L_WQ = 65536, L_PHI = 98304;
constexpr int L_WO = 0;
constexpr int L_W3 = 57344;
constexpr int L_W4 = 0;
constexpr int L_MISC = 155648;
constexpr int L_RED = L_MISC, L_MODB = L_MISC + 1024, L_KVL = L_MISC + 3072, L_ZL = L_KVL + 2048, L_ZERO = L_ZL + 128, L_UT = L_MISC + 5376;
constexpr int L_XRES = 126976;
constexpr int LDS_BYTES = 163840;
constexpr int AUX_SC1 = 16;
constexpr u32 SPIN_CAP = 1u << 18;

struct Params { const u16 *x, *wqkv, *wphi, *wo, *wg, *wu, *wd, *c, *wmod; u16* out; char* ws; };

DI float asf(u32 v) { return __builtin_bit_cast(float, v); }
DI u32 asu(float v) { return __builtin_bit_cast(u32, v); }
DI float bflo(u32 v) { return __builtin_bit_cast(float, v << 16); }
DI float bfhi(u32 v) { return __builtin_bit_cast(float, v & 0xffff0000u); }
DI u32 pk(float a, float b) { f2 t = {a, b}; return __builtin_bit_cast(u32, __builtin_convertvector(t, b2)); }
DI float rbf(float a) { return bflo(pk(a, a)); }
DI f16v mfma32(v4u a, v4u b, f16v c) { return __builtin_amdgcn_mfma_f32_32x32x16_bf16(__builtin_bit_cast(b8, a), __builtin_bit_cast(b8, b), c, 0, 0, 0); }
DI f4 mfma16(v4u a, v4u b, f4 c) { return __builtin_amdgcn_mfma_f32_16x16x32_bf16(__builtin_bit_cast(b8, a), __builtin_bit_cast(b8, b), c, 0, 0, 0); }
DI v2u trd(LAS char* p) { return __builtin_bit_cast(v2u, __builtin_amdgcn_ds_read_tr16_b64_v4i16((LAS s4*)p)); }
DI v4u trd2(LAS char* p0, LAS char* p1) { v2u a = trd(p0), b = trd(p1); return v4u{a.x, a.y, b.x, b.y}; }
DI rsrc_t mkrs(const void* p, int bytes) { return __builtin_amdgcn_make_buffer_rsrc((void*)p, 0, bytes, 0x00020000); }
template <int AUX> DI void st128(v4u v, rsrc_t rs, u32 voff, u32 soff) { __builtin_amdgcn_raw_buffer_store_b128(v, rs, voff + soff, 0, AUX); }
DI void dma16(rsrc_t rs, u32 voff, u32 soff, u32 lds) {
  asm volatile("s_mov_b32 m0, %0\n\ts_nop 0\n\tbuffer_load_dwordx4 %1, %2, %3 offen lds" ::"s"(lds), "v"(voff), "s"(rs), "s"(soff) : "memory");
}
DI void bar() { asm volatile("s_waitcnt lgkmcnt(0)\n\ts_barrier" ::: "memory"); }
DI void drain() { asm volatile("s_waitcnt vmcnt(0)" ::: "memory"); }
DI void cfence() { asm volatile("" ::: "memory"); }
template <int CTRL> DI float dppf(float v) { return __builtin_bit_cast(float, __builtin_amdgcn_update_dpp(0, __builtin_bit_cast(int, v), CTRL, 0xf, 0xf, true)); }
DI float sum16(float v) { auto r = __builtin_amdgcn_permlane16_swap(__builtin_bit_cast(u32, v), __builtin_bit_cast(u32, v), false, false); return asf(r[0]) + asf(r[1]); }
DI float sum32(float v) { auto r = __builtin_amdgcn_permlane32_swap(__builtin_bit_cast(u32, v), __builtin_bit_cast(u32, v), false, false); return asf(r[0]) + asf(r[1]); }
constexpr int ROR8 = 0x128, ROR4 = 0x124, ROR2 = 0x122, ROR1 = 0x121;
DI void wait_flags(rsrc_t ws, u32 off, int n, int lane, v2u tag) {
  const u32 vo = off + (lane < n ? lane : 0) * 8;
  for (u32 sp = 0; sp < SPIN_CAP; ++sp) {
    v2u f = __builtin_amdgcn_raw_buffer_load_b64(ws, vo, 0, AUX_SC1);
    cfence();
    if (__all(f.x == tag.x && f.y == tag.y)) break;
  }
  cfence();
}
DI void set_flag(rsrc_t ws, u32 off, int lane, v2u tag) { if (lane == 0) __builtin_amdgcn_raw_buffer_store_b64(tag, ws, off, 0, AUX_SC1); }
DI v2u wait_gran(rsrc_t ws, u32 voff, u32 tag) {
  v4u g;
  for (u32 sp = 0; sp < SPIN_CAP; ++sp) {
    g = __builtin_amdgcn_raw_buffer_load_b128(ws, voff, 0, AUX_SC1);
    cfence();
    if (__all(g.y == tag && g.w == tag)) break;
  }
  return v2u{g.x, g.z};
}
#define STAMP(i) do {} while (0)

DI void feature_map(const f16v& acc, v4u wp0, v4u wp1, u32 (&ph)[16]) {
  v4u b0 = {pk(acc[0], acc[1]), pk(acc[2], acc[3]), pk(acc[4], acc[5]), pk(acc[6], acc[7])};
  v4u b1 = {pk(acc[8], acc[9]), pk(acc[10], acc[11]), pk(acc[12], acc[13]), pk(acc[14], acc[15])};
  f16v z = {};
  f16v lg = mfma32(wp0, b0, z);
  lg = mfma32(wp1, b1, lg);
  float mx = lg[0], mn = lg[0];
#pragma unroll
  for (int i = 1; i < 16; ++i) { mx = __builtin_fmaxf(mx, lg[i]); mn = __builtin_fminf(mn, lg[i]); }
  const float L2E = 1.44269504088896f;
  const float cp = -mx * L2E, cn = mn * L2E;
  float ep[16], en[16];
#pragma unroll
  for (int i = 0; i < 16; ++i) { ep[i] = __builtin_amdgcn_exp2f(__builtin_fmaf(lg[i], L2E, cp)); en[i] = __builtin_amdgcn_exp2f(__builtin_fmaf(lg[i], -L2E, cn)); }
  f2 sp = {ep[0], ep[1]}, sn = {en[0], en[1]};
#pragma unroll
  for (int i = 1; i < 8; ++i) { sp += f2{ep[2 * i], ep[2 * i + 1]}; sn += f2{en[2 * i], en[2 * i + 1]}; }
  const float rp = __builtin_amdgcn_rcpf(sp.x + sp.y), rn = __builtin_amdgcn_rcpf(sn.x + sn.y);
#pragma unroll
  for (int i = 0; i < 8; ++i) { ph[i] = pk(ep[2 * i] * rp, ep[2 * i + 1] * rp); ph[8 + i] = pk(en[2 * i] * rn, en[2 * i + 1] * rn); }
}

__global__ void __launch_bounds__(256, 1) fused_block(Params p) {
  extern __shared__ __attribute__((aligned(16))) char smem_[];
  LAS char* L = (LAS char*)smem_;
  const u32 Lb = (u32)(size_t)L;
  const int tid = threadIdx.x, lane = tid & 63, b = blockIdx.x;
  const int w = __builtin_amdgcn_readfirstlane(tid >> 6);
  const int xg = b & 7, jj = b >> 3;
  const int pan = 4 * (xg >> 1) + (jj >> 3), mem = 8 * (xg & 1) + (jj & 7);
  const int rb = 4 * xg + (jj >> 3), ct = jj & 7;
  const int sp3 = 4 * (xg >> 2) + (jj >> 3), m3 = 8 * (xg & 3) + (jj & 7);
  const int u0 = m3 * 3 < 83 ? m3 * 3 : 83;
  const bool has3 = m3 < 29;
  STAMP(0);
  const rsrc_t ws = mkrs(p.ws, WS_END);
  const rsrc_t rx = mkrs(p.x, S_ * D_ * 2), rqkv = mkrs(p.wqkv, D_ * 3 * D_ * 2), rwo = mkrs(p.wo, D_ * D_ * 2);
  const rsrc_t rg = mkrs(p.wg, D_ * F_ * 2), ru = mkrs(p.wu, D_ * F_ * 2), rd = mkrs(p.wd, F_ * D_ * 2);
  const rsrc_t rmod = mkrs(p.wmod, D_ * 6 * D_ * 2), rc = mkrs(p.c, D_ * 2), rphi = mkrs(p.wphi, 32 * 256 * 2), rout = mkrs(p.out, S_ * D_ * 2);
  const int l31 = lane & 31, hh = lane >> 5, l15 = lane & 15, qq = lane >> 4;
  const int ta = (lane >> 2) & 3, tp = lane & 3;

  const v2u ep = __builtin_amdgcn_raw_buffer_load_b64(ws, WS_EPOCH, 0, AUX_SC1);
  const bool hasmod = jj < 24;
  const int mcol = jj < 8 ? (2 * xg + (jj >> 2)) * 64 + (jj & 3) * 16 : 1024 + (4 * xg + ((jj - 8) >> 2)) * 64 + (jj & 3) * 16;
  v4u wm[4]; u16 cc[4];
  if (hasmod) {
#pragma unroll
    for (int i = 0; i < 4; ++i) cc[i] = __builtin_amdgcn_raw_buffer_load_b16(rc, ((tid >> 1) + 128 * i) * 2, 0, 0);
#pragma unroll
    for (int i = 0; i < 4; ++i) wm[i] = __builtin_amdgcn_raw_buffer_load_b128(rmod, ((tid >> 1) + 128 * i) * 6144 + (tid & 1) * 16, mcol * 2, 0);
  }
  v4u xr[2];
#pragma unroll
  for (int i = 0; i < 2; ++i) xr[i] = __builtin_amdgcn_raw_buffer_load_b128(rx, (lane & 7) * 1024 + ((lane >> 3) + 8 * i) * 16, b * 8192 + w * 256, 0);
  u16 wp[8];
  {
    const int hd = (l31 >> 2) & 1, f = 4 * (l31 >> 3) + (l31 & 3);
#pragma unroll
    for (int j = 0; j < 8; ++j) wp[j] = __builtin_amdgcn_raw_buffer_load_b16(rphi, (hd * 256 + (8 * (j >> 2) + 4 * hh + (j & 3)) * 16 + f) * 2, mem * 1024, 0);
  }
#pragma unroll
  for (int i = 0; i < 2; ++i) dma16(rx, (lane >> 3) * 1024 + (lane & 7) * 16, (rb * 64 + w * 16 + i * 8) * 1024 + ct * 128, Lb + L_XRES + w * 2048 + i * 1024);
  {
    const u32 vo = (lane >> 2) * 3072 + (lane & 3) * 16;
    for (int i = 0; i < 8; ++i) {
      const int r16 = i * 4 + w;
      dma16(rqkv, vo, r16 * 16 * 3072 + 1024 + mem * 64, Lb + L_WK + r16 * 1024);
      dma16(rqkv, vo, r16 * 16 * 3072 + 2048 + mem * 64, Lb + L_WV + r16 * 1024);
    }
    for (int i = 0; i < 8; ++i) { const int r16 = i * 4 + w; dma16(rqkv, vo, r16 * 16 * 3072 + mem * 64, Lb + L_WQ + r16 * 1024); }
  }
  const u64 tag64 = (((u64)ep.y << 32) | ep.x) + 0x9E3779B97F4A7C15ull;
  const v2u tag = {(u32)__builtin_amdgcn_readfirstlane((u32)tag64), (u32)__builtin_amdgcn_readfirstlane((u32)(tag64 >> 32))};
  if (tid < 16) *(LAS u32*)(L + L_ZERO + tid * 4) = 0u;
  STAMP(1);
  if (hasmod) {
    float a[8] = {};
#pragma unroll
    for (int i = 0; i < 4; ++i) {
      const float cv = bflo(cc[i]);
      const float s = rbf(cv * __builtin_amdgcn_rcpf(1.f + __builtin_amdgcn_exp2f(-1.44269504088896f * cv)));
#pragma unroll
      for (int j = 0; j < 4; ++j) { a[2 * j] += s * bflo(wm[i][j]); a[2 * j + 1] += s * bfhi(wm[i][j]); }
    }
#pragma unroll
    for (int j = 0; j < 8; ++j) { float v = a[j]; v += dppf<ROR2>(v); v += dppf<ROR4>(v); v += dppf<ROR8>(v); a[j] = v; }
    if (l15 < 2) { *(LAS f4*)(L + L_RED + (w * 4 + qq) * 64 + l15 * 32) = f4{a[0], a[1], a[2], a[3]}; *(LAS f4*)(L + L_RED + (w * 4 + qq) * 64 + l15 * 32 + 16) = f4{a[4], a[5], a[6], a[7]}; }
  }
  float xv[16];
#pragma unroll
  for (int i = 0; i < 2; ++i)
#pragma unroll
    for (int j = 0; j < 4; ++j) { xv[8 * i + 2 * j] = bflo(xr[i][j]); xv[8 * i + 2 * j + 1] = bfhi(xr[i][j]); }
  float s1 = 0.f;
#pragma unroll
  for (int i = 0; i < 16; ++i) s1 += xv[i];
  s1 += dppf<ROR8>(s1); s1 = sum16(s1); s1 = sum32(s1);
  const float ml = s1 * (1.f / 128.f);
  float s2 = 0.f;
#pragma unroll
  for (int i = 0; i < 16; ++i) s2 += (xv[i] - ml) * (xv[i] - ml);
  s2 += dppf<ROR8>(s2); s2 = sum16(s2); s2 = sum32(s2);
  if (lane < 8) *(LAS f2*)(L + L_UT + lane * 32 + w * 8) = f2{ml, s2};
  bar();
  if (hasmod && w == 0) {
    f2 v = *(LAS f2*)(L + L_RED + (lane >> 3) * 128 + (lane & 7) * 8);
    v += *(LAS f2*)(L + L_RED + (lane >> 3) * 128 + 64 + (lane & 7) * 8);
    v.x += dppf<ROR8>(v.x); v.y += dppf<ROR8>(v.y);
    v.x = sum32(sum16(v.x)); v.y = sum32(sum16(v.y));
    if (lane < 8) __builtin_amdgcn_raw_buffer_store_b64(v2u{pk(v.x, v.y), tag.x}, ws, WS_MODG + lane * 8, mcol * 4, AUX_SC1);
  }
  STAMP(2);
  {
    const f4 t0 = *(LAS f4*)(L + L_UT + (lane & 7) * 32), t1 = *(LAS f4*)(L + L_UT + (lane & 7) * 32 + 16);
    const float m = (t0.x + t0.z + t1.x + t1.z) * 0.25f;
    const float M2 = t0.y + t0.w + t1.y + t1.w + 128.f * ((t0.x - m) * (t0.x - m) + (t0.z - m) * (t0.z - m) + (t1.x - m) * (t1.x - m) + (t1.z - m) * (t1.z - m));
    const float r = __builtin_amdgcn_rsqf(M2 * (1.f / 512.f) + 1e-5f);
#pragma unroll
    for (int i = 0; i < 16; ++i) xv[i] = (xv[i] - m) * r;
  }
  {
    const v2u g = wait_gran(ws, WS_MODG + hh * 2048 + w * 512 + l31 * 16, tag.x);
    *(LAS v2u*)(L + L_MODB + w * 512 + lane * 8) = g;
    cfence();
    STAMP(3);
#pragma unroll
    for (int i = 0; i < 2; ++i) {
      const v4u sa = *(LAS v4u*)(L + L_MODB + w * 512 + ((lane >> 3) + 8 * i) * 16);
      const v4u ca = *(LAS v4u*)(L + L_MODB + w * 512 + 256 + ((lane >> 3) + 8 * i) * 16);
      v4u h;
#pragma unroll
      for (int j = 0; j < 4; ++j) {
        const u32 g1 = pk(1.f + bflo(ca[j]), 1.f + bfhi(ca[j]));
        h[j] = pk(xv[8 * i + 2 * j] * bflo(g1) + bflo(sa[j]), xv[8 * i + 2 * j + 1] * bfhi(g1) + bfhi(sa[j]));
      }
      st128<AUX_SC1>(h, ws, ((lane >> 3) + 8 * i) * 512 + (lane & 7) * 16, WS_H + (b >> 2) * 32768 + w * 8192 + (b & 3) * 128);
    }
  }
  drain();
  bar();
  if (w == 0) set_flag(ws, WS_FH + (b >> 2) * 128 + (b & 3) * 8, lane, tag);
  STAMP(4);
  v2u xres[4];
#pragma unroll
  for (int t = 0; t < 4; ++t) xres[t] = *(LAS v2u*)(L + L_XRES + w * 2048 + l15 * 128 + t * 32 + qq * 8);
  v4u wp0, wp1;
  {
    const v4u v = {wp[0] | ((u32)wp[1] << 16), wp[2] | ((u32)wp[3] << 16), wp[4] | ((u32)wp[5] << 16), wp[6] | ((u32)wp[7] << 16)};
    const u32 m1 = ((l31 >> 2) & 1) ? 0xffffffffu : 0u;
    wp1 = v & m1; wp0 = v & ~m1;
  }

  const int t32 = pan * 4 + w;
  wait_flags(ws, WS_FH + t32 * 128, 4, lane, tag);
  STAMP(5);
  v4u hf[32];
#pragma unroll
  for (int s = 0; s < 32; ++s) hf[s] = __builtin_amdgcn_raw_buffer_load_b128(ws, lane * 16 + (s & 3) * 1024, WS_H + t32 * 32768 + (s >> 2) * 4096, AUX_SC1);
  __builtin_amdgcn_sched_barrier(0);
  LAS char* wa = L + (8 * hh + ta) * 64 + (qq & 1) * 32 + tp * 8;
  f16v ak = {}, av = {};
#pragma unroll
  for (int s = 0; s < 32; ++s) {
    ak = mfma32(trd2(wa + L_WK + s * 1024, wa + L_WK + s * 1024 + 256), hf[s], ak);
    av = mfma32(trd2(wa + L_WV + s * 1024, wa + L_WV + s * 1024 + 256), hf[s], av);
  }
  STAMP(6);
  {
    u32 ph[16];
    feature_map(ak, wp0, wp1, ph);
    LAS char* row = L + L_PHI + (w * 32 + l31) * 224;
#pragma unroll
    for (int i = 0; i < 4; ++i) *(LAS v4u*)(row + hh * 64 + i * 16) = v4u{ph[4 * i], ph[4 * i + 1], ph[4 * i + 2], ph[4 * i + 3]};
#pragma unroll
    for (int a = 0; a < 4; ++a) *(LAS v2u*)(row + 128 + a * 16 + hh * 8) = v2u{pk(av[4 * a], av[4 * a + 1]), pk(av[4 * a + 2], av[4 * a + 3])};
  }
  bar();
  STAMP(7);
  {
    LAS char* ra = L + L_PHI + (4 * qq + ta) * 224 + tp * 8;
    f4 kv = {}, zz = {};
    const v4u ones = {0x3f803f80u, 0x3f803f80u, 0x3f803f80u, 0x3f803f80u};
#pragma unroll
    for (int s = 0; s < 4; ++s) {
      const v4u A = trd2(ra + w * 32 + s * 32 * 224, ra + w * 32 + (s * 32 + 16) * 224);
      const v4u B = trd2(ra + 128 + (w >> 1) * 32 + s * 32 * 224, ra + 128 + (w >> 1) * 32 + (s * 32 + 16) * 224);
      kv = mfma16(A, B, kv); zz = mfma16(A, ones, zz);
    }
    st128<AUX_SC1>(__builtin_bit_cast(v4u, kv), ws, lane * 16, WS_KVP + (mem * 16 + pan) * 4096 + w * 1024);
    if (l15 == 0) st128<AUX_SC1>(__builtin_bit_cast(v4u, zz), ws, qq * 16, WS_ZP + (mem * 16 + pan) * 256 + w * 64);
  }
  drain();
  bar();
  if (w == 0) set_flag(ws, WS_FKV + mem * 128 + pan * 8, lane, tag);
  STAMP(8);
  const u32 vo128 = (lane >> 3) * 1024;
  {
    for (int i = 0; i < 16; ++i) {
      const int r8 = i * 4 + w;
      const u32 sw = (((lane >> 4) & 1) << 1) | ((w & 1) << 2);
      dma16(rwo, vo128 + ((lane & 7) ^ sw) * 16, r8 * 8192 + ct * 128, Lb + L_WO + r8 * 1024);
    }
  }
  u32 pq[16];
  {
    f16v aq = {};
#pragma unroll
    for (int s = 0; s < 32; ++s) aq = mfma32(trd2(wa + L_WQ + s * 1024, wa + L_WQ + s * 1024 + 256), hf[s], aq);
    feature_map(aq, wp0, wp1, pq);
  }
  STAMP(9);
  wait_flags(ws, WS_FKV + mem * 128, 16, lane, tag);
  STAMP(10);
  {
    f4 zq = __builtin_bit_cast(f4, __builtin_amdgcn_raw_buffer_load_b128(ws, (lane >> 2) * 256 + (lane & 3) * 16, WS_ZP + mem * 4096 + w * 64, AUX_SC1));
    v4u kp[16];
#pragma unroll
    for (int i = 0; i < 16; ++i) kp[i] = __builtin_amdgcn_raw_buffer_load_b128(ws, lane * 16, WS_KVP + (mem * 16 + i) * 4096 + w * 1024, AUX_SC1);
    __builtin_amdgcn_sched_barrier(0);
#pragma unroll
    for (int j = 0; j < 4; ++j) { float v = zq[j]; v += dppf<ROR4>(v); v += dppf<ROR8>(v); zq[j] = sum32(sum16(v)); }
    if (lane < 4) *(LAS v2u*)(L + L_ZL + w * 32 + lane * 8) = v2u{pk(zq.x, zq.y), pk(zq.z, zq.w)};
    f4 s = __builtin_bit_cast(f4, kp[0]);
#pragma unroll
    for (int i = 1; i < 16; ++i) s += __builtin_bit_cast(f4, kp[i]);
    *(LAS v2u*)(L + L_KVL + ((w >> 1) * 16 + l15) * 64 + (w & 1) * 32 + qq * 8) = v2u{pk(s.x, s.y), pk(s.z, s.w)};
  }
  bar();
  STAMP(11);
  {
    const bool mine = ((l31 >> 2) & 1) == hh;
    LAS char* pa = L + (mine ? L_KVL + (hh * 16 + 4 * (l31 >> 3) + (l31 & 3)) * 64 : L_ZERO);
    LAS char* pz = L + (mine ? L_ZL + hh * 64 : L_ZERO);
    f16v num = {}, den = {};
#pragma unroll
    for (int u = 0; u < 4; ++u) {
      const v4u B = {pq[4 * u], pq[4 * u + 1], pq[4 * u + 2], pq[4 * u + 3]};
      den = mfma32(*(LAS v4u*)(pz + u * 16), B, den);
      num = mfma32(*(LAS v4u*)(pa + u * 16), B, num);
    }
    const float ri = __builtin_amdgcn_rcpf(den[0] + 1e-6f);
#pragma unroll
    for (int e = 0; e < 2; ++e) {
      v4u o;
#pragma unroll
      for (int j = 0; j < 4; ++j) o[j] = pk(num[8 * e + 2 * j] * ri, num[8 * e + 2 * j + 1] * ri);
      st128<AUX_SC1>(o, ws, (l31 >> 4) * 16384 + (2 * hh + e) * 256 + l15 * 16, WS_O + t32 * 32768 + mem * 1024);
    }
  }
  drain();
  set_flag(ws, WS_FO + t32 * 128 + mem * 8, lane, tag);
  STAMP(12);
  u32 v3[3], v3u[3];
#pragma unroll
  for (int i = 0; i < 3; ++i) { const int id = i * 64 + lane, r = id / 6, c = id - 6 * r; v3[i] = r * 2752 + c * 16; v3u[i] = (r ^ 4) * 2752 + c * 16; }
  if (has3) {
    for (int g = 2 + (w >> 1); g < 16; g += 2) {
#pragma unroll
      for (int i = 0; i < 3; ++i) {
        if (w & 1) dma16(ru, v3u[i], g * 32 * 2752 + u0 * 32, Lb + L_W3 + g * 6144 + 3072 + i * 1024);
        else dma16(rg, v3[i], g * 32 * 2752 + u0 * 32, Lb + L_W3 + g * 6144 + i * 1024);
      }
    }
  }
  v2u gam[4], cbm[4], gbm[4];
#pragma unroll
  for (int t = 0; t < 4; ++t) {
    gam[t] = wait_gran(ws, WS_MODG + (1024 + ct * 64 + t * 16 + qq * 4) * 4, tag.x);
    cbm[t] = wait_gran(ws, WS_MODG + (2048 + ct * 64 + t * 16 + qq * 4) * 4, tag.x);
    gbm[t] = wait_gran(ws, WS_MODG + (2560 + ct * 64 + t * 16 + qq * 4) * 4, tag.x);
  }
  {
    v2u g = wait_gran(ws, WS_MODG + (1536 + tid * 4) * 4, tag.x);
    if (w >= 2) g = v2u{pk(1.f + bflo(g.x), 1.f + bfhi(g.x)), pk(1.f + bflo(g.y), 1.f + bfhi(g.y))};
    const int c4 = tid & 127;
    *(LAS v2u*)(L + L_MODB + (w >= 2 ? 0 : 1024) + (c4 >> 3) * 64 + (c4 & 3) * 16 + ((c4 >> 2) & 1) * 8) = g;
  }
  v4u wf[64];
  {
    const int sw = (((ta >> 1) & 1) << 1) | ((qq & 1) << 2);
#pragma unroll
    for (int t = 0; t < 4; ++t) {
      LAS char* a = L + L_WO + (8 * qq + ta) * 128 + (((2 * t + (tp >> 1)) ^ sw) * 16) + (tp & 1) * 8;
#pragma unroll
      for (int s = 0; s < 16; ++s) wf[s * 4 + t] = trd2(a + s * 4096, a + s * 4096 + 512);
    }
  }
  bar();
  STAMP(13);
  wait_flags(ws, WS_FO + (rb * 2 + (w >> 1)) * 128, 16, lane, tag);
  if (b == 0 && w == 0 && lane == 0) __builtin_amdgcn_raw_buffer_store_b64(tag, ws, WS_EPOCH, 0, AUX_SC1);
  STAMP(14);
  f4 x1[4];
  {
    v4u of[16];
#pragma unroll
    for (int s = 0; s < 16; ++s) of[s] = __builtin_amdgcn_raw_buffer_load_b128(ws, lane * 16 + (s & 3) * 1024, WS_O + (rb * 4 + w) * 16384 + (s >> 2) * 4096, AUX_SC1);
    __builtin_amdgcn_sched_barrier(0);
    f4 acc[4] = {};
#pragma unroll
    for (int s = 0; s < 16; ++s)
#pragma unroll
      for (int t = 0; t < 4; ++t) acc[t] = mfma16(wf[s * 4 + t], of[s], acc[t]);
    float sm = 0.f;
#pragma unroll
    for (int t = 0; t < 4; ++t) {
      x1[t] = f4{bflo(xres[t].x) + bflo(gam[t].x) * acc[t].x, bfhi(xres[t].x) + bfhi(gam[t].x) * acc[t].y, bflo(xres[t].y) + bflo(gam[t].y) * acc[t].z, bfhi(xres[t].y) + bfhi(gam[t].y) * acc[t].w};
      sm += (x1[t].x + x1[t].y) + (x1[t].z + x1[t].w);
    }
#pragma unroll
    for (int T = 0; T < 2; ++T) {
      v4u xs;
#pragma unroll
      for (int e = 0; e < 2; ++e) {
        const int t = 2 * T + e;
        const u32 g0 = pk(1.f + bflo(cbm[t].x), 1.f + bfhi(cbm[t].x)), g1 = pk(1.f + bflo(cbm[t].y), 1.f + bfhi(cbm[t].y));
        xs[2 * e] = pk(x1[t].x * bflo(g0), x1[t].y * bfhi(g0));
        xs[2 * e + 1] = pk(x1[t].z * bflo(g1), x1[t].w * bfhi(g1));
      }
      st128<AUX_SC1>(xs, ws, qq * 512 + l15 * 16 + T * 2048, WS_XS + (rb * 2 + (w >> 1)) * 32768 + ct * 4096 + (w & 1) * 256);
    }
    const float m = sum32(sum16(sm)) * (1.f / 64.f);
    float q2 = 0.f;
#pragma unroll
    for (int t = 0; t < 4; ++t)
#pragma unroll
      for (int r = 0; r < 4; ++r) q2 += (x1[t][r] - m) * (x1[t][r] - m);
    q2 = sum32(sum16(q2));
    if (lane < 16) __builtin_amdgcn_raw_buffer_store_b64(v2u{__builtin_bit_cast(u32, m), __builtin_bit_cast(u32, q2)}, ws, lane * 8, WS_ST + (rb * 2 + (w >> 1)) * 2048 + ct * 256 + (w & 1) * 128, AUX_SC1);
  }
  drain();
  bar();
  if (w == 0) set_flag(ws, WS_FX + rb * 128 + ct * 8, lane, tag);
  STAMP(15);
  const u32 vo4 = vo128 + ((lane & 7) ^ (((lane >> 4) & 3) << 1)) * 16;
  if (has3) {
    const int g = w >> 1;
#pragma unroll
    for (int i = 0; i < 3; ++i) {
      if (w & 1) dma16(ru, v3u[i], g * 32 * 2752 + u0 * 32, Lb + L_W3 + g * 6144 + 3072 + i * 1024);
      else dma16(rg, v3[i], g * 32 * 2752 + u0 * 32, Lb + L_W3 + g * 6144 + i * 1024);
    }
    drain();
  }
  for (int i = 0; i < 14; ++i) { const int r8 = i * 4 + w; dma16(rd, vo4, r8 * 8192 + ct * 128, Lb + L_W4 + r8 * 1024); }
  bar();
  if (has3) {
    LAS char* w3 = L + L_W3 + (qq & 1) * 3072 + ((4 * hh + ta) ^ ((qq & 1) * 4)) * 96 + tp * 8;
    {
      f16v ut[3] = {};
      LAS char* mb = L + L_MODB + ((l31 >> 2) & 1) * 1024 + hh * 16 + w * 256;
      LAS char* wk = w3 + w * 4 * 6144;
#pragma unroll
      for (int s = 0; s < 8; ++s) {
        const v4u A = *(LAS v4u*)(mb + s * 32);
#pragma unroll
        for (int t = 0; t < 3; ++t) { LAS char* a = wk + (s >> 1) * 6144 + (s & 1) * 768 + t * 32; ut[t] = mfma32(A, trd2(a, a + 1536), ut[t]); }
      }
#pragma unroll
      for (int t = 0; t < 3; ++t) *(LAS float*)(L + L_UT - 2304 + t * 1024 + lane * 16 + w * 4) = ut[t][0];
    }
    bar();
    u32 utp[3];
#pragma unroll
    for (int t = 0; t < 3; ++t) {
      const f4 v = *(LAS f4*)(L + L_UT - 2304 + t * 1024 + lane * 16);
      const float s = (v.x + v.y) + (v.z + v.w), hi = rbf(s);
      utp[t] = pk(hi, s - hi);
    }
    STAMP(16);
    const int rb3 = sp3 * 4 + w;
    wait_flags(ws, WS_FX + rb3 * 128, 8, lane, tag);
    STAMP(17);
    constexpr int RING = 12;
    v4u xf[2][RING];
    const u32 xo = WS_XS + rb3 * 65536;
#pragma unroll
    for (int s = 0; s < RING; ++s)
#pragma unroll
      for (int mt = 0; mt < 2; ++mt) xf[mt][s] = __builtin_amdgcn_raw_buffer_load_b128(ws, lane * 16 + (s & 3) * 1024, xo + mt * 32768 + (s >> 2) * 4096, AUX_SC1);
    __builtin_amdgcn_sched_barrier(0);
    f16v acc[2][3] = {};
    v2u st[2][4];
#pragma unroll
    for (int s = 0; s < 32; ++s) {
      v4u A[3];
#pragma unroll
      for (int t = 0; t < 3; ++t) { LAS char* a = w3 + (s >> 1) * 6144 + (s & 1) * 768 + t * 32; A[t] = trd2(a, a + 1536); }
#pragma unroll
      for (int mt = 0; mt < 2; ++mt) {
        const v4u B = xf[mt][s % RING];
#pragma unroll
        for (int t = 0; t < 3; ++t) acc[mt][t] = mfma32(A[t], B, acc[mt][t]);
        if (s + RING < 32) xf[mt][s % RING] = __builtin_amdgcn_raw_buffer_load_b128(ws, lane * 16 + ((s + RING) & 3) * 1024, xo + mt * 32768 + ((s + RING) >> 2) * 4096, AUX_SC1);
        if (s + RING == 32) {
#pragma unroll
          for (int i = 0; i < 4; ++i) st[mt][i] = __builtin_amdgcn_raw_buffer_load_b64(ws, (hh * 4 + i) * 256 + l31 * 8, WS_ST + (rb3 * 2 + mt) * 2048, AUX_SC1);
        }
      }
    }
    STAMP(18);
#pragma unroll
    for (int mt = 0; mt < 2; ++mt) {
      float mi[4], ms = 0.f, M2 = 0.f;
#pragma unroll
      for (int i = 0; i < 4; ++i) { mi[i] = asf(st[mt][i].x); ms += mi[i]; M2 += asf(st[mt][i].y); }
      const float m = sum32(ms) * 0.125f;
#pragma unroll
      for (int i = 0; i < 4; ++i) M2 += 64.f * (mi[i] - m) * (mi[i] - m);
      const float var = sum32(M2) * (1.f / 512.f) + 1e-5f;
      const float c = hh ? __builtin_amdgcn_sqrtf(var) : -m, chi = rbf(c);
      const float r2 = __builtin_amdgcn_rcpf(var);
      const v4u B = {pk(chi, chi), pk(c - chi, c - chi), 0u, 0u};
#pragma unroll
      for (int t = 0; t < 3; ++t) {
        const f16v a = mfma32(v4u{utp[t], utp[t], 0u, 0u}, B, acc[mt][t]);
        v4u h2;
#pragma unroll
        for (int j = 0; j < 4; ++j) h2[j] = pk(__builtin_fmaxf(a[2 * j], 0.f) * (a[8 + 2 * j] * r2), __builtin_fmaxf(a[2 * j + 1], 0.f) * (a[9 + 2 * j] * r2));
        const int u = u0 + t;
        st128<AUX_SC1>(h2, ws, (l31 >> 4) * 44032 + hh * 256 + l15 * 16, WS_H2 + (rb3 * 4 + mt * 2) * 44032 + u * 512);
      }
    }
    drain();
    set_flag(ws, WS_FH2 + rb3 * 256 + m3 * 8, lane, tag);
  } else {
    bar();
    drain();
  }
  bar();
  STAMP(19);
  for (int i = 14; i < 38; ++i) { const int r8 = i * 4 + w; dma16(rd, vo4, r8 * 8192 + ct * 128, Lb + L_W4 + r8 * 1024); }
  {
    const int r0 = 16 * (qq >> 1) + 4 * (qq & 1) + ta;
    const int sw = ((r0 >> 1) & 3) << 1;
    LAS char* a4[4];
#pragma unroll
    for (int t = 0; t < 4; ++t) a4[t] = L + L_W4 + r0 * 128 + (((2 * t + (tp >> 1)) ^ sw) * 16) + (tp & 1) * 8;
    v4u wd[56];
#pragma unroll
    for (int s = 0; s < 14; ++s)
#pragma unroll
      for (int t = 0; t < 4; ++t) wd[s * 4 + t] = trd2(a4[t] + s * 4096, a4[t] + s * 4096 + 1024);
    bar();
    for (int i = 38; i < 43; ++i) { const int r8 = i * 4 + w; dma16(rd, vo4, r8 * 8192 + ct * 128, Lb + L_W4 + (r8 - 152) * 1024); }
    STAMP(20);
    wait_flags(ws, WS_FH2 + rb * 256, 29, lane, tag);
    bar();
    STAMP(21);
    v4u gf[43];
#pragma unroll
    for (int s = 0; s < 43; ++s) gf[s] = __builtin_amdgcn_raw_buffer_load_b128(ws, lane * 16 + (s & 3) * 1024, WS_H2 + (rb * 4 + w) * 44032 + (s >> 2) * 4096, AUX_SC1);
    __builtin_amdgcn_sched_barrier(0);
    f4 acc[4] = {};
#pragma unroll
    for (int s = 0; s < 43; ++s)
#pragma unroll
      for (int t = 0; t < 4; ++t) {
        const int so = (s < 38 ? s : s - 38) * 4096;
        acc[t] = mfma16(s < 14 ? wd[s * 4 + t] : trd2(a4[t] + so, a4[t] + so + 1024), gf[s], acc[t]);
      }
    STAMP(22);
    LAS char* ob = L + L_MISC + w * 2048;
#pragma unroll
    for (int t = 0; t < 4; ++t) {
      const v2u o = {pk(x1[t].x + bflo(gbm[t].x) * acc[t].x, x1[t].y + bfhi(gbm[t].x) * acc[t].y), pk(x1[t].z + bflo(gbm[t].y) * acc[t].z, x1[t].w + bfhi(gbm[t].y) * acc[t].w)};
      *(LAS v2u*)(ob + l15 * 128 + (((2 * t + (qq >> 1)) ^ (l15 & 7)) * 16) + (qq & 1) * 8) = o;
    }
    cfence();
#pragma unroll
    for (int i = 0; i < 2; ++i) {
      const v4u o = *(LAS v4u*)(ob + i * 1024 + (lane >> 3) * 128 + (((lane & 7) ^ (lane >> 3)) * 16));
      st128<0>(o, rout, (lane >> 3) * 1024 + (lane & 7) * 16, (rb * 64 + w * 16 + i * 8) * 1024 + ct * 128);
    }
  }
  STAMP(23);
}

__global__ void tinyA(char* ws) { if (blockIdx.x == 0 && threadIdx.x == 0) *(GAS u64*)(ws + WS_STAMP) = __builtin_amdgcn_s_memrealtime(); }
__global__ void tinyZ(char* ws) { const u64 now = __builtin_amdgcn_s_memrealtime(); u64 t = *(GAS u64*)(ws + WS_STAMP); if (now - t > 10000) t = now; const u64 tgt = t + 10000; while (__builtin_amdgcn_s_memrealtime() < tgt) {} }
extern "C" void kernel_launch(void* const* d_in, const int* in_sizes, int n_in, void* d_out, int out_size, void* d_ws, size_t ws_size, hipStream_t stream) {
  Params p;
  p.x = (const u16*)d_in[0]; p.wqkv = (const u16*)d_in[1]; p.wphi = (const u16*)d_in[2]; p.wo = (const u16*)d_in[3];
  p.wg = (const u16*)d_in[5]; p.wu = (const u16*)d_in[6]; p.wd = (const u16*)d_in[7]; p.c = (const u16*)d_in[8]; p.wmod = (const u16*)d_in[9];
  p.out = (u16*)d_out; p.ws = (char*)d_ws;
  (void)hipFuncSetAttribute((const void*)fused_block, hipFuncAttributeMaxDynamicSharedMemorySize, LDS_BYTES);
  tinyA<<<1, 64, 0, stream>>>(p.ws);
  fused_block<<<256, 256, LDS_BYTES, stream>>>(p);
  tinyZ<<<1, 64, 0, stream>>>(p.ws);
}
```

```cpp
#include <hip/hip_runtime.h>
#define GAS __attribute__((address_space(1)))
#define LAS __attribute__((address_space(3)))
#define DI __device__ __forceinline__
typedef unsigned u32; typedef unsigned long long u64; typedef unsigned short u16;
typedef u32 v4u __attribute__((ext_vector_type(4)));
typedef u32 v2u __attribute__((ext_vector_type(2)));
typedef float f2 __attribute__((ext_vector_type(2)));
typedef float f4 __attribute__((ext_vector_type(4)));
typedef float f16v __attribute__((ext_vector_type(16)));
typedef short s4 __attribute__((ext_vector_type(4)));
typedef __bf16 b2 __attribute__((ext_vector_type(2)));
typedef __bf16 b8 __attribute__((ext_vector_type(8)));
typedef __amdgpu_buffer_rsrc_t rsrc_t;

#define STAMPS 0

constexpr int S_ = 2048, D_ = 512, F_ = 1376;
constexpr int WS_EPOCH = 0;
constexpr int WS_MODG = 4096;
constexpr int WS_FH = 20480;
constexpr int WS_FKV = WS_FH + 8192;
constexpr int WS_FO = WS_FKV + 2048;
constexpr int WS_FX = WS_FO + 8192;
constexpr int WS_FH2 = WS_FX + 4096;
constexpr int WS_ZP = 65536;
constexpr int WS_ST = 131072;
constexpr int WS_STAMP = 524288;
constexpr int WS_KVP = 1 << 20;
constexpr int WS_H = 2 << 20;
constexpr int WS_O = 4 << 20;
constexpr int WS_XS = 6 << 20;
constexpr int WS_H2 = 8 << 20;
constexpr int WS_END = WS_H2 + 128 * 43 * 1024;
constexpr int L_WK = 0, L_WV = 32768, L_WQ = 65536, L_PHI = 98304;
constexpr int L_WO = 0;
constexpr int L_W3 = 57344;
constexpr int L_W4 = 0;
constexpr int L_MISC = 155648;
constexpr int L_RED = L_MISC, L_MODB = L_MISC + 1024, L_KVL = L_MISC + 3072, L_ZL = L_KVL + 2048, L_ZERO = L_ZL + 128, L_UT = L_MISC + 5376;
constexpr int L_XRES = 126976;
constexpr int LDS_BYTES = 163840;
constexpr int AUX_SC1 = 16;
constexpr u32 SPIN_CAP = 1u << 18;

struct Params { const u16 *x, *wqkv, *wphi, *wo, *wg, *wu, *wd, *c, *wmod; u16* out; char* ws; };

DI float asf(u32 v) { return __builtin_bit_cast(float, v); }
DI u32 asu(float v) { return __builtin_bit_cast(u32, v); }
DI float bflo(u32 v) { return __builtin_bit_cast(float, v << 16); }
DI float bfhi(u32 v) { return __builtin_bit_cast(float, v & 0xffff0000u); }
DI u32 pk(float a, float b) { f2 t = {a, b}; return __builtin_bit_cast(u32, __builtin_convertvector(t, b2)); }
DI float rbf(float a) { return bflo(pk(a, a)); }
DI f16v mfma32(v4u a, v4u b, f16v c) { return __builtin_amdgcn_mfma_f32_32x32x16_bf16(__builtin_bit_cast(b8, a), __builtin_bit_cast(b8, b), c, 0, 0, 0); }
DI f4 mfma16(v4u a, v4u b, f4 c) { return __builtin_amdgcn_mfma_f32_16x16x32_bf16(__builtin_bit_cast(b8, a), __builtin_bit_cast(b8, b), c, 0, 0, 0); }
DI v2u trd(LAS char* p) { return __builtin_bit_cast(v2u, __builtin_amdgcn_ds_read_tr16_b64_v4i16((LAS s4*)p)); }
DI v4u trd2(LAS char* p0, LAS char* p1) { v2u a = trd(p0), b = trd(p1); return v4u{a.x, a.y, b.x, b.y}; }
DI rsrc_t mkrs(const void* p, int bytes) { return __builtin_amdgcn_make_buffer_rsrc((void*)p, 0, bytes, 0x00020000); }
template <int AUX> DI void st128(v4u v, rsrc_t rs, u32 voff, u32 soff) { __builtin_amdgcn_raw_buffer_store_b128(v, rs, voff + soff, 0, AUX); }
DI void dma16(rsrc_t rs, u32 voff, u32 soff, u32 lds) {
  asm volatile("s_mov_b32 m0, %0\n\ts_nop 0\n\tbuffer_load_dwordx4 %1, %2, %3 offen lds" ::"s"(lds), "v"(voff), "s"(rs), "s"(soff) : "memory");
}
DI void bar() { asm volatile("s_waitcnt lgkmcnt(0)\n\ts_barrier" ::: "memory"); }
DI void drain() { asm volatile("s_waitcnt vmcnt(0)" ::: "memory"); }
DI void cfence() { asm volatile("" ::: "memory"); }
template <int CTRL> DI float dppf(float v) { return __builtin_bit_cast(float, __builtin_amdgcn_update_dpp(0, __builtin_bit_cast(int, v), CTRL, 0xf, 0xf, true)); }
DI float sum16(float v) { auto r = __builtin_amdgcn_permlane16_swap(__builtin_bit_cast(u32, v), __builtin_bit_cast(u32, v), false, false); return asf(r[0]) + asf(r[1]); }
DI float sum32(float v) { auto r = __builtin_amdgcn_permlane32_swap(__builtin_bit_cast(u32, v), __builtin_bit_cast(u32, v), false, false); return asf(r[0]) + asf(r[1]); }
constexpr int ROR8 = 0x128, ROR4 = 0x124, ROR2 = 0x122, ROR1 = 0x121;
DI void wait_flags(rsrc_t ws, u32 off, int n, int lane, v2u tag) {
  const u32 vo = off + (lane < n ? lane : 0) * 8;
  for (u32 sp = 0; sp < SPIN_CAP; ++sp) {
    v2u f = __builtin_amdgcn_raw_buffer_load_b64(ws, vo, 0, AUX_SC1);
    cfence();
    if (__all(f.x == tag.x && f.y == tag.y)) break;
  }
  cfence();
}
DI void set_flag(rsrc_t ws, u32 off, int lane, v2u tag) { if (lane == 0) __builtin_amdgcn_raw_buffer_store_b64(tag, ws, off, 0, AUX_SC1); }
DI v2u wait_gran(rsrc_t ws, u32 voff, u32 tag) {
  v4u g;
  for (u32 sp = 0; sp < SPIN_CAP; ++sp) {
    g = __builtin_amdgcn_raw_buffer_load_b128(ws, voff, 0, AUX_SC1);
    cfence();
    if (__all(g.y == tag && g.w == tag)) break;
  }
  return v2u{g.x, g.z};
}
#define STAMP(i) do {} while (0)

DI void feature_map(const f16v& acc, v4u wp0, v4u wp1, u32 (&ph)[16]) {
  v4u b0 = {pk(acc[0], acc[1]), pk(acc[2], acc[3]), pk(acc[4], acc[5]), pk(acc[6], acc[7])};
  v4u b1 = {pk(acc[8], acc[9]), pk(acc[10], acc[11]), pk(acc[12], acc[13]), pk(acc[14], acc[15])};
  f16v z = {};
  f16v lg = mfma32(wp0, b0, z);
  lg = mfma32(wp1, b1, lg);
  float mx = lg[0], mn = lg[0];
#pragma unroll
  for (int i = 1; i < 16; ++i) { mx = __builtin_fmaxf(mx, lg[i]); mn = __builtin_fminf(mn, lg[i]); }
  const float L2E = 1.44269504088896f;
  const float cp = -mx * L2E, cn = mn * L2E;
  float ep[16], en[16];
#pragma unroll
  for (int i = 0; i < 16; ++i) { ep[i] = __builtin_amdgcn_exp2f(__builtin_fmaf(lg[i], L2E, cp)); en[i] = __builtin_amdgcn_exp2f(__builtin_fmaf(lg[i], -L2E, cn)); }
  f2 sp = {ep[0], ep[1]}, sn = {en[0], en[1]};
#pragma unroll
  for (int i = 1; i < 8; ++i) { sp += f2{ep[2 * i], ep[2 * i + 1]}; sn += f2{en[2 * i], en[2 * i + 1]}; }
  const float rp = __builtin_amdgcn_rcpf(sp.x + sp.y), rn = __builtin_amdgcn_rcpf(sn.x + sn.y);
#pragma unroll
  for (int i = 0; i < 8; ++i) { ph[i] = pk(ep[2 * i] * rp, ep[2 * i + 1] * rp); ph[8 + i] = pk(en[2 * i] * rn, en[2 * i + 1] * rn); }
}

__global__ void __launch_bounds__(256, 1) fused_block(Params p) {
  extern __shared__ __attribute__((aligned(16))) char smem_[];
  LAS char* L = (LAS char*)smem_;
  const u32 Lb = (u32)(size_t)L;
  const int tid = threadIdx.x, lane = tid & 63, b = blockIdx.x;
  const int w = __builtin_amdgcn_readfirstlane(tid >> 6);
  const int xg = b & 7, jj = b >> 3;
  const int pan = 4 * (xg >> 1) + (jj >> 3), mem = 8 * (xg & 1) + (jj & 7);
  const int rb = 4 * xg + (jj >> 3), ct = jj & 7;
  const int sp3 = 4 * (xg >> 2) + (jj >> 3), m3 = 8 * (xg & 3) + (jj & 7);
  const int u0 = m3 * 3 < 83 ? m3 * 3 : 83;
  const bool has3 = m3 < 29;
  STAMP(0);
  const u64 rt0_ = __builtin_amdgcn_s_memrealtime(); u64 rtx_ = rt0_;
  const rsrc_t ws = mkrs(p.ws, WS_END);
  const rsrc_t rx = mkrs(p.x, S_ * D_ * 2), rqkv = mkrs(p.wqkv, D_ * 3 * D_ * 2), rwo = mkrs(p.wo, D_ * D_ * 2);
  const rsrc_t rg = mkrs(p.wg, D_ * F_ * 2), ru = mkrs(p.wu, D_ * F_ * 2), rd = mkrs(p.wd, F_ * D_ * 2);
  const rsrc_t rmod = mkrs(p.wmod, D_ * 6 * D_ * 2), rc = mkrs(p.c, D_ * 2), rphi = mkrs(p.wphi, 32 * 256 * 2), rout = mkrs(p.out, S_ * D_ * 2);
  const int l31 = lane & 31, hh = lane >> 5, l15 = lane & 15, qq = lane >> 4;
  const int ta = (lane >> 2) & 3, tp = lane & 3;

  { const GAS char* q = (const GAS char*)p.ws + (64 << 20) + (size_t)b * 4096 + tid * 16; v4u z_[1];
#pragma unroll
    for (int i = 0; i < 1; ++i) asm volatile("global_load_dwordx4 %0, %1, off" : "=v"(z_[i]) : "v"(q + i * 4096) : "memory");
    asm volatile("s_waitcnt vmcnt(0)" ::: "memory");
#pragma unroll
    for (int i = 0; i < 1; ++i) asm volatile("" :: "v"(z_[i]));
    __builtin_amdgcn_sched_barrier(0); rtx_ = __builtin_amdgcn_s_memrealtime(); __builtin_amdgcn_sched_barrier(0); }
  const v2u ep = __builtin_amdgcn_raw_buffer_load_b64(ws, WS_EPOCH, 0, AUX_SC1);
  const bool hasmod = jj < 24;
  const int mcol = jj < 8 ? (2 * xg + (jj >> 2)) * 64 + (jj & 3) * 16 : 1024 + (4 * xg + ((jj - 8) >> 2)) * 64 + (jj & 3) * 16;
  v4u wm[4]; u16 cc[4];
  if (hasmod) {
#pragma unroll
    for (int i = 0; i < 4; ++i) cc[i] = __builtin_amdgcn_raw_buffer_load_b16(rc, ((tid >> 1) + 128 * i) * 2, 0, 0);
#pragma unroll
    for (int i = 0; i < 4; ++i) wm[i] = __builtin_amdgcn_raw_buffer_load_b128(rmod, ((tid >> 1) + 128 * i) * 6144 + (tid & 1) * 16, mcol * 2, 0);
  }
  v4u xr[2];
#pragma unroll
  for (int i = 0; i < 2; ++i) xr[i] = __builtin_amdgcn_raw_buffer_load_b128(rx, (lane & 7) * 1024 + ((lane >> 3) + 8 * i) * 16, b * 8192 + w * 256, 0);
  u16 wp[8];
  {
    const int hd = (l31 >> 2) & 1, f = 4 * (l31 >> 3) + (l31 & 3);
#pragma unroll
    for (int j = 0; j < 8; ++j) wp[j] = __builtin_amdgcn_raw_buffer_load_b16(rphi, (hd * 256 + (8 * (j >> 2) + 4 * hh + (j & 3)) * 16 + f) * 2, mem * 1024, 0);
  }
#pragma unroll
  for (int i = 0; i < 2; ++i) dma16(rx, (lane >> 3) * 1024 + (lane & 7) * 16, (rb * 64 + w * 16 + i * 8) * 1024 + ct * 128, Lb + L_XRES + w * 2048 + i * 1024);
  {
    const u32 vo = (lane >> 2) * 3072 + (lane & 3) * 16;
    for (int i = 0; i < 8; ++i) {
      const int r16 = i * 4 + w;
      dma16(rqkv, vo, r16 * 16 * 3072 + 1024 + mem * 64, Lb + L_WK + r16 * 1024);
      dma16(rqkv, vo, r16 * 16 * 3072 + 2048 + mem * 64, Lb + L_WV + r16 * 1024);
    }
    for (int i = 0; i < 8; ++i) { const int r16 = i * 4 + w; dma16(rqkv, vo, r16 * 16 * 3072 + mem * 64, Lb + L_WQ + r16 * 1024); }
  }
  const u64 tag64 = (((u64)ep.y << 32) | ep.x) + 0x9E3779B97F4A7C15ull;
  const v2u tag = {(u32)__builtin_amdgcn_readfirstlane((u32)tag64), (u32)__builtin_amdgcn_readfirstlane((u32)(tag64 >> 32))};
  if (tid < 16) *(LAS u32*)(L + L_ZERO + tid * 4) = 0u;
  STAMP(1);
  if (hasmod) {
    float a[8] = {};
#pragma unroll
    for (int i = 0; i < 4; ++i) {
      const float cv = bflo(cc[i]);
      const float s = rbf(cv * __builtin_amdgcn_rcpf(1.f + __builtin_amdgcn_exp2f(-1.44269504088896f * cv)));
#pragma unroll
      for (int j = 0; j < 4; ++j) { a[2 * j] += s * bflo(wm[i][j]); a[2 * j + 1] += s * bfhi(wm[i][j]); }
    }
#pragma unroll
    for (int j = 0; j < 8; ++j) { float v = a[j]; v += dppf<ROR2>(v); v += dppf<ROR4>(v); v += dppf<ROR8>(v); a[j] = v; }
    if (l15 < 2) { *(LAS f4*)(L + L_RED + (w * 4 + qq) * 64 + l15 * 32) = f4{a[0], a[1], a[2], a[3]}; *(LAS f4*)(L + L_RED + (w * 4 + qq) * 64 + l15 * 32 + 16) = f4{a[4], a[5], a[6], a[7]}; }
  }
  float xv[16];
#pragma unroll
  for (int i = 0; i < 2; ++i)
#pragma unroll
    for (int j = 0; j < 4; ++j) { xv[8 * i + 2 * j] = bflo(xr[i][j]); xv[8 * i + 2 * j + 1] = bfhi(xr[i][j]); }
  float s1 = 0.f;
#pragma unroll
  for (int i = 0; i < 16; ++i) s1 += xv[i];
  s1 += dppf<ROR8>(s1); s1 = sum16(s1); s1 = sum32(s1);
  const float ml = s1 * (1.f / 128.f);
  float s2 = 0.f;
#pragma unroll
  for (int i = 0; i < 16; ++i) s2 += (xv[i] - ml) * (xv[i] - ml);
  s2 += dppf<ROR8>(s2); s2 = sum16(s2); s2 = sum32(s2);
  if (lane < 8) *(LAS f2*)(L + L_UT + lane * 32 + w * 8) = f2{ml, s2};
  bar();
  if (hasmod && w == 0) {
    f2 v = *(LAS f2*)(L + L_RED + (lane >> 3) * 128 + (lane & 7) * 8);
    v += *(LAS f2*)(L + L_RED + (lane >> 3) * 128 + 64 + (lane & 7) * 8);
    v.x += dppf<ROR8>(v.x); v.y += dppf<ROR8>(v.y);
    v.x = sum32(sum16(v.x)); v.y = sum32(sum16(v.y));
    if (lane < 8) __builtin_amdgcn_raw_buffer_store_b64(v2u{pk(v.x, v.y), tag.x}, ws, WS_MODG + lane * 8, mcol * 4, AUX_SC1);
  }
  STAMP(2);
  {
    const f4 t0 = *(LAS f4*)(L + L_UT + (lane & 7) * 32), t1 = *(LAS f4*)(L + L_UT + (lane & 7) * 32 + 16);
    const float m = (t0.x + t0.z + t1.x + t1.z) * 0.25f;
    const float M2 = t0.y + t0.w + t1.y + t1.w + 128.f * ((t0.x - m) * (t0.x - m) + (t0.z - m) * (t0.z - m) + (t1.x - m) * (t1.x - m) + (t1.z - m) * (t1.z - m));
    const float r = __builtin_amdgcn_rsqf(M2 * (1.f / 512.f) + 1e-5f);
#pragma unroll
    for (int i = 0; i < 16; ++i) xv[i] = (xv[i] - m) * r;
  }
  {
    const v2u g = wait_gran(ws, WS_MODG + hh * 2048 + w * 512 + l31 * 16, tag.x);
    *(LAS v2u*)(L + L_MODB + w * 512 + lane * 8) = g;
    cfence();
    STAMP(3);
#pragma unroll
    for (int i = 0; i < 2; ++i) {
      const v4u sa = *(LAS v4u*)(L + L_MODB + w * 512 + ((lane >> 3) + 8 * i) * 16);
      const v4u ca = *(LAS v4u*)(L + L_MODB + w * 512 + 256 + ((lane >> 3) + 8 * i) * 16);
      v4u h;
#pragma unroll
      for (int j = 0; j < 4; ++j) {
        const u32 g1 = pk(1.f + bflo(ca[j]), 1.f + bfhi(ca[j]));
        h[j] = pk(xv[8 * i + 2 * j] * bflo(g1) + bflo(sa[j]), xv[8 * i + 2 * j + 1] * bfhi(g1) + bfhi(sa[j]));
      }
      st128<AUX_SC1>(h, ws, ((lane >> 3) + 8 * i) * 512 + (lane & 7) * 16, WS_H + (b >> 2) * 32768 + w * 8192 + (b & 3) * 128);
    }
  }
  drain();
  bar();
  if (w == 0) set_flag(ws, WS_FH + (b >> 2) * 128 + (b & 3) * 8, lane, tag);
  STAMP(4);
  v2u xres[4];
#pragma unroll
  for (int t = 0; t < 4; ++t) xres[t] = *(LAS v2u*)(L + L_XRES + w * 2048 + l15 * 128 + t * 32 + qq * 8);
  v4u wp0, wp1;
  {
    const v4u v = {wp[0] | ((u32)wp[1] << 16), wp[2] | ((u32)wp[3] << 16), wp[4] | ((u32)wp[5] << 16), wp[6] | ((u32)wp[7] << 16)};
    const u32 m1 = ((l31 >> 2) & 1) ? 0xffffffffu : 0u;
    wp1 = v & m1; wp0 = v & ~m1;
  }

  const int t32 = pan * 4 + w;
  wait_flags(ws, WS_FH + t32 * 128, 4, lane, tag);
  STAMP(5);
  v4u hf[32];
#pragma unroll
  for (int s = 0; s < 32; ++s) hf[s] = __builtin_amdgcn_raw_buffer_load_b128(ws, lane * 16 + (s & 3) * 1024, WS_H + t32 * 32768 + (s >> 2) * 4096, AUX_SC1);
  __builtin_amdgcn_sched_barrier(0);
  LAS char* wa = L + (8 * hh + ta) * 64 + (qq & 1) * 32 + tp * 8;
  f16v ak = {}, av = {};
#pragma unroll
  for (int s = 0; s < 32; ++s) {
    ak = mfma32(trd2(wa + L_WK + s * 1024, wa + L_WK + s * 1024 + 256), hf[s], ak);
    av = mfma32(trd2(wa + L_WV + s * 1024, wa + L_WV + s * 1024 + 256), hf[s], av);
  }
  STAMP(6);
  {
    u32 ph[16];
    feature_map(ak, wp0, wp1, ph);
    LAS char* row = L + L_PHI + (w * 32 + l31) * 224;
#pragma unroll
    for (int i = 0; i < 4; ++i) *(LAS v4u*)(row + hh * 64 + i * 16) = v4u{ph[4 * i], ph[4 * i + 1], ph[4 * i + 2], ph[4 * i + 3]};
#pragma unroll
    for (int a = 0; a < 4; ++a) *(LAS v2u*)(row + 128 + a * 16 + hh * 8) = v2u{pk(av[4 * a], av[4 * a + 1]), pk(av[4 * a + 2], av[4 * a + 3])};
  }
  bar();
  STAMP(7);
  {
    LAS char* ra = L + L_PHI + (4 * qq + ta) * 224 + tp * 8;
    f4 kv = {}, zz = {};
    const v4u ones = {0x3f803f80u, 0x3f803f80u, 0x3f803f80u, 0x3f803f80u};
#pragma unroll
    for (int s = 0; s < 4; ++s) {
      const v4u A = trd2(ra + w * 32 + s * 32 * 224, ra + w * 32 + (s * 32 + 16) * 224);
      const v4u B = trd2(ra + 128 + (w >> 1) * 32 + s * 32 * 224, ra + 128 + (w >> 1) * 32 + (s * 32 + 16) * 224);
      kv = mfma16(A, B, kv); zz = mfma16(A, ones, zz);
    }
    st128<AUX_SC1>(__builtin_bit_cast(v4u, kv), ws, lane * 16, WS_KVP + (mem * 16 + pan) * 4096 + w * 1024);
    if (l15 == 0) st128<AUX_SC1>(__builtin_bit_cast(v4u, zz), ws, qq * 16, WS_ZP + (mem * 16 + pan) * 256 + w * 64);
  }
  drain();
  bar();
  if (w == 0) set_flag(ws, WS_FKV + mem * 128 + pan * 8, lane, tag);
  STAMP(8);
  const u32 vo128 = (lane >> 3) * 1024;
  {
    for (int i = 0; i < 16; ++i) {
      const int r8 = i * 4 + w;
      const u32 sw = (((lane >> 4) & 1) << 1) | ((w & 1) << 2);
      dma16(rwo, vo128 + ((lane & 7) ^ sw) * 16, r8 * 8192 + ct * 128, Lb + L_WO + r8 * 1024);
    }
  }
  u32 pq[16];
  {
    f16v aq = {};
#pragma unroll
    for (int s = 0; s < 32; ++s) aq = mfma32(trd2(wa + L_WQ + s * 1024, wa + L_WQ + s * 1024 + 256), hf[s], aq);
    feature_map(aq, wp0, wp1, pq);
  }
  STAMP(9);
  wait_flags(ws, WS_FKV + mem * 128, 16, lane, tag);
  STAMP(10);
  {
    f4 zq = __builtin_bit_cast(f4, __builtin_amdgcn_raw_buffer_load_b128(ws, (lane >> 2) * 256 + (lane & 3) * 16, WS_ZP + mem * 4096 + w * 64, AUX_SC1));
    v4u kp[16];
#pragma unroll
    for (int i = 0; i < 16; ++i) kp[i] = __builtin_amdgcn_raw_buffer_load_b128(ws, lane * 16, WS_KVP + (mem * 16 + i) * 4096 + w * 1024, AUX_SC1);
    __builtin_amdgcn_sched_barrier(0);
#pragma unroll
    for (int j = 0; j < 4; ++j) { float v = zq[j]; v += dppf<ROR4>(v); v += dppf<ROR8>(v); zq[j] = sum32(sum16(v)); }
    if (lane < 4) *(LAS v2u*)(L + L_ZL + w * 32 + lane * 8) = v2u{pk(zq.x, zq.y), pk(zq.z, zq.w)};
    f4 s = __builtin_bit_cast(f4, kp[0]);
#pragma unroll
    for (int i = 1; i < 16; ++i) s += __builtin_bit_cast(f4, kp[i]);
    *(LAS v2u*)(L + L_KVL + ((w >> 1) * 16 + l15) * 64 + (w & 1) * 32 + qq * 8) = v2u{pk(s.x, s.y), pk(s.z, s.w)};
  }
  bar();
  STAMP(11);
  {
    const bool mine = ((l31 >> 2) & 1) == hh;
    LAS char* pa = L + (mine ? L_KVL + (hh * 16 + 4 * (l31 >> 3) + (l31 & 3)) * 64 : L_ZERO);
    LAS char* pz = L + (mine ? L_ZL + hh * 64 : L_ZERO);
    f16v num = {}, den = {};
#pragma unroll
    for (int u = 0; u < 4; ++u) {
      const v4u B = {pq[4 * u], pq[4 * u + 1], pq[4 * u + 2], pq[4 * u + 3]};
      den = mfma32(*(LAS v4u*)(pz + u * 16), B, den);
      num = mfma32(*(LAS v4u*)(pa + u * 16), B, num);
    }
    const float ri = __builtin_amdgcn_rcpf(den[0] + 1e-6f);
#pragma unroll
    for (int e = 0; e < 2; ++e) {
      v4u o;
#pragma unroll
      for (int j = 0; j < 4; ++j) o[j] = pk(num[8 * e + 2 * j] * ri, num[8 * e + 2 * j + 1] * ri);
      st128<AUX_SC1>(o, ws, (l31 >> 4) * 16384 + (2 * hh + e) * 256 + l15 * 16, WS_O + t32 * 32768 + mem * 1024);
    }
  }
  drain();
  set_flag(ws, WS_FO + t32 * 128 + mem * 8, lane, tag);
  STAMP(12);
  u32 v3[3], v3u[3];
#pragma unroll
  for (int i = 0; i < 3; ++i) { const int id = i * 64 + lane, r = id / 6, c = id - 6 * r; v3[i] = r * 2752 + c * 16; v3u[i] = (r ^ 4) * 2752 + c * 16; }
  if (has3) {
    for (int g = 2 + (w >> 1); g < 16; g += 2) {
#pragma unroll
      for (int i = 0; i < 3; ++i) {
        if (w & 1) dma16(ru, v3u[i], g * 32 * 2752 + u0 * 32, Lb + L_W3 + g * 6144 + 3072 + i * 1024);
        else dma16(rg, v3[i], g * 32 * 2752 + u0 * 32, Lb + L_W3 + g * 6144 + i * 1024);
      }
    }
  }
  v2u gam[4], cbm[4], gbm[4];
#pragma unroll
  for (int t = 0; t < 4; ++t) {
    gam[t] = wait_gran(ws, WS_MODG + (1024 + ct * 64 + t * 16 + qq * 4) * 4, tag.x);
    cbm[t] = wait_gran(ws, WS_MODG + (2048 + ct * 64 + t * 16 + qq * 4) * 4, tag.x);
    gbm[t] = wait_gran(ws, WS_MODG + (2560 + ct * 64 + t * 16 + qq * 4) * 4, tag.x);
  }
  {
    v2u g = wait_gran(ws, WS_MODG + (1536 + tid * 4) * 4, tag.x);
    if (w >= 2) g = v2u{pk(1.f + bflo(g.x), 1.f + bfhi(g.x)), pk(1.f + bflo(g.y), 1.f + bfhi(g.y))};
    const int c4 = tid & 127;
    *(LAS v2u*)(L + L_MODB + (w >= 2 ? 0 : 1024) + (c4 >> 3) * 64 + (c4 & 3) * 16 + ((c4 >> 2) & 1) * 8) = g;
  }
  v4u wf[64];
  {
    const int sw = (((ta >> 1) & 1) << 1) | ((qq & 1) << 2);
#pragma unroll
    for (int t = 0; t < 4; ++t) {
      LAS char* a = L + L_WO + (8 * qq + ta) * 128 + (((2 * t + (tp >> 1)) ^ sw) * 16) + (tp & 1) * 8;
#pragma unroll
      for (int s = 0; s < 16; ++s) wf[s * 4 + t] = trd2(a + s * 4096, a + s * 4096 + 512);
    }
  }
  bar();
  STAMP(13);
  wait_flags(ws, WS_FO + (rb * 2 + (w >> 1)) * 128, 16, lane, tag);
  if (b == 0 && w == 0 && lane == 0) __builtin_amdgcn_raw_buffer_store_b64(tag, ws, WS_EPOCH, 0, AUX_SC1);
  STAMP(14);
  f4 x1[4];
  {
    v4u of[16];
#pragma unroll
    for (int s = 0; s < 16; ++s) of[s] = __builtin_amdgcn_raw_buffer_load_b128(ws, lane * 16 + (s & 3) * 1024, WS_O + (rb * 4 + w) * 16384 + (s >> 2) * 4096, AUX_SC1);
    __builtin_amdgcn_sched_barrier(0);
    f4 acc[4] = {};
#pragma unroll
    for (int s = 0; s < 16; ++s)
#pragma unroll
      for (int t = 0; t < 4; ++t) acc[t] = mfma16(wf[s * 4 + t], of[s], acc[t]);
    float sm = 0.f;
#pragma unroll
    for (int t = 0; t < 4; ++t) {
      x1[t] = f4{bflo(xres[t].x) + bflo(gam[t].x) * acc[t].x, bfhi(xres[t].x) + bfhi(gam[t].x) * acc[t].y, bflo(xres[t].y) + bflo(gam[t].y) * acc[t].z, bfhi(xres[t].y) + bfhi(gam[t].y) * acc[t].w};
      sm += (x1[t].x + x1[t].y) + (x1[t].z + x1[t].w);
    }
#pragma unroll
    for (int T = 0; T < 2; ++T) {
      v4u xs;
#pragma unroll
      for (int e = 0; e < 2; ++e) {
        const int t = 2 * T + e;
        const u32 g0 = pk(1.f + bflo(cbm[t].x), 1.f + bfhi(cbm[t].x)), g1 = pk(1.f + bflo(cbm[t].y), 1.f + bfhi(cbm[t].y));
        xs[2 * e] = pk(x1[t].x * bflo(g0), x1[t].y * bfhi(g0));
        xs[2 * e + 1] = pk(x1[t].z * bflo(g1), x1[t].w * bfhi(g1));
      }
      st128<AUX_SC1>(xs, ws, qq * 512 + l15 * 16 + T * 2048, WS_XS + (rb * 2 + (w >> 1)) * 32768 + ct * 4096 + (w & 1) * 256);
    }
    const float m = sum32(sum16(sm)) * (1.f / 64.f);
    float q2 = 0.f;
#pragma unroll
    for (int t = 0; t < 4; ++t)
#pragma unroll
      for (int r = 0; r < 4; ++r) q2 += (x1[t][r] - m) * (x1[t][r] - m);
    q2 = sum32(sum16(q2));
    if (lane < 16) __builtin_amdgcn_raw_buffer_store_b64(v2u{__builtin_bit_cast(u32, m), __builtin_bit_cast(u32, q2)}, ws, lane * 8, WS_ST + (rb * 2 + (w >> 1)) * 2048 + ct * 256 + (w & 1) * 128, AUX_SC1);
  }
  drain();
  bar();
  if (w == 0) set_flag(ws, WS_FX + rb * 128 + ct * 8, lane, tag);
  STAMP(15);
  const u32 vo4 = vo128 + ((lane & 7) ^ (((lane >> 4) & 3) << 1)) * 16;
  if (has3) {
    const int g = w >> 1;
#pragma unroll
    for (int i = 0; i < 3; ++i) {
      if (w & 1) dma16(ru, v3u[i], g * 32 * 2752 + u0 * 32, Lb + L_W3 + g * 6144 + 3072 + i * 1024);
      else dma16(rg, v3[i], g * 32 * 2752 + u0 * 32, Lb + L_W3 + g * 6144 + i * 1024);
    }
    drain();
  }
  for (int i = 0; i < 14; ++i) { const int r8 = i * 4 + w; dma16(rd, vo4, r8 * 8192 + ct * 128, Lb + L_W4 + r8 * 1024); }
  bar();
  if (has3) {
    LAS char* w3 = L + L_W3 + (qq & 1) * 3072 + ((4 * hh + ta) ^ ((qq & 1) * 4)) * 96 + tp * 8;
    {
      f16v ut[3] = {};
      LAS char* mb = L + L_MODB + ((l31 >> 2) & 1) * 1024 + hh * 16 + w * 256;
      LAS char* wk = w3 + w * 4 * 6144;
#pragma unroll
      for (int s = 0; s < 8; ++s) {
        const v4u A = *(LAS v4u*)(mb + s * 32);
#pragma unroll
        for (int t = 0; t < 3; ++t) { LAS char* a = wk + (s >> 1) * 6144 + (s & 1) * 768 + t * 32; ut[t] = mfma32(A, trd2(a, a + 1536), ut[t]); }
      }
#pragma unroll
      for (int t = 0; t < 3; ++t) *(LAS float*)(L + L_UT - 2304 + t * 1024 + lane * 16 + w * 4) = ut[t][0];
    }
    bar();
    u32 utp[3];
#pragma unroll
    for (int t = 0; t < 3; ++t) {
      const f4 v = *(LAS f4*)(L + L_UT - 2304 + t * 1024 + lane * 16);
      const float s = (v.x + v.y) + (v.z + v.w), hi = rbf(s);
      utp[t] = pk(hi, s - hi);
    }
    STAMP(16);
    const int rb3 = sp3 * 4 + w;
    wait_flags(ws, WS_FX + rb3 * 128, 8, lane, tag);
    STAMP(17);
    constexpr int RING = 12;
    v4u xf[2][RING];
    const u32 xo = WS_XS + rb3 * 65536;
#pragma unroll
    for (int s = 0; s < RING; ++s)
#pragma unroll
      for (int mt = 0; mt < 2; ++mt) xf[mt][s] = __builtin_amdgcn_raw_buffer_load_b128(ws, lane * 16 + (s & 3) * 1024, xo + mt * 32768 + (s >> 2) * 4096, AUX_SC1);
    __builtin_amdgcn_sched_barrier(0);
    f16v acc[2][3] = {};
    v2u st[2][4];
#pragma unroll
    for (int s = 0; s < 32; ++s) {
      v4u A[3];
#pragma unroll
      for (int t = 0; t < 3; ++t) { LAS char* a = w3 + (s >> 1) * 6144 + (s & 1) * 768 + t * 32; A[t] = trd2(a, a + 1536); }
#pragma unroll
      for (int mt = 0; mt < 2; ++mt) {
        const v4u B = xf[mt][s % RING];
#pragma unroll
        for (int t = 0; t < 3; ++t) acc[mt][t] = mfma32(A[t], B, acc[mt][t]);
        if (s + RING < 32) xf[mt][s % RING] = __builtin_amdgcn_raw_buffer_load_b128(ws, lane * 16 + ((s + RING) & 3) * 1024, xo + mt * 32768 + ((s + RING) >> 2) * 4096, AUX_SC1);
        if (s + RING == 32) {
#pragma unroll
          for (int i = 0; i < 4; ++i) st[mt][i] = __builtin_amdgcn_raw_buffer_load_b64(ws, (hh * 4 + i) * 256 + l31 * 8, WS_ST + (rb3 * 2 + mt) * 2048, AUX_SC1);
        }
      }
    }
    STAMP(18);
#pragma unroll
    for (int mt = 0; mt < 2; ++mt) {
      float mi[4], ms = 0.f, M2 = 0.f;
#pragma unroll
      for (int i = 0; i < 4; ++i) { mi[i] = asf(st[mt][i].x); ms += mi[i]; M2 += asf(st[mt][i].y); }
      const float m = sum32(ms) * 0.125f;
#pragma unroll
      for (int i = 0; i < 4; ++i) M2 += 64.f * (mi[i] - m) * (mi[i] - m);
      const float var = sum32(M2) * (1.f / 512.f) + 1e-5f;
      const float c = hh ? __builtin_amdgcn_sqrtf(var) : -m, chi = rbf(c);
      const float r2 = __builtin_amdgcn_rcpf(var);
      const v4u B = {pk(chi, chi), pk(c - chi, c - chi), 0u, 0u};
#pragma unroll
      for (int t = 0; t < 3; ++t) {
        const f16v a = mfma32(v4u{utp[t], utp[t], 0u, 0u}, B, acc[mt][t]);
        v4u h2;
#pragma unroll
        for (int j = 0; j < 4; ++j) h2[j] = pk(__builtin_fmaxf(a[2 * j], 0.f) * (a[8 + 2 * j] * r2), __builtin_fmaxf(a[2 * j + 1], 0.f) * (a[9 + 2 * j] * r2));
        const int u = u0 + t;
        st128<AUX_SC1>(h2, ws, (l31 >> 4) * 44032 + hh * 256 + l15 * 16, WS_H2 + (rb3 * 4 + mt * 2) * 44032 + u * 512);
      }
    }
    drain();
    set_flag(ws, WS_FH2 + rb3 * 256 + m3 * 8, lane, tag);
  } else {
    bar();
    drain();
  }
  bar();
  STAMP(19);
  for (int i = 14; i < 38; ++i) { const int r8 = i * 4 + w; dma16(rd, vo4, r8 * 8192 + ct * 128, Lb + L_W4 + r8 * 1024); }
  {
    const int r0 = 16 * (qq >> 1) + 4 * (qq & 1) + ta;
    const int sw = ((r0 >> 1) & 3) << 1;
    LAS char* a4[4];
#pragma unroll
    for (int t = 0; t < 4; ++t) a4[t] = L + L_W4 + r0 * 128 + (((2 * t + (tp >> 1)) ^ sw) * 16) + (tp & 1) * 8;
    v4u wd[56];
#pragma unroll
    for (int s = 0; s < 14; ++s)
#pragma unroll
      for (int t = 0; t < 4; ++t) wd[s * 4 + t] = trd2(a4[t] + s * 4096, a4[t] + s * 4096 + 1024);
    bar();
    for (int i = 38; i < 43; ++i) { const int r8 = i * 4 + w; dma16(rd, vo4, r8 * 8192 + ct * 128, Lb + L_W4 + (r8 - 152) * 1024); }
    STAMP(20);
    wait_flags(ws, WS_FH2 + rb * 256, 29, lane, tag);
    bar();
    STAMP(21);
    v4u gf[43];
#pragma unroll
    for (int s = 0; s < 43; ++s) gf[s] = __builtin_amdgcn_raw_buffer_load_b128(ws, lane * 16 + (s & 3) * 1024, WS_H2 + (rb * 4 + w) * 44032 + (s >> 2) * 4096, AUX_SC1);
    __builtin_amdgcn_sched_barrier(0);
    f4 acc[4] = {};
#pragma unroll
    for (int s = 0; s < 43; ++s)
#pragma unroll
      for (int t = 0; t < 4; ++t) {
        const int so = (s < 38 ? s : s - 38) * 4096;
        acc[t] = mfma16(s < 14 ? wd[s * 4 + t] : trd2(a4[t] + so, a4[t] + so + 1024), gf[s], acc[t]);
      }
    STAMP(22);
    LAS char* ob = L + L_MISC + w * 2048;
#pragma unroll
    for (int t = 0; t < 4; ++t) {
      const v2u o = {pk(x1[t].x + bflo(gbm[t].x) * acc[t].x, x1[t].y + bfhi(gbm[t].x) * acc[t].y), pk(x1[t].z + bflo(gbm[t].y) * acc[t].z, x1[t].w + bfhi(gbm[t].y) * acc[t].w)};
      *(LAS v2u*)(ob + l15 * 128 + (((2 * t + (qq >> 1)) ^ (l15 & 7)) * 16) + (qq & 1) * 8) = o;
    }
    cfence();
#pragma unroll
    for (int i = 0; i < 2; ++i) {
      const v4u o = *(LAS v4u*)(ob + i * 1024 + (lane >> 3) * 128 + (((lane & 7) ^ (lane >> 3)) * 16));
      st128<0>(o, rout, (lane >> 3) * 1024 + (lane & 7) * 16, (rb * 64 + w * 16 + i * 8) * 1024 + ct * 128);
    }
  }
  STAMP(23);
  { const u64 tgt = rt0_ + 10000 + 10 * (rtx_ - rt0_); while (__builtin_amdgcn_s_memrealtime() < tgt) {} }
}

extern "C" void kernel_launch(void* const* d_in, const int* in_sizes, int n_in, void* d_out, int out_size, void* d_ws, size_t ws_size, hipStream_t stream) {
  Params p;
  p.x = (const u16*)d_in[0]; p.wqkv = (const u16*)d_in[1]; p.wphi = (const u16*)d_in[2]; p.wo = (const u16*)d_in[3];
  p.wg = (const u16*)d_in[5]; p.wu = (const u16*)d_in[6]; p.wd = (const u16*)d_in[7]; p.c = (const u16*)d_in[8]; p.wmod = (const u16*)d_in[9];
  p.out = (u16*)d_out; p.ws = (char*)d_ws;
  (void)hipFuncSetAttribute((const void*)fused_block, hipFuncAttributeMaxDynamicSharedMemorySize, LDS_BYTES);
  fused_block<<<256, 256, LDS_BYTES, stream>>>(p);
}
```

```cpp
#include <hip/hip_runtime.h>
struct Srd { const void* p; unsigned n; unsigned f; };
struct Params { Srd r[11]; };

constexpr unsigned WS_EPOCH = 0, WS_MODG = 4096, WS_VOTE = 8192, WS_F1 = 16384, WS_F3 = 32768, WS_F4 = 49152, WS_RS = 65536, WS_MODP = 262144, WS_MODL = 393216, WS_STAMP = 524288, WS_PQ = 2097152, WS_PWO = 2621440, WS_JUNK = 3145728, WS_PW4 = 6291456, WS_PW3 = 37748736, WS_END = 41943040, WS_ZP = 1048576, WS_ST = 1179648, MB_H = 1310720, MB_KV = 1572864, MB_O = 1835008, WS_H = 4194304, WS_O = 8388608, WS_XS = 12582912, WS_H2 = 16777216, WS_KVP = 33554432, MB_X = 34603008, MB_A = 35127296, L_WKV = 0, L_WQ = 65536, L_PHI = 98304, L_RP1 = 98304, L_XT = 106496, L_WPH = 114688, L_LT = 126976, L_R4 = 135168, L_R3 = 143360, L_WO = 0, L_W3A = 57344, L_W3B = 122880, L_W4B = 0, L_MISC = 155648, L_MODF = 155648, L_KVL = 156672, L_ZL = 158720, L_ZERO = 158848, L_MODB = 161792, L_UT = 156672, L_OUT = 24576;
constexpr unsigned sp(unsigned x) { return x + (x & ~8191u); }
struct Tab { unsigned s[1024 * 96]; };
constexpr Tab make_tab() {
  Tab t{};
  for (unsigned i = 0; i < 1024; ++i) {
    const unsigned b = i >> 2, w = i & 3, xg = b & 7, jj = b >> 3, ct = jj & 7, kap = b >> 4, sig = b & 15;
    const unsigned mem = (xg&3)*4 + (jj&3), pan = (xg>>2)*8 + (jj>>2);
    const unsigned rb = xg*4 + (jj>>3), t16 = rb*4 + w, t32 = pan*4 + w, sp3 = (xg>>2)*4 + (jj>>3), rb3 = sp3*4 + w, m3 = (xg&3)*8 + ct, g3 = sp3*2 + (w>>1);
    unsigned* r = t.s + ((xg*32 + jj)*4 + w)*96;
    r[0] = kap*64;
    r[1] = kap*32*6144 + sig*128 + w*32;
    r[2] = ((xg*32+jj)*4+w)*2048;
    r[3] = (jj*16+w*4)*3072 + 1024 + (xg&3)*256;
    r[4] = L_XT + w*2048;
    r[5] = WS_MODP + sig*8192 + kap*512 + w*128;
    r[6] = WS_RS + ((xg*32+jj)*4+w)*64;
    r[7] = (jj < 2 && w == 3) ? MB_O + ((xg>>2)^jj)*8192 + xg*16896 : 0;
    r[8] = WS_VOTE + xg*128 + (jj&1)*64;
    r[9] = WS_MODP + (w>>1)*65536 + ct*8192 + (w&1)*256;
    r[10] = WS_MODP + (w>>1)*65536 + ct*8192 + (w&1)*256 + 4096;
    r[11] = WS_RS + t16*512;
    r[12] = (w>>1) ? 0x3f800000u : 0;
    r[13] = WS_H + sp((t16&63)*16384 + ct*2048);
    r[14] = (xg>>2)*8192;
    r[15] = t16*16384 + ct*128 + 8192;
    r[16] = (jj>>2)*8192;
    r[17] = (w>>1)*4096 + (w&1)*1024;
    r[18] = w*16*3072 + mem*64 + 1024;
    r[19] = t32*8*((mem&1) ? 1024 : 3072) + (mem>>1)*128;
    r[20] = L_RP1 + w*1024;
    r[21] = (mem&1) ? 0 : 2048;
    r[22] = mem*1024 + w*256;
    r[23] = L_WPH + w*256;
    r[24] = ((b>>5)*64 + w*8)*6144 + (b&31)*128 + 2048;
    r[25] = (b>>5)*128;
    r[26] = L_LT + w*1024;
    r[27] = MB_H + ((rb&15)>>1)*32768 + sp(w*4096) + ((rb&1)*8 + ct)*64;
    r[28] = (mem&1) ? WS_PWO + (mem>>1)*65536 + t32*1024 : WS_PQ + (mem + (w>>1))*32768 + pan*2048 + (w&1)*1024;
    r[29] = MB_H + (pan&7)*32768 + sp(mem*1024);
    r[30] = WS_H + (t32&31)*65536;
    r[31] = WS_F1 + mem*1024 + pan*64;
    r[32] = w;
    r[33] = w*4096;
    r[34] = (jj>>3)*16384 + w*4096;
    r[35] = t16*16384 + ct*128;
    r[36] = pan;
    r[37] = (0xffffu) & ~((pan>>2) == w ? 0xfu << (4*(pan&3)) : 0u);
    r[38] = rb;
    r[39] = m3;
    r[40] = WS_KVP + (mem*4+w)*16384;
    r[41] = WS_ZP + sp((mem*4+w)*1024);
    r[42] = WS_PW3 + m3*131072;
    r[43] = WS_PW3 + m3*131072 + g3*4096 + (w&1)*1024;
    r[44] = WS_PW3 + m3*131072 + 65536 + g3*2048 + (w&1)*1024;
    r[45] = WS_PW4 + ct*262144;
    r[46] = WS_PW4 + ct*262144 + t16*1024;
    r[47] = WS_PW4 + ct*262144 + (32 + (rb < 10 ? rb : 10))*4096 + w*1024;
    r[48] = WS_MODL + (b&31)*4096 + (b>>5)*512 + w*128;
    r[49] = WS_PQ + mem*32768;
    r[50] = WS_PWO + ct*65536;
    r[51] = WS_KVP + (mem*4+w)*16384 + pan*512;
    r[52] = WS_KVP + (mem*4+w)*16384 + pan*512 + 8192;
    r[53] = WS_ZP + sp((mem*4+w)*1024) + pan*64;
    r[54] = WS_ZP + sp((mem*4+w)*1024) + pan*64 + 8192;
    r[55] = MB_KV + mem*16384 + w*4096 + pan*64;
    r[56] = MB_KV + mem*16384 + (pan&7)*1024;
    r[57] = g3*32*2752 + m3*96;
    r[58] = L_R3 + w*3072;
    r[59] = (rb < 29 ? rb*48 : (rb-29)*96 + 32 + (w>>1)*32)*1024 + w*8192 + ct*128;
    r[60] = (((rb < 10 ? rb : 10) + 3)*96 + 32 + (w>>1)*32)*1024 + w*8192 + ct*128;
    r[61] = L_R4 + w*1024;
    r[62] = WS_F1 + (mem>>1)*2048;
    r[63] = ct*2048 + 1024 - (mem>>1)*2048;
    r[64] = WS_XS + sp((t16&63)*16384 + ct*2048);
    r[65] = WS_ST + sp((rb&15)*4096) + ct*512 + w*128;
    r[66] = MB_X + (rb&15)*32768 + sp(w*4096) + ct*64;
    r[67] = WS_F4 + ct*2048;
    r[68] = WS_MODG + w*1024;
    r[69] = w == 2 ? 0x3f800000u : 0;
    r[70] = MB_X + (rb3&15)*32768 + sp(m3*512);
    r[71] = WS_XS + (rb3&15)*131072;
    r[72] = WS_H2 + ((rb3&15)>>2)*4194304 + w*524288 + sp((m3 + (m3>>1))*1024);
    r[73] = m3 < 28 ? WS_H2 + ((rb3&15)>>2)*4194304 + w*524288 + sp(m3*512 + (m3>>1)*2048 + 2048) : WS_JUNK;
    r[74] = MB_A + (rb3&15)*32768 + m3*64;
    r[75] = WS_ST + sp((rb3&15)*4096);
    r[76] = MB_A + (rb&15)*32768 + sp(ct*2048);
    r[77] = WS_H2 + ((t16&63)>>4)*4194304 + (t16&15)*131072;
    r[78] = WS_ST + sp((rb&15)*4096) + w*128;
    r[79] = w < 3 ? 0xffffffffu : 0xfffffu;
    r[80] = WS_F3 + m3*512;
    r[81] = (w&1) ? WS_F4 + ct*2048 + rb*64 : WS_F3 + m3*512 + (rb3>>2)*64;
    r[82] = WS_O + (t32&31)*65536 + sp(mem*1024);
    r[83] = MB_O + (pan&7)*32768 + sp(w*4096) + mem*64;
    r[84] = MB_O + (rb&15)*16384 + ct*1024;
    r[85] = WS_MODL + w*32768 + ct*4096;
    r[86] = WS_O + (t16&63)*32768;
    r[87] = (rb == 0 && (w == 1 || w == 2)) ? WS_MODG + (w-1)*2048 + ct*256 : 0;
  }
  return t;
}
static __device__ const __attribute__((aligned(256))) Tab tab = make_tab();

__global__ void __launch_bounds__(256, 1) fused_block(Params p) {
  asm volatile(
  "s_mov_b64 s[0:1], %0\n"
  "s_mov_b32 s2, %1\n"
  "v_mov_b32 v0, %2\n"
  "s_mov_b64 s[98:99], %3\n"
  "s_load_dwordx16 s[4:19], s[0:1], 0x0\n"
  "s_load_dwordx16 s[20:35], s[0:1], 0x40\n"
  "s_load_dwordx8 s[36:43], s[0:1], 0x80\n"
  "s_load_dwordx4 s[44:47], s[0:1], 0xa0\n"
  "s_getpc_b64 s[90:91]\n"
  ".Lpc:\n"
  "s_lshr_b32 s48, s2, 3\n"
  "v_lshrrev_b32 v3, 5, v0\n"
  "v_lshl_add_u32 v3, s48, 3, v3\n"
  "v_lshlrev_b32 v3, 7, v3\n"
  "v_min_u32 v3, .Lend-.Lpc-4, v3\n"
  "global_load_dword v23, v3, s[90:91]\n"
  "v_lshrrev_b32 v3, 6, v0\n"
  "v_and_b32 v2, 63, v0\n"
  "v_readfirstlane_b32 s49, v3\n"
  "v_lshlrev_b32 v1, 4, v2\n"
  "v_and_b32 v16, 62, v2\n"
  "v_and_b32 v3, 1, v2\n"
  "v_mul_u32_u24 v17, 3072, v16\n"
  "v_lshl_or_b32 v17, v3, 4, v17\n"
  "v_lshrrev_b32 v3, 5, v2\n"
  "v_lshl_add_u32 v18, v3, 9, v1\n"
  "s_lshr_b32 s50, s2, 4\n"
  "s_and_b32 s51, s2, 15\n"
  "s_lshl2_add_u32 s51, s51, s49\n"
  "s_mul_i32 s52, s50, 196608\n"
  "s_lshl_b32 s97, s51, 5\n"
  "s_add_u32 s52, s52, s97\n"
  "s_lshl_b32 s50, s50, 6\n"
  "s_and_b32 s51, s2, 7\n"
  "s_lshl2_add_u32 s53, s48, s49\n"
  "s_lshl_b32 s54, s51, 18\n"
  "s_lshl_b32 s97, s53, 11\n"
  "s_add_u32 s54, s54, s97\n"
  "s_lshr_b32 s55, s54, 11\n"
  "s_mulk_i32 s55, 384\n"
  "s_add_u32 s98, s98, s55\n"
  "s_addc_u32 s99, s99, 0\n"
  "s_waitcnt lgkmcnt(0)\n"
  "buffer_load_dwordx4 v[36:39], v17, s[12:15], s52 offen\n"
  "buffer_load_ushort v32, v16, s[8:11], s50 offen\n"
  "buffer_load_dwordx4 v[44:47], v18, s[16:19], s54 offen\n"
  "buffer_load_dwordx4 v[48:51], v18, s[16:19], s54 offen offset:512\n"
  "s_load_dwordx2 s[88:89], s[4:5], 0x0\n"
  "s_and_b32 s56, s51, 3\n"
  "s_mul_i32 s57, s53, 12288\n"
  "s_lshl_b32 s97, s56, 8\n"
  "s_add_u32 s57, s57, s97\n"
  "s_add_u32 s57, s57, 1024\n"
  "v_bfe_u32 v3, v2, 1, 1\n"
  "v_lshlrev_b32 v22, 10, v3\n"
  "v_and_b32 v3, 1, v2\n"
  "v_lshl_or_b32 v22, v3, 7, v22\n"
  "v_lshrrev_b32 v3, 2, v2\n"
  "v_mul_u32_u24 v3, 3072, v3\n"
  "v_add_u32 v22, v3, v22\n"
  "s_mov_b64 exec, 0xffff\n"
  "buffer_load_dword v25, v22, s[20:23], s57 offen\n"
  "s_mov_b64 exec, -1\n"
  "v_lshrrev_b32 v3, 3, v2\n"
  "v_lshlrev_b32 v9, 10, v3\n"
  "v_and_b32 v3, 7, v2\n"
  "v_lshl_or_b32 v9, v3, 4, v9\n"
  "v_lshrrev_b32 v3, 3, v2\n"
  "v_lshlrev_b32 v3, 4, v3\n"
  "v_xor_b32 v20, v3, v9\n"
  "s_lshr_b32 s56, s48, 3\n"
  "s_and_b32 s57, s48, 7\n"
  "s_lshl_b32 s58, s51, 18\n"
  "s_lshl_b32 s97, s56, 16\n"
  "s_add_u32 s58, s58, s97\n"
  "s_lshl_b32 s97, s49, 14\n"
  "s_add_u32 s58, s58, s97\n"
  "s_lshl_b32 s97, s57, 7\n"
  "s_add_u32 s58, s58, s97\n"
  "s_lshl_b32 m0, s49, 11\n"
  "s_add_u32 m0, m0, 106496\n"
  "s_add_u32 s59, s58, 8192\n"
  "buffer_load_dwordx4 v20, s[16:19], s58 offen lds\n"
  "s_addk_i32 m0, 1024\n"
  "s_bitcmp1_b32 s49, 0\n"
  "buffer_load_dwordx4 v20, s[16:19], s59 offen lds\n"
  "s_cselect_b64 s[32:33], s[36:37], s[32:33]\n"
  "s_and_b32 s56, s2, 15\n"
  "s_lshl_b32 s94, s56, 13\n"
  "s_lshl_b32 s97, s50, 3\n"
  "s_add_u32 s94, s94, s97\n"
  "s_lshl_b32 s97, s49, 7\n"
  "s_add_u32 s94, s94, s97\n"
  "s_add_u32 s94, s94, 262144\n"
  "s_lshr_b32 s95, s54, 5\n"
  "s_add_u32 s95, s95, 65536\n"
  "s_lshr_b32 s56, s51, 2\n"
  "s_xor_b32 s56, s56, s48\n"
  "s_lshl_b32 s57, s56, 13\n"
  "s_mul_i32 s97, s51, 16896\n"
  "s_add_u32 s57, s57, s97\n"
  "s_add_u32 s57, s57, 1835008\n"
  "s_lshl_b32 s58, s51, 7\n"
  "s_lshl_b32 s97, s48, 6\n"
  "s_add_u32 s58, s58, s97\n"
  "s_add_u32 s58, s58, 8192\n"
  "s_mov_b32 s3, 0xffffff00\n"
  "s_mov_b32 s91, 0xffff0000\n"
  "s_mov_b32 s92, 0x3fb8aa3b\n"
  "v_and_b32 v3, 1, v2\n"
  "v_lshlrev_b32 v34, 6, v3\n"
  "v_lshrrev_b32 v3, 4, v2\n"
  "v_lshl_or_b32 v34, v3, 4, v34\n"
  "v_lshrrev_b32 v3, 5, v2\n"
  "v_lshlrev_b32 v35, 4, v3\n"
  "v_and_b32 v3, 1, v2\n"
  "v_lshl_or_b32 v35, v3, 5, v35\n"
  "s_waitcnt lgkmcnt(0)\n"
  "s_add_u32 s88, s88, 0x7f4a7c15\n"
  "s_addc_u32 s89, s89, 0x9e3779b9\n"
  "s_and_b32 s90, s89, 0xff\n"
  "v_mov_b32 v69, s88\n"
  "v_mov_b32 v71, s88\n"
  "v_mov_b32 v29, s88\n"
  "v_mov_b32 v31, s88\n"
  "s_or_b32 s56, s53, 4\n"
  "s_mov_b32 s39, s49\n"
  "s_cmp_eq_u32 s56, 7\n"
  "s_cbranch_scc0 .Lskip_1\n"
  "s_memtime s[16:17]\n"
  "s_load_dword s18, s[4:5], s57\n"
  ".Lskip_1:\n"
  "v_lshlrev_b32 v8, 3, v2\n"
  "v_add_u32 v11, 0x8000, v1\n"
  "v_lshrrev_b32 v3, 4, v2\n"
  "v_lshlrev_b32 v10, 9, v3\n"
  "v_and_b32 v3, 15, v2\n"
  "v_lshl_or_b32 v10, v3, 4, v10\n"
  "v_lshlrev_b32 v208, 6, v3\n"
  "v_lshlrev_b32 v204, 7, v3\n"
  "v_lshlrev_b32 v206, 3, v3\n"
  "v_mul_u32_u24 v98, 224, v3\n"
  "v_lshlrev_b32 v219, 4, v3\n"
  "v_and_b32 v3, 7, v2\n"
  "v_lshl_or_b32 v204, v3, 4, v204\n"
  "v_lshlrev_b32 v216, 7, v3\n"
  "s_mul_i32 s37, s39, 2048\n"
  "s_add_u32 s37, s37, 106496\n"
  "v_add_u32 v204, s37, v204\n"
  "v_lshrrev_b32 v3, 4, v2\n"
  "v_lshlrev_b32 v7, 3, v3\n"
  "v_xor_b32 v205, v7, v204\n"
  "v_add_u32 v98, v7, v98\n"
  "v_lshlrev_b32 v7, 4, v3\n"
  "v_xor_b32 v204, v7, v204\n"
  "v_lshlrev_b32 v7, 5, v3\n"
  "v_add_u32 v207, 155648, v7\n"
  "v_xor_b32 v13, v7, v9\n"
  "v_lshlrev_b32 v210, 10, v3\n"
  "v_lshlrev_b32 v96, 9, v3\n"
  "v_lshrrev_b32 v3, 5, v2\n"
  "v_lshl_or_b32 v206, v3, 2, v206\n"
  "v_lshl_or_b32 v219, v3, 9, v219\n"
  "v_lshl_or_b32 v216, v3, 6, v216\n"
  "v_lshlrev_b32 v107, 11, v3\n"
  "v_lshlrev_b32 v211, 8, v3\n"
  "v_lshlrev_b32 v218, 6, v3\n"
  "s_mul_i32 s37, s39, 128\n"
  "s_add_u32 s37, s37, 155648\n"
  "v_add_u32 v206, s37, v206\n"
  "v_lshrrev_b32 v3, 2, v2\n"
  "v_mul_u32_u24 v12, 3072, v3\n"
  "v_mul_u32_u24 v99, 224, v3\n"
  "v_mul_u32_u24 v101, 96, v3\n"
  "v_lshlrev_b32 v14, 10, v3\n"
  "v_and_b32 v3, 3, v2\n"
  "v_lshl_or_b32 v12, v3, 4, v12\n"
  "v_lshl_or_b32 v14, v3, 4, v14\n"
  "v_lshlrev_b32 v217, 6, v3\n"
  "v_lshlrev_b32 v3, 3, v3\n"
  "v_add_u32 v99, v3, v99\n"
  "v_add_u32 v101, v3, v101\n"
  "v_add_u32 v96, v3, v96\n"
  "v_add_u32 v210, v3, v210\n"
  "v_add_u32 v107, v3, v107\n"
  "s_mul_i32 s37, s39, 32\n"
  "s_add_u32 s37, s37, 158720\n"
  "v_add_u32 v103, s37, v3\n"
  "v_and_b32 v3, 32, v2\n"
  "v_xor_b32 v12, v3, v12\n"
  "s_cmp_eq_u32 s56, 7\n"
  "s_cbranch_scc0 .Lskip_2\n"
  "s_waitcnt lgkmcnt(0)\n"
  "s_memtime s[36:37]\n"
  "s_waitcnt lgkmcnt(0)\n"
  "s_sub_u32 s36, s36, s16\n"
  "s_lshl_b32 s36, s36, 8\n"
  "s_or_b32 s36, s36, s90\n"
  "v_mov_b32 v68, s36\n"
  "v_mov_b32 v70, s36\n"
  "s_mov_b64 exec, 0xf\n"
  "buffer_store_dwordx4 v[68:71], v1, s[4:7], s58 offen sc1\n"
  "s_mov_b64 exec, -1\n"
  ".Lskip_2:\n"
  "s_waitcnt lgkmcnt(0)\n"
  "s_load_dwordx16 s[48:63], s[98:99], 0x0\n"
  "s_load_dwordx16 s[64:79], s[98:99], 0x40\n"
  "s_load_dwordx8 s[80:87], s[98:99], 0x80\n"
  "s_load_dword s16, s[98:99], 0xc0\n"
  "s_load_dword s17, s[98:99], 0x100\n"
  "s_load_dword s36, s[98:99], 0x140\n"
  "v_mov_b32 v24, s90\n"
  "v_mov_b32 v4, s88\n"
  "v_mov_b32 v5, s89\n"
  "s_sub_u32 s18, s6, 8192\n"
  "s_mov_b32 s19, s7\n"
  "v_add_u32 v109, 65536, v1\n"
  "v_and_b32 v3, 31, v2\n"
  "v_lshlrev_b32 v17, 3, v3\n"
  "v_and_b32 v3, 7, v2\n"
  "v_lshlrev_b32 v222, 6, v3\n"
  "v_mov_b32 v7, 0\n"
  "v_and_b32 v3, 15, v2\n"
  "v_lshlrev_b32 v19, 2, v3\n"
  "v_add_u32 v19, 158848, v19\n"
  "ds_write_b32 v19, v7\n"
  "v_mov_b32 v116, 0x3f803f80\n"
  "v_mov_b32 v117, 0x3f803f80\n"
  "v_mov_b32 v118, 0x3f803f80\n"
  "v_mov_b32 v119, 0x3f803f80\n"
  "s_waitcnt vmcnt(5)\n"
  "v_lshlrev_b32 v32, 16, v32\n"
  "v_mov_b32 v78, 0x3f803f80\n"
  "v_mul_f32 v52, 0xbfb8aa3b, v32\n"
  "s_waitcnt vmcnt(4)\n"
  "v_dot2_f32_bf16 v74, v44, v78, 0\n"
  "v_exp_f32 v52, v52\n"
  "v_dot2_f32_bf16 v75, v44, v44, 0\n"
  "v_add_f32 v52, 1.0, v52\n"
  "v_dot2_f32_bf16 v76, v45, v78, 0\n"
  "v_rcp_f32 v52, v52\n"
  "v_dot2_f32_bf16 v77, v45, v45, 0\n"
  "v_mul_f32 v32, v32, v52\n"
  "v_dot2_f32_bf16 v74, v46, v78, v74\n"
  "v_cvt_pk_bf16_f32 v52, v32, 0\n"
  "v_dot2_f32_bf16 v75, v46, v46, v75\n"
  "v_cvt_pk_bf16_f32 v54, 0, v32\n"
  "v_dot2_f32_bf16 v76, v47, v78, v76\n"
  "v_dot2_f32_bf16 v56, v36, v52, 0\n"
  "v_dot2_f32_bf16 v77, v47, v47, v77\n"
  "v_dot2_f32_bf16 v57, v36, v54, 0\n"
  "s_waitcnt vmcnt(3)\n"
  "v_dot2_f32_bf16 v74, v48, v78, v74\n"
  "v_dot2_f32_bf16 v60, v38, v52, 0\n"
  "v_dot2_f32_bf16 v75, v48, v48, v75\n"
  "v_dot2_f32_bf16 v61, v38, v54, 0\n"
  "v_dot2_f32_bf16 v76, v49, v78, v76\n"
  "v_dot2_f32_bf16 v58, v37, v52, 0\n"
  "v_dot2_f32_bf16 v77, v49, v49, v77\n"
  "v_dot2_f32_bf16 v59, v37, v54, 0\n"
  "v_dot2_f32_bf16 v74, v50, v78, v74\n"
  "v_dot2_f32_bf16 v62, v39, v52, 0\n"
  "v_dot2_f32_bf16 v75, v50, v50, v75\n"
  "v_dot2_f32_bf16 v63, v39, v54, 0\n"
  "v_dot2_f32_bf16 v76, v51, v78, v76\n"
  "v_permlane32_swap_b32 v56, v60\n"
  "v_dot2_f32_bf16 v77, v51, v51, v77\n"
  "v_permlane32_swap_b32 v57, v61\n"
  "s_nop 1\n"
  "v_pk_add_f32 v[74:75], v[74:75], v[76:77]\n"
  "v_permlane32_swap_b32 v58, v62\n"
  "s_nop 0\n"
  "v_add_f32_dpp v74, v74, v74 row_ror:1 row_mask:0xf bank_mask:0xf\n"
  "v_permlane32_swap_b32 v59, v63\n"
  "v_add_f32_dpp v75, v75, v75 row_ror:1 row_mask:0xf bank_mask:0xf\n"
  "v_pk_add_f32 v[56:57], v[56:57], v[60:61]\n"
  "v_add_f32_dpp v74, v74, v74 row_ror:2 row_mask:0xf bank_mask:0xf\n"
  "v_pk_add_f32 v[58:59], v[58:59], v[62:63]\n"
  "v_add_f32_dpp v75, v75, v75 row_ror:2 row_mask:0xf bank_mask:0xf\n"
  "s_nop 0\n"
  "v_permlane16_swap_b32 v56, v58\n"
  "v_add_f32_dpp v74, v74, v74 row_ror:4 row_mask:0xf bank_mask:0xf\n"
  "v_permlane16_swap_b32 v57, v59\n"
  "v_add_f32_dpp v75, v75, v75 row_ror:4 row_mask:0xf bank_mask:0xf\n"
  "v_pk_add_f32 v[56:57], v[56:57], v[58:59]\n"
  "v_add_f32_dpp v74, v74, v74 row_ror:8 row_mask:0xf bank_mask:0xf\n"
  "s_nop 0\n"
  "v_add_f32_dpp v56, v56, v56 row_ror:2 row_mask:0xf bank_mask:0xf\n"
  "v_add_f32_dpp v75, v75, v75 row_ror:8 row_mask:0xf bank_mask:0xf\n"
  "v_add_f32_dpp v57, v57, v57 row_ror:2 row_mask:0xf bank_mask:0xf\n"
  "v_mov_b32 v76, v74\n"
  "v_add_f32_dpp v56, v56, v56 row_ror:4 row_mask:0xf bank_mask:0xf\n"
  "v_mov_b32 v77, v75\n"
  "v_add_f32_dpp v57, v57, v57 row_ror:4 row_mask:0xf bank_mask:0xf\n"
  "v_permlane16_swap_b32 v74, v76\n"
  "v_add_f32_dpp v56, v56, v56 row_ror:8 row_mask:0xf bank_mask:0xf\n"
  "v_permlane16_swap_b32 v75, v77\n"
  "v_add_f32_dpp v57, v57, v57 row_ror:8 row_mask:0xf bank_mask:0xf\n"
  "v_pk_add_f32 v[74:75], v[74:75], v[76:77]\n"
  "v_and_or_b32 v28, v56, s3, v24\n"
  "v_mul_f32 v74, 0x3b000000, v74\n"
  "v_and_or_b32 v30, v57, s3, v24\n"
  "v_mul_f32 v75, 0x3b000000, v75\n"
  "v_fma_f32 v75, -v74, v74, v75\n"
  "v_max_f32 v75, 0, v75\n"
  "v_add_f32 v75, 0x3727c5ac, v75\n"
  "v_rsq_f32 v75, v75\n"
  "v_and_or_b32 v68, v74, s3, v24\n"
  "v_and_or_b32 v70, v75, s3, v24\n"
  "s_mov_b32 exec_lo, 0x30003\n"
  "s_mov_b32 exec_hi, 0x30003\n"
  "buffer_store_dwordx4 v[28:31], v34, s[4:7], s94 offen sc1\n"
  "s_mov_b64 exec, -1\n"
  "s_mov_b32 exec_lo, 3\n"
  "s_mov_b32 exec_hi, 3\n"
  "buffer_store_dwordx4 v[68:71], v35, s[4:7], s95 offen sc1\n"
  "s_mov_b64 exec, -1\n"
  "v_bfe_u32 v3, v2, 1, 3\n"
  "v_lshlrev_b32 v15, 6, v3\n"
  "v_and_b32 v3, 1, v2\n"
  "v_lshl_or_b32 v15, v3, 4, v15\n"
  "v_lshl_or_b32 v211, v3, 9, v211\n"
  "v_lshlrev_b32 v102, 4, v3\n"
  "v_lshrrev_b32 v3, 3, v2\n"
  "v_mul_u32_u24 v209, 5120, v3\n"
  "v_add_u32 v209, v13, v209\n"
  "v_lshl_or_b32 v217, v3, 8, v217\n"
  "v_and_b32 v7, 3, v3\n"
  "v_lshl_or_b32 v216, v7, 3, v216\n"
  "v_and_b32 v3, 1, v3\n"
  "v_lshl_or_b32 v210, v3, 5, v210\n"
  "v_lshl_or_b32 v107, v3, 5, v107\n"
  "v_bfe_u32 v3, v2, 2, 2\n"
  "v_lshl_or_b32 v96, v3, 6, v96\n"
  "v_lshl_or_b32 v210, v3, 7, v210\n"
  "v_lshl_or_b32 v107, v3, 7, v107\n"
  "v_lshl_or_b32 v211, v3, 5, v211\n"
  "v_lshl_or_b32 v102, v3, 5, v102\n"
  "v_bfe_u32 v3, v2, 4, 1\n"
  "v_lshl_or_b32 v96, v3, 5, v96\n"
  "v_lshl_or_b32 v107, v3, 9, v107\n"
  "v_lshl_or_b32 v107, v3, 6, v107\n"
  "v_lshl_or_b32 v211, v3, 4, v211\n"
  "v_lshl_or_b32 v219, v3, 15, v219\n"
  "v_lshrrev_b32 v3, 1, v2\n"
  "v_lshl_or_b32 v102, v3, 7, v102\n"
  "v_and_b32 v3, 1, v3\n"
  "v_lshl_or_b32 v211, v3, 3, v211\n"
  "v_add_u32 v211, 114688, v211\n"
  "s_lshl_b32 s37, s39, 5\n"
  "v_xor_b32 v210, s37, v210\n"
  "v_add_u32 v210, 98304, v210\n"
  "v_xor_b32 v107, s37, v107\n"
  "v_add_u32 v107, 135168, v107\n"
  "v_xor_b32 v102, s37, v102\n"
  "v_add_u32 v102, 126976, v102\n"
  "v_and_b32 v3, 31, v2\n"
  "v_mul_u32_u24 v97, 224, v3\n"
  "v_add_u32 v97, v218, v97\n"
  "s_mul_i32 s37, s39, 7168\n"
  "s_add_u32 s37, s37, 98304\n"
  "v_add_u32 v97, s37, v97\n"
  "v_add_u32 v98, s37, v98\n"
  "s_lshr_b32 s37, s39, 1\n"
  "s_lshl_b32 s37, s37, 5\n"
  "s_add_u32 s37, s37, 98432\n"
  "v_add_u32 v100, s37, v99\n"
  "s_mul_i32 s37, s39, 32\n"
  "s_add_u32 s37, s37, 98304\n"
  "v_add_u32 v99, s37, v99\n"
  "s_mul_i32 s37, s39, 3072\n"
  "s_add_u32 s37, s37, 143360\n"
  "v_add_u32 v101, s37, v101\n"
  "s_mul_i32 s37, s39, 0x220\n"
  "s_and_b32 s37, s37, 0x420\n"
  "s_add_u32 s37, s37, 156672\n"
  "v_add_u32 v216, s37, v216\n"
  "v_add_u32 v218, 158720, v218\n"
  "v_add_u32 v217, 156672, v217\n"
  "v_lshrrev_b32 v3, 5, v2\n"
  "v_bfe_u32 v7, v2, 2, 1\n"
  "v_cmp_eq_u32 vcc, v3, v7\n"
  "s_movk_i32 s37, 2656\n"
  "v_mov_b32 v7, 158848\n"
  "v_cndmask_b32 v217, v7, v217, vcc\n"
  "v_cndmask_b32 v218, v7, v218, vcc\n"
  "v_add_u32 v3, 0, v2\n"
  "v_lshlrev_b32 v104, 4, v3\n"
  "v_mul_u32_u24 v3, 171, v3\n"
  "v_lshrrev_b32 v3, 10, v3\n"
  "v_mad_u32_u24 v104, v3, s37, v104\n"
  "v_add_u32 v3, 64, v2\n"
  "v_lshlrev_b32 v105, 4, v3\n"
  "v_mul_u32_u24 v3, 171, v3\n"
  "v_lshrrev_b32 v3, 10, v3\n"
  "v_mad_u32_u24 v105, v3, s37, v105\n"
  "v_add_u32 v3, 128, v2\n"
  "v_lshlrev_b32 v106, 4, v3\n"
  "v_mul_u32_u24 v3, 171, v3\n"
  "v_lshrrev_b32 v3, 10, v3\n"
  "v_mad_u32_u24 v106, v3, s37, v106\n"
  "s_movk_i32 s96, 8192\n"
  "s_waitcnt lgkmcnt(0)\n"
  "s_cmp_lg_u32 s69, 0\n"
  "s_cselect_b64 s[28:29], s[20:21], s[28:29]\n"
  "s_cselect_b32 s30, s22, s30\n"
  "v_lshrrev_b32 v3, 3, v2\n"
  "v_mad_u32_u24 v22, v3, s69, v13\n"
  "s_waitcnt vmcnt(2)\n"
  "ds_read_b128 v[80:83], v204\n"
  "v_xor_b32 v204, 64, v204\n"
  "ds_read_b128 v[84:87], v204\n"
  "ds_read_b64 v[232:233], v205\n"
  "v_xor_b32 v205, 32, v205\n"
  "ds_read_b64 v[234:235], v205\n"
  "v_xor_b32 v205, 96, v205\n"
  "ds_read_b64 v[236:237], v205\n"
  "v_xor_b32 v205, 32, v205\n"
  "ds_read_b64 v[238:239], v205\n"
  "s_waitcnt lgkmcnt(5)\n"
  "v_lshlrev_b32 v188, 16, v80\n"
  "v_and_b32 v189, s91, v80\n"
  "v_lshlrev_b32 v190, 16, v81\n"
  "v_and_b32 v191, s91, v81\n"
  "v_lshlrev_b32 v192, 16, v82\n"
  "v_and_b32 v193, s91, v82\n"
  "v_lshlrev_b32 v194, 16, v83\n"
  "v_and_b32 v195, s91, v83\n"
  "s_waitcnt lgkmcnt(4)\n"
  "v_lshlrev_b32 v196, 16, v84\n"
  "v_and_b32 v197, s91, v84\n"
  "v_lshlrev_b32 v198, 16, v85\n"
  "v_and_b32 v199, s91, v85\n"
  "v_lshlrev_b32 v200, 16, v86\n"
  "v_and_b32 v201, s91, v86\n"
  "v_lshlrev_b32 v202, 16, v87\n"
  "v_and_b32 v203, s91, v87\n"
  "s_barrier\n"
  "s_add_u32 m0, s64, s65\n"
  "s_mul_i32 s95, s64, 24\n"
  "s_add_u32 s95, s95, s66\n"
  "buffer_load_dwordx4 v12, s[20:23], s95 offen lds\n"
  "s_addk_i32 m0, 1024\n"
  "s_addk_i32 s64, 8192\n"
  "buffer_load_dwordx4 v12, s[20:23], s95 offen offset:1024 lds\n"
  "s_and_b32 s64, s64, 0xffff\n"
  "s_add_u32 m0, s64, s65\n"
  "s_mul_i32 s95, s64, 24\n"
  "s_add_u32 s95, s95, s66\n"
  "buffer_load_dwordx4 v12, s[20:23], s95 offen lds\n"
  "s_addk_i32 m0, 1024\n"
  "s_addk_i32 s64, 8192\n"
  "buffer_load_dwordx4 v12, s[20:23], s95 offen offset:1024 lds\n"
  "s_and_b32 s64, s64, 0xffff\n"
  "s_add_u32 m0, s64, s65\n"
  "s_mul_i32 s95, s64, 24\n"
  "s_add_u32 s95, s95, s66\n"
  "buffer_load_dwordx4 v12, s[20:23], s95 offen lds\n"
  "s_addk_i32 m0, 1024\n"
  "s_addk_i32 s64, 8192\n"
  "buffer_load_dwordx4 v12, s[20:23], s95 offen offset:1024 lds\n"
  "s_and_b32 s64, s64, 0xffff\n"
  "s_add_u32 m0, s64, s65\n"
  "s_mul_i32 s95, s64, 24\n"
  "s_add_u32 s95, s95, s66\n"
  "buffer_load_dwordx4 v12, s[20:23], s95 offen lds\n"
  "s_addk_i32 m0, 1024\n"
  "s_addk_i32 s64, 8192\n"
  "buffer_load_dwordx4 v12, s[20:23], s95 offen offset:1024 lds\n"
  "s_and_b32 s64, s64, 0xffff\n"
  "buffer_load_dwordx4 v[184:187], v208, s[4:7], s96 offen sc1\n"
  "buffer_load_dwordx4 v[180:183], v15, s[4:7], s59 offen sc1\n"
  "buffer_load_dwordx4 v[164:167], v10, s[4:7], s57 offen sc1\n"
  "buffer_load_dwordx4 v[168:171], v10, s[4:7], s57 offen offset:2048 sc1\n"
  "buffer_load_dwordx4 v[172:175], v10, s[4:7], s58 offen sc1\n"
  "buffer_load_dwordx4 v[176:179], v10, s[4:7], s58 offen offset:2048 sc1\n"
  "s_barrier\n"
  "s_mov_b32 m0, s68\n"
  "s_nop 0\n"
  "buffer_load_dwordx4 v22, s[28:31], s67 offen lds\n"
  "s_mov_b32 m0, s71\n"
  "s_mov_b64 exec, 0xffff\n"
  "buffer_load_dwordx4 v1, s[24:27], s70 offen lds\n"
  "s_mov_b64 exec, -1\n"
  "s_mov_b32 m0, s74\n"
  "buffer_load_ushort v26, v16, s[8:11], s73 offen\n"
  "buffer_load_ushort v27, v16, s[8:11], s73 offen offset:64\n"
  "buffer_load_dwordx4 v209, s[12:15], s72 offen lds\n"
  "s_addk_i32 m0, 4096\n"
  "s_add_u32 s72, s72, 196608\n"
  "buffer_load_dwordx4 v209, s[12:15], s72 offen lds\n"
  "s_waitcnt vmcnt(11)\n"
  "v_xor_b32 v7, s88, v185\n"
  "v_xor_b32 v23, s90, v184\n"
  "v_bitop3_b32 v7, s88, v187, v7 bitop3:0xbe\n"
  "v_bitop3_b32 v23, s90, v186, v23 bitop3:0xbe\n"
  "s_waitcnt vmcnt(10)\n"
  "v_bitop3_b32 v7, s88, v181, v7 bitop3:0xbe\n"
  "v_bitop3_b32 v23, s90, v180, v23 bitop3:0xbe\n"
  "v_bitop3_b32 v7, s88, v183, v7 bitop3:0xbe\n"
  "v_bitop3_b32 v23, s90, v182, v23 bitop3:0xbe\n"
  "s_waitcnt vmcnt(9)\n"
  "v_bitop3_b32 v7, s88, v165, v7 bitop3:0xbe\n"
  "v_bitop3_b32 v23, s90, v164, v23 bitop3:0xbe\n"
  "v_bitop3_b32 v7, s88, v167, v7 bitop3:0xbe\n"
  "v_bitop3_b32 v23, s90, v166, v23 bitop3:0xbe\n"
  "s_waitcnt vmcnt(8)\n"
  "v_bitop3_b32 v7, s88, v169, v7 bitop3:0xbe\n"
  "v_bitop3_b32 v23, s90, v168, v23 bitop3:0xbe\n"
  "v_bitop3_b32 v7, s88, v171, v7 bitop3:0xbe\n"
  "v_bitop3_b32 v23, s90, v170, v23 bitop3:0xbe\n"
  "s_waitcnt vmcnt(7)\n"
  "v_bitop3_b32 v7, s88, v173, v7 bitop3:0xbe\n"
  "v_bitop3_b32 v23, s90, v172, v23 bitop3:0xbe\n"
  "v_bitop3_b32 v7, s88, v175, v7 bitop3:0xbe\n"
  "v_bitop3_b32 v23, s90, v174, v23 bitop3:0xbe\n"
  "s_waitcnt vmcnt(6)\n"
  "v_bitop3_b32 v7, s88, v177, v7 bitop3:0xbe\n"
  "v_bitop3_b32 v23, s90, v176, v23 bitop3:0xbe\n"
  "v_bitop3_b32 v7, s88, v179, v7 bitop3:0xbe\n"
  "v_bitop3_b32 v23, s90, v178, v23 bitop3:0xbe\n"
  "v_lshl_or_b32 v7, v23, 24, v7\n"
  "v_cmp_ne_u32 vcc, 0, v7\n"
  "s_cbranch_vccz .Lhave_3\n"
  "s_mov_b32 s94, 0x40000\n"
  ".Lpoll_4:\n"
  "buffer_load_dwordx4 v[184:187], v208, s[4:7], s96 offen sc1\n"
  "buffer_load_dwordx4 v[180:183], v15, s[4:7], s59 offen sc1\n"
  "buffer_load_dwordx4 v[164:167], v10, s[4:7], s57 offen sc1\n"
  "buffer_load_dwordx4 v[168:171], v10, s[4:7], s57 offen offset:2048 sc1\n"
  "buffer_load_dwordx4 v[172:175], v10, s[4:7], s58 offen sc1\n"
  "buffer_load_dwordx4 v[176:179], v10, s[4:7], s58 offen offset:2048 sc1\n"
  "s_waitcnt vmcnt(5)\n"
  "v_xor_b32 v7, s88, v185\n"
  "v_xor_b32 v23, s90, v184\n"
  "v_bitop3_b32 v7, s88, v187, v7 bitop3:0xbe\n"
  "v_bitop3_b32 v23, s90, v186, v23 bitop3:0xbe\n"
  "s_waitcnt vmcnt(4)\n"
  "v_bitop3_b32 v7, s88, v181, v7 bitop3:0xbe\n"
  "v_bitop3_b32 v23, s90, v180, v23 bitop3:0xbe\n"
  "v_bitop3_b32 v7, s88, v183, v7 bitop3:0xbe\n"
  "v_bitop3_b32 v23, s90, v182, v23 bitop3:0xbe\n"
  "s_waitcnt vmcnt(3)\n"
  "v_bitop3_b32 v7, s88, v165, v7 bitop3:0xbe\n"
  "v_bitop3_b32 v23, s90, v164, v23 bitop3:0xbe\n"
  "v_bitop3_b32 v7, s88, v167, v7 bitop3:0xbe\n"
  "v_bitop3_b32 v23, s90, v166, v23 bitop3:0xbe\n"
  "s_waitcnt vmcnt(2)\n"
  "v_bitop3_b32 v7, s88, v169, v7 bitop3:0xbe\n"
  "v_bitop3_b32 v23, s90, v168, v23 bitop3:0xbe\n"
  "v_bitop3_b32 v7, s88, v171, v7 bitop3:0xbe\n"
  "v_bitop3_b32 v23, s90, v170, v23 bitop3:0xbe\n"
  "s_waitcnt vmcnt(1)\n"
  "v_bitop3_b32 v7, s88, v173, v7 bitop3:0xbe\n"
  "v_bitop3_b32 v23, s90, v172, v23 bitop3:0xbe\n"
  "v_bitop3_b32 v7, s88, v175, v7 bitop3:0xbe\n"
  "v_bitop3_b32 v23, s90, v174, v23 bitop3:0xbe\n"
  "s_waitcnt vmcnt(0)\n"
  "v_bitop3_b32 v7, s88, v177, v7 bitop3:0xbe\n"
  "v_bitop3_b32 v23, s90, v176, v23 bitop3:0xbe\n"
  "v_bitop3_b32 v7, s88, v179, v7 bitop3:0xbe\n"
  "v_bitop3_b32 v23, s90, v178, v23 bitop3:0xbe\n"
  "v_lshl_or_b32 v7, v23, 24, v7\n"
  "v_cmp_ne_u32 vcc, 0, v7\n"
  "s_cbranch_vccz .Ldone_5\n"
  "s_sleep 2\n"
  "s_sub_u32 s94, s94, 1\n"
  "s_cbranch_scc0 .Lpoll_4\n"
  ".Ldone_5:\n"
  ".Lhave_3:\n"
  "v_and_b32 v164, s3, v164\n"
  "v_and_b32 v165, s3, v166\n"
  "v_and_b32 v168, s3, v168\n"
  "v_and_b32 v169, s3, v170\n"
  "v_and_b32 v172, s3, v172\n"
  "v_and_b32 v173, s3, v174\n"
  "v_and_b32 v176, s3, v176\n"
  "v_and_b32 v177, s3, v178\n"
  "v_pk_add_f32 v[164:165], v[164:165], v[168:169]\n"
  "v_pk_add_f32 v[172:173], v[172:173], v[176:177]\n"
  "v_and_b32 v180, s3, v180\n"
  "v_and_b32 v181, s3, v182\n"
  "v_pk_add_f32 v[164:165], v[164:165], v[172:173]\n"
  "v_mul_f32 v180, -v180, v181\n"
  "s_nop 0\n"
  "v_permlane32_swap_b32 v164, v165\n"
  "v_pk_fma_f32 v[188:189], v[188:189], v[180:181], v[180:181] op_sel:[0,1,0] op_sel_hi:[1,1,0]\n"
  "v_add_f32 v164, v164, v165\n"
  "v_pk_fma_f32 v[190:191], v[190:191], v[180:181], v[180:181] op_sel:[0,1,0] op_sel_hi:[1,1,0]\n"
  "v_mov_b32 v165, v164\n"
  "v_pk_fma_f32 v[192:193], v[192:193], v[180:181], v[180:181] op_sel:[0,1,0] op_sel_hi:[1,1,0]\n"
  "s_nop 0\n"
  "v_permlane16_swap_b32 v164, v165\n"
  "v_pk_fma_f32 v[194:195], v[194:195], v[180:181], v[180:181] op_sel:[0,1,0] op_sel_hi:[1,1,0]\n"
  "v_add_f32 v164, v164, v165\n"
  "v_pk_fma_f32 v[196:197], v[196:197], v[180:181], v[180:181] op_sel:[0,1,0] op_sel_hi:[1,1,0]\n"
  "v_cvt_pk_bf16_f32 v164, 0, v164\n"
  "v_pk_fma_f32 v[198:199], v[198:199], v[180:181], v[180:181] op_sel:[0,1,0] op_sel_hi:[1,1,0]\n"
  "v_add_f32 v164, s60, v164\n"
  "v_pk_fma_f32 v[200:201], v[200:201], v[180:181], v[180:181] op_sel:[0,1,0] op_sel_hi:[1,1,0]\n"
  "v_cvt_pk_bf16_f32 v164, 0, v164\n"
  "v_pk_fma_f32 v[202:203], v[202:203], v[180:181], v[180:181] op_sel:[0,1,0] op_sel_hi:[1,1,0]\n"
  "ds_write_b32 v206, v164\n"
  "s_waitcnt lgkmcnt(0)\n"
  "s_barrier\n"
  "ds_read_b128 v[128:131], v207 offset:0\n"
  "ds_read_b128 v[132:135], v207 offset:256\n"
  "ds_read_b128 v[136:139], v207 offset:16\n"
  "ds_read_b128 v[140:143], v207 offset:272\n"
  "ds_read_b128 v[144:147], v207 offset:128\n"
  "ds_read_b128 v[148:151], v207 offset:384\n"
  "ds_read_b128 v[152:155], v207 offset:144\n"
  "ds_read_b128 v[156:159], v207 offset:400\n"
  "v_mov_b32_dpp v185, v184 quad_perm:[1,0,3,2] row_mask:0xf bank_mask:0xf\n"
  "v_cmp_gt_u32 vcc, v184, v185\n"
  "s_and_b32 s36, vcc_lo, 0x5555\n"
  "s_bcnt1_i32_b32 s36, s36\n"
  "s_cmp_gt_u32 s36, 4\n"
  "s_cselect_b32 s96, 0x2000, 0\n"
  "s_xor_b32 s37, s96, s62\n"
  "s_add_u32 s16, s4, s37\n"
  "s_addc_u32 s17, s5, 0\n"
  "s_waitcnt lgkmcnt(6)\n"
  "v_pk_fma_f32 v[188:189], v[188:189], v[132:133], v[128:129]\n"
  "v_pk_fma_f32 v[190:191], v[190:191], v[134:135], v[130:131]\n"
  "v_cvt_pk_bf16_f32 v80, v188, v189\n"
  "v_cvt_pk_bf16_f32 v81, v190, v191\n"
  "s_waitcnt lgkmcnt(4)\n"
  "v_pk_fma_f32 v[192:193], v[192:193], v[140:141], v[136:137]\n"
  "v_pk_fma_f32 v[194:195], v[194:195], v[142:143], v[138:139]\n"
  "v_cvt_pk_bf16_f32 v82, v192, v193\n"
  "v_cvt_pk_bf16_f32 v83, v194, v195\n"
  "buffer_store_dwordx4 v[80:83], v1, s[16:19], s61 offen sc1\n"
  "s_waitcnt lgkmcnt(2)\n"
  "v_pk_fma_f32 v[196:197], v[196:197], v[148:149], v[144:145]\n"
  "v_pk_fma_f32 v[198:199], v[198:199], v[150:151], v[146:147]\n"
  "v_cvt_pk_bf16_f32 v84, v196, v197\n"
  "v_cvt_pk_bf16_f32 v85, v198, v199\n"
  "s_waitcnt lgkmcnt(0)\n"
  "v_pk_fma_f32 v[200:201], v[200:201], v[156:157], v[152:153]\n"
  "v_pk_fma_f32 v[202:203], v[202:203], v[158:159], v[154:155]\n"
  "v_cvt_pk_bf16_f32 v86, v200, v201\n"
  "v_cvt_pk_bf16_f32 v87, v202, v203\n"
  "buffer_store_dwordx4 v[84:87], v1, s[16:19], s61 offen offset:1024 sc1\n"
  "s_barrier\n"
  "s_load_dwordx8 s[8:15], s[98:99], 0xa0\n"
  "s_load_dwordx16 s[48:63], s[98:99], 0xc0\n"
  "s_load_dwordx8 s[24:31], s[98:99], 0x140\n"
  "s_add_u32 m0, s64, s65\n"
  "s_mul_i32 s95, s64, 24\n"
  "s_add_u32 s95, s95, s66\n"
  "buffer_load_dwordx4 v12, s[20:23], s95 offen lds\n"
  "s_addk_i32 m0, 1024\n"
  "s_addk_i32 s64, 8192\n"
  "buffer_load_dwordx4 v12, s[20:23], s95 offen offset:1024 lds\n"
  "s_and_b32 s64, s64, 0xffff\n"
  "s_add_u32 m0, s64, s65\n"
  "s_mul_i32 s95, s64, 24\n"
  "s_add_u32 s95, s95, s66\n"
  "buffer_load_dwordx4 v12, s[20:23], s95 offen lds\n"
  "s_addk_i32 m0, 1024\n"
  "s_addk_i32 s64, 8192\n"
  "buffer_load_dwordx4 v12, s[20:23], s95 offen offset:1024 lds\n"
  "s_and_b32 s64, s64, 0xffff\n"
  "v_lshlrev_b32 v10, 6, v2\n"
  "v_xor_b32 v19, 64, v210\n"
  "v_bfe_i32 v3, v2, 4, 1\n"
  "s_waitcnt lgkmcnt(0)\n"
  "v_and_b32 v3, s63, v3\n"
  "v_add3_u32 v114, v208, v3, s62\n"
  "v_add_u32 v104, s57, v104\n"
  "v_add_u32 v105, s57, v105\n"
  "v_add_u32 v106, s57, v106\n"
  "s_xor_b32 s55, s55, s96\n"
  "v_xor_b32 v108, 32, v96\n"
  "s_cmp_lt_u32 s86, 29\n"
  "s_cselect_b32 s36, 0x400, 0\n"
  "v_bfe_i32 v3, v2, 5, 1\n"
  "v_and_b32 v3, s36, v3\n"
  "v_sub_u32 v112, v107, v3\n"
  "s_addk_i32 s36, 1024\n"
  "v_add_u32 v113, s36, v112\n"
  "s_waitcnt vmcnt(4)\n"
  "s_barrier\n"
  "s_mov_b32 exec_lo, 0xffff\n"
  "s_mov_b32 exec_hi, 0\n"
  "buffer_store_dwordx4 v[4:7], v14, s[16:19], s75 offen sc1\n"
  "s_mov_b64 exec, -1\n"
  "ds_read_b64_tr_b16 v[160:161], v210 offset:0\n"
  "ds_read_b64_tr_b16 v[162:163], v19 offset:512\n"
  "ds_read_b64_tr_b16 v[224:225], v211 offset:0\n"
  "ds_read_b64_tr_b16 v[226:227], v211 offset:128\n"
  "ds_read_b128 v[36:39], v102 offset:0\n"
  "ds_read_b128 v[40:43], v102 offset:4096\n"
  "s_add_u32 m0, s64, s65\n"
  "s_mul_i32 s95, s64, 24\n"
  "s_add_u32 s95, s95, s66\n"
  "buffer_load_dwordx4 v12, s[20:23], s95 offen lds\n"
  "s_addk_i32 m0, 1024\n"
  "s_addk_i32 s64, 8192\n"
  "buffer_load_dwordx4 v12, s[20:23], s95 offen offset:1024 lds\n"
  "s_and_b32 s64, s64, 0xffff\n"
  "s_add_u32 m0, s64, s65\n"
  "s_mul_i32 s95, s64, 24\n"
  "s_add_u32 s95, s95, s66\n"
  "buffer_load_dwordx4 v12, s[20:23], s95 offen lds\n"
  "s_addk_i32 m0, 1024\n"
  "s_addk_i32 s64, 8192\n"
  "buffer_load_dwordx4 v12, s[20:23], s95 offen offset:1024 lds\n"
  "s_and_b32 s64, s64, 0xffff\n"
  "s_waitcnt lgkmcnt(4)\n"
  "buffer_store_dwordx4 v[160:163], v1, s[4:7], s76 offen sc1\n"
  "s_cmp_eq_u32 s80, 0\n"
  "s_cbranch_scc0 .Lskip_6\n"
  "s_mov_b64 exec, 0xffff\n"
  "v_mov_b64 v[18:19], 0\n"
  "buffer_atomic_or_x2 v[18:19], v10, s[16:19], s77 offen sc0 sc1\n"
  "s_mov_b64 exec, -1\n"
  ".Lskip_6:\n"
  "s_cmp_eq_u32 s80, 0\n"
  "s_cbranch_scc0 .Lskip_7\n"
  "s_mov_b64 exec, 0xffff\n"
  "s_waitcnt vmcnt(0)\n"
  "v_cmp_ne_u64 vcc, s[88:89], v[18:19]\n"
  "s_mov_b64 exec, -1\n"
  "s_cbranch_vccz .Lhave_8\n"
  "s_mov_b64 exec, 0xffff\n"
  "s_mov_b32 s94, 0x40000\n"
  ".Lpoll_9:\n"
  "v_mov_b64 v[18:19], 0\n"
  "buffer_atomic_or_x2 v[18:19], v10, s[16:19], s77 offen sc0 sc1\n"
  "s_waitcnt vmcnt(0)\n"
  "v_cmp_ne_u64 vcc, s[88:89], v[18:19]\n"
  "s_cbranch_vccz .Ldone_10\n"
  "s_sub_u32 s94, s94, 1\n"
  "s_cbranch_scc0 .Lpoll_9\n"
  ".Ldone_10:\n"
  "s_mov_b64 exec, -1\n"
  ".Lhave_8:\n"
  ".Lskip_7:\n"
  "s_waitcnt vmcnt(0)\n"
  "s_barrier\n"
  "buffer_load_dwordx4 a[0:3], v1, s[16:19], s78 offen sc1\n"
  "buffer_load_dwordx4 a[4:7], v1, s[16:19], s78 offen offset:1024 sc1\n"
  "s_cmp_eq_u32 s80, 3\n"
  "s_cbranch_scc0 .Lskip_11\n"
  "s_mov_b64 exec, 1\n"
  "buffer_store_dwordx2 v[4:5], off, s[4:7], s79 sc1\n"
  "s_mov_b64 exec, -1\n"
  ".Lskip_11:\n"
  "buffer_load_dwordx4 a[64:67], v11, s[16:19], s78 offen sc1\n"
  "v_bfe_i32 v3, v2, 2, 1\n"
  "s_waitcnt lgkmcnt(3)\n"
  "v_and_b32 v228, v3, v224\n"
  "v_and_b32 v229, v3, v225\n"
  "s_waitcnt lgkmcnt(2)\n"
  "v_and_b32 v230, v3, v226\n"
  "v_and_b32 v231, v3, v227\n"
  "v_xor_b32 v224, v228, v224\n"
  "v_xor_b32 v225, v229, v225\n"
  "v_xor_b32 v226, v230, v226\n"
  "buffer_load_dwordx4 a[68:71], v11, s[16:19], s78 offen offset:1024 sc1\n"
  "v_xor_b32 v227, v231, v227\n"
  "v_lshlrev_b32 v26, 16, v26\n"
  "v_lshlrev_b32 v27, 16, v27\n"
  "v_mul_f32 v52, 0xbfb8aa3b, v26\n"
  "v_mul_f32 v53, 0xbfb8aa3b, v27\n"
  "v_exp_f32 v52, v52\n"
  "v_exp_f32 v53, v53\n"
  "v_add_f32 v52, 1.0, v52\n"
  "buffer_load_dwordx4 a[8:11], v1, s[16:19], s78 offen offset:2048 sc1\n"
  "v_add_f32 v53, 1.0, v53\n"
  "v_rcp_f32 v52, v52\n"
  "v_rcp_f32 v53, v53\n"
  "v_mul_f32 v26, v26, v52\n"
  "v_mul_f32 v27, v27, v53\n"
  "v_cvt_pk_bf16_f32 v52, v26, 0\n"
  "v_cvt_pk_bf16_f32 v54, 0, v26\n"
  "v_cvt_pk_bf16_f32 v53, v27, 0\n"
  "buffer_load_dwordx4 a[12:15], v1, s[16:19], s78 offen offset:3072 sc1\n"
  "v_cvt_pk_bf16_f32 v55, 0, v27\n"
  "s_waitcnt lgkmcnt(1)\n"
  "v_dot2_f32_bf16 v56, v36, v52, 0\n"
  "v_dot2_f32_bf16 v57, v36, v54, 0\n"
  "v_dot2_f32_bf16 v60, v38, v52, 0\n"
  "v_dot2_f32_bf16 v61, v38, v54, 0\n"
  "v_dot2_f32_bf16 v58, v37, v52, 0\n"
  "v_dot2_f32_bf16 v59, v37, v54, 0\n"
  "v_dot2_f32_bf16 v62, v39, v52, 0\n"
  "buffer_load_dwordx4 a[72:75], v11, s[16:19], s78 offen offset:2048 sc1\n"
  "v_dot2_f32_bf16 v63, v39, v54, 0\n"
  "s_waitcnt lgkmcnt(0)\n"
  "v_dot2_f32_bf16 v56, v40, v53, v56\n"
  "v_dot2_f32_bf16 v57, v40, v55, v57\n"
  "v_dot2_f32_bf16 v60, v42, v53, v60\n"
  "v_dot2_f32_bf16 v61, v42, v55, v61\n"
  "v_dot2_f32_bf16 v58, v41, v53, v58\n"
  "v_dot2_f32_bf16 v59, v41, v55, v59\n"
  "v_dot2_f32_bf16 v62, v43, v53, v62\n"
  "buffer_load_dwordx4 a[76:79], v11, s[16:19], s78 offen offset:3072 sc1\n"
  "v_dot2_f32_bf16 v63, v43, v55, v63\n"
  "v_permlane32_swap_b32 v56, v60\n"
  "v_permlane32_swap_b32 v57, v61\n"
  "v_permlane32_swap_b32 v58, v62\n"
  "v_permlane32_swap_b32 v59, v63\n"
  "v_pk_add_f32 v[56:57], v[56:57], v[60:61]\n"
  "v_pk_add_f32 v[58:59], v[58:59], v[62:63]\n"
  "s_nop 1\n"
  "v_permlane16_swap_b32 v56, v58\n"
  "s_addk_i32 s78, 4096\n"
  "buffer_load_dwordx4 a[16:19], v1, s[16:19], s78 offen sc1\n"
  "v_permlane16_swap_b32 v57, v59\n"
  "v_pk_add_f32 v[56:57], v[56:57], v[58:59]\n"
  "s_nop 1\n"
  "v_add_f32_dpp v56, v56, v56 row_ror:2 row_mask:0xf bank_mask:0xf\n"
  "v_add_f32_dpp v57, v57, v57 row_ror:2 row_mask:0xf bank_mask:0xf\n"
  "s_nop 0\n"
  "v_add_f32_dpp v56, v56, v56 row_ror:4 row_mask:0xf bank_mask:0xf\n"
  "v_add_f32_dpp v57, v57, v57 row_ror:4 row_mask:0xf bank_mask:0xf\n"
  "s_nop 0\n"
  "v_add_f32_dpp v56, v56, v56 row_ror:8 row_mask:0xf bank_mask:0xf\n"
  "v_add_f32_dpp v57, v57, v57 row_ror:8 row_mask:0xf bank_mask:0xf\n"
  "buffer_load_dwordx4 a[20:23], v1, s[16:19], s78 offen offset:1024 sc1\n"
  "v_and_or_b32 v28, v56, s3, v24\n"
  "v_and_or_b32 v30, v57, s3, v24\n"
  "ds_read_b64_tr_b16 v[128:129], v96 offset:0\n"
  "ds_read_b64_tr_b16 v[130:131], v96 offset:256\n"
  "ds_read_b64_tr_b16 v[132:133], v96 offset:4096\n"
  "ds_read_b64_tr_b16 v[134:135], v96 offset:4352\n"
  "ds_read_b64_tr_b16 v[136:137], v108 offset:0\n"
  "ds_read_b64_tr_b16 v[138:139], v108 offset:256\n"
  "buffer_load_dwordx4 a[80:83], v11, s[16:19], s78 offen sc1\n"
  "ds_read_b64_tr_b16 v[140:141], v108 offset:4096\n"
  "ds_read_b64_tr_b16 v[142:143], v108 offset:4352\n"
  "ds_read_b64_tr_b16 v[144:145], v96 offset:2048\n"
  "ds_read_b64_tr_b16 v[146:147], v96 offset:2304\n"
  "ds_read_b64_tr_b16 v[148:149], v96 offset:6144\n"
  "ds_read_b64_tr_b16 v[150:151], v96 offset:6400\n"
  "ds_read_b64_tr_b16 v[152:153], v108 offset:2048\n"
  "ds_read_b64_tr_b16 v[154:155], v108 offset:2304\n"
  "buffer_load_dwordx4 a[84:87], v11, s[16:19], s78 offen offset:1024 sc1\n"
  "ds_read_b64_tr_b16 v[156:157], v108 offset:6144\n"
  "ds_read_b64_tr_b16 v[158:159], v108 offset:6400\n"
  "s_waitcnt vmcnt(10) lgkmcnt(0)\n"
  "v_mfma_f32_16x16x32_bf16 v[64:67], v[128:131], a[0:3], 0\n"
  "ds_read_b64_tr_b16 v[160:161], v96 offset:8192\n"
  "ds_read_b64_tr_b16 v[162:163], v96 offset:8448\n"
  "v_mfma_f32_16x16x32_bf16 v[64:67], v[132:135], a[4:7], v[64:67]\n"
  "ds_read_b64_tr_b16 v[164:165], v96 offset:12288\n"
  "ds_read_b64_tr_b16 v[166:167], v96 offset:12544\n"
  "v_mfma_f32_16x16x32_bf16 v[68:71], v[136:139], a[0:3], 0\n"
  "ds_read_b64_tr_b16 v[168:169], v108 offset:8192\n"
  "ds_read_b64_tr_b16 v[170:171], v108 offset:8448\n"
  "v_mfma_f32_16x16x32_bf16 v[68:71], v[140:143], a[4:7], v[68:71]\n"
  "ds_read_b64_tr_b16 v[172:173], v108 offset:12288\n"
  "ds_read_b64_tr_b16 v[174:175], v108 offset:12544\n"
  "v_mfma_f32_16x16x32_bf16 v[80:83], v[144:147], a[0:3], 0\n"
  "ds_read_b64_tr_b16 v[176:177], v96 offset:10240\n"
  "ds_read_b64_tr_b16 v[178:179], v96 offset:10496\n"
  "v_mfma_f32_16x16x32_bf16 v[80:83], v[148:151], a[4:7], v[80:83]\n"
  "ds_read_b64_tr_b16 v[180:181], v96 offset:14336\n"
  "ds_read_b64_tr_b16 v[182:183], v96 offset:14592\n"
  "v_mfma_f32_16x16x32_bf16 v[84:87], v[152:155], a[0:3], 0\n"
  "ds_read_b64_tr_b16 v[184:185], v108 offset:10240\n"
  "ds_read_b64_tr_b16 v[186:187], v108 offset:10496\n"
  "v_mfma_f32_16x16x32_bf16 v[84:87], v[156:159], a[4:7], v[84:87]\n"
  "ds_read_b64_tr_b16 v[188:189], v108 offset:14336\n"
  "s_waitcnt vmcnt(8)\n"
  "v_mfma_f32_16x16x32_bf16 v[72:75], v[128:131], a[64:67], 0\n"
  "ds_read_b64_tr_b16 v[190:191], v108 offset:14592\n"
  "v_mfma_f32_16x16x32_bf16 v[72:75], v[132:135], a[68:71], v[72:75]\n"
  "buffer_load_dwordx4 a[24:27], v1, s[16:19], s78 offen offset:2048 sc1\n"
  "v_mfma_f32_16x16x32_bf16 v[76:79], v[136:139], a[64:67], 0\n"
  "v_mfma_f32_16x16x32_bf16 v[76:79], v[140:143], a[68:71], v[76:79]\n"
  "buffer_load_dwordx4 a[28:31], v1, s[16:19], s78 offen offset:3072 sc1\n"
  "v_mfma_f32_16x16x32_bf16 v[88:91], v[144:147], a[64:67], 0\n"
  "s_mov_b32 exec_lo, 0x30003\n"
  "s_mov_b32 exec_hi, 0x30003\n"
  "buffer_store_dwordx4 v[28:31], v34, s[4:7], s48 offen sc1\n"
  "s_mov_b64 exec, -1\n"
  "v_mfma_f32_16x16x32_bf16 v[88:91], v[148:151], a[68:71], v[88:91]\n"
  "buffer_load_dwordx4 a[88:91], v11, s[16:19], s78 offen offset:2048 sc1\n"
  "v_mfma_f32_16x16x32_bf16 v[92:95], v[152:155], a[64:67], 0\n"
  "buffer_load_dwordx4 a[92:95], v11, s[16:19], s78 offen offset:3072 sc1\n"
  "v_mfma_f32_16x16x32_bf16 v[92:95], v[156:159], a[68:71], v[92:95]\n"
  "s_waitcnt vmcnt(11) lgkmcnt(0)\n"
  "v_mfma_f32_16x16x32_bf16 v[64:67], v[160:163], a[8:11], v[64:67]\n"
  "ds_read_b64_tr_b16 v[128:129], v96 offset:16384\n"
  "ds_read_b64_tr_b16 v[130:131], v96 offset:16640\n"
  "v_mfma_f32_16x16x32_bf16 v[64:67], v[164:167], a[12:15], v[64:67]\n"
  "ds_read_b64_tr_b16 v[132:133], v96 offset:20480\n"
  "ds_read_b64_tr_b16 v[134:135], v96 offset:20736\n"
  "v_mfma_f32_16x16x32_bf16 v[68:71], v[168:171], a[8:11], v[68:71]\n"
  "ds_read_b64_tr_b16 v[136:137], v108 offset:16384\n"
  "ds_read_b64_tr_b16 v[138:139], v108 offset:16640\n"
  "v_mfma_f32_16x16x32_bf16 v[68:71], v[172:175], a[12:15], v[68:71]\n"
  "ds_read_b64_tr_b16 v[140:141], v108 offset:20480\n"
  "ds_read_b64_tr_b16 v[142:143], v108 offset:20736\n"
  "v_mfma_f32_16x16x32_bf16 v[80:83], v[176:179], a[8:11], v[80:83]\n"
  "ds_read_b64_tr_b16 v[144:145], v96 offset:18432\n"
  "ds_read_b64_tr_b16 v[146:147], v96 offset:18688\n"
  "v_mfma_f32_16x16x32_bf16 v[80:83], v[180:183], a[12:15], v[80:83]\n"
  "ds_read_b64_tr_b16 v[148:149], v96 offset:22528\n"
  "ds_read_b64_tr_b16 v[150:151], v96 offset:22784\n"
  "v_mfma_f32_16x16x32_bf16 v[84:87], v[184:187], a[8:11], v[84:87]\n"
  "ds_read_b64_tr_b16 v[152:153], v108 offset:18432\n"
  "ds_read_b64_tr_b16 v[154:155], v108 offset:18688\n"
  "v_mfma_f32_16x16x32_bf16 v[84:87], v[188:191], a[12:15], v[84:87]\n"
  "ds_read_b64_tr_b16 v[156:157], v108 offset:22528\n"
  "s_waitcnt vmcnt(9)\n"
  "v_mfma_f32_16x16x32_bf16 v[72:75], v[160:163], a[72:75], v[72:75]\n"
  "ds_read_b64_tr_b16 v[158:159], v108 offset:22784\n"
  "v_mfma_f32_16x16x32_bf16 v[72:75], v[164:167], a[76:79], v[72:75]\n"
  "s_addk_i32 s78, 12288\n"
  "buffer_load_dwordx4 a[32:35], v1, s[16:19], s78 offen sc1\n"
  "v_mfma_f32_16x16x32_bf16 v[76:79], v[168:171], a[72:75], v[76:79]\n"
  "v_mfma_f32_16x16x32_bf16 v[76:79], v[172:175], a[76:79], v[76:79]\n"
  "buffer_load_dwordx4 a[36:39], v1, s[16:19], s78 offen offset:1024 sc1\n"
  "v_mfma_f32_16x16x32_bf16 v[88:91], v[176:179], a[72:75], v[88:91]\n"
  "v_mfma_f32_16x16x32_bf16 v[88:91], v[180:183], a[76:79], v[88:91]\n"
  "buffer_load_dwordx4 a[96:99], v11, s[16:19], s78 offen sc1\n"
  "v_mfma_f32_16x16x32_bf16 v[92:95], v[184:187], a[72:75], v[92:95]\n"
  "buffer_load_dwordx4 a[100:103], v11, s[16:19], s78 offen offset:1024 sc1\n"
  "v_mfma_f32_16x16x32_bf16 v[92:95], v[188:191], a[76:79], v[92:95]\n"
  "s_waitcnt vmcnt(11) lgkmcnt(0)\n"
  "v_mfma_f32_16x16x32_bf16 v[64:67], v[128:131], a[16:19], v[64:67]\n"
  "ds_read_b64_tr_b16 v[160:161], v96 offset:24576\n"
  "ds_read_b64_tr_b16 v[162:163], v96 offset:24832\n"
  "v_mfma_f32_16x16x32_bf16 v[64:67], v[132:135], a[20:23], v[64:67]\n"
  "ds_read_b64_tr_b16 v[164:165], v96 offset:28672\n"
  "ds_read_b64_tr_b16 v[166:167], v96 offset:28928\n"
  "v_mfma_f32_16x16x32_bf16 v[68:71], v[136:139], a[16:19], v[68:71]\n"
  "ds_read_b64_tr_b16 v[168:169], v108 offset:24576\n"
  "ds_read_b64_tr_b16 v[170:171], v108 offset:24832\n"
  "v_mfma_f32_16x16x32_bf16 v[68:71], v[140:143], a[20:23], v[68:71]\n"
  "ds_read_b64_tr_b16 v[172:173], v108 offset:28672\n"
  "ds_read_b64_tr_b16 v[174:175], v108 offset:28928\n"
  "v_mfma_f32_16x16x32_bf16 v[80:83], v[144:147], a[16:19], v[80:83]\n"
  "ds_read_b64_tr_b16 v[176:177], v96 offset:26624\n"
  "ds_read_b64_tr_b16 v[178:179], v96 offset:26880\n"
  "v_mfma_f32_16x16x32_bf16 v[80:83], v[148:151], a[20:23], v[80:83]\n"
  "ds_read_b64_tr_b16 v[180:181], v96 offset:30720\n"
  "ds_read_b64_tr_b16 v[182:183], v96 offset:30976\n"
  "v_mfma_f32_16x16x32_bf16 v[84:87], v[152:155], a[16:19], v[84:87]\n"
  "ds_read_b64_tr_b16 v[184:185], v108 offset:26624\n"
  "ds_read_b64_tr_b16 v[186:187], v108 offset:26880\n"
  "v_mfma_f32_16x16x32_bf16 v[84:87], v[156:159], a[20:23], v[84:87]\n"
  "ds_read_b64_tr_b16 v[188:189], v108 offset:30720\n"
  "s_waitcnt vmcnt(9)\n"
  "v_mfma_f32_16x16x32_bf16 v[72:75], v[128:131], a[80:83], v[72:75]\n"
  "ds_read_b64_tr_b16 v[190:191], v108 offset:30976\n"
  "v_mfma_f32_16x16x32_bf16 v[72:75], v[132:135], a[84:87], v[72:75]\n"
  "buffer_load_dwordx4 a[40:43], v1, s[16:19], s78 offen offset:2048 sc1\n"
  "v_mfma_f32_16x16x32_bf16 v[76:79], v[136:139], a[80:83], v[76:79]\n"
  "v_mfma_f32_16x16x32_bf16 v[76:79], v[140:143], a[84:87], v[76:79]\n"
  "buffer_load_dwordx4 a[44:47], v1, s[16:19], s78 offen offset:3072 sc1\n"
  "v_mfma_f32_16x16x32_bf16 v[88:91], v[144:147], a[80:83], v[88:91]\n"
  "buffer_load_dwordx2 v[110:111], v114, s[4:7], 0 offen sc1\n"
  "v_mfma_f32_16x16x32_bf16 v[88:91], v[148:151], a[84:87], v[88:91]\n"
  "buffer_load_dwordx4 a[104:107], v11, s[16:19], s78 offen offset:2048 sc1\n"
  "v_mfma_f32_16x16x32_bf16 v[92:95], v[152:155], a[80:83], v[92:95]\n"
  "buffer_load_dwordx4 a[108:111], v11, s[16:19], s78 offen offset:3072 sc1\n"
  "v_mfma_f32_16x16x32_bf16 v[92:95], v[156:159], a[84:87], v[92:95]\n"
  "s_waitcnt vmcnt(12) lgkmcnt(0)\n"
  "v_mfma_f32_16x16x32_bf16 v[64:67], v[160:163], a[24:27], v[64:67]\n"
  "ds_read_b64_tr_b16 v[128:129], v96 offset:32768\n"
  "ds_read_b64_tr_b16 v[130:131], v96 offset:33024\n"
  "v_mfma_f32_16x16x32_bf16 v[64:67], v[164:167], a[28:31], v[64:67]\n"
  "ds_read_b64_tr_b16 v[132:133], v96 offset:36864\n"
  "ds_read_b64_tr_b16 v[134:135], v96 offset:37120\n"
  "v_mfma_f32_16x16x32_bf16 v[68:71], v[168:171], a[24:27], v[68:71]\n"
  "ds_read_b64_tr_b16 v[136:137], v108 offset:32768\n"
  "ds_read_b64_tr_b16 v[138:139], v108 offset:33024\n"
  "v_mfma_f32_16x16x32_bf16 v[68:71], v[172:175], a[28:31], v[68:71]\n"
  "ds_read_b64_tr_b16 v[140:141], v108 offset:36864\n"
  "ds_read_b64_tr_b16 v[142:143], v108 offset:37120\n"
  "v_mfma_f32_16x16x32_bf16 v[80:83], v[176:179], a[24:27], v[80:83]\n"
  "ds_read_b64_tr_b16 v[144:145], v96 offset:34816\n"
  "ds_read_b64_tr_b16 v[146:147], v96 offset:35072\n"
  "v_mfma_f32_16x16x32_bf16 v[80:83], v[180:183], a[28:31], v[80:83]\n"
  "ds_read_b64_tr_b16 v[148:149], v96 offset:38912\n"
  "ds_read_b64_tr_b16 v[150:151], v96 offset:39168\n"
  "v_mfma_f32_16x16x32_bf16 v[84:87], v[184:187], a[24:27], v[84:87]\n"
  "ds_read_b64_tr_b16 v[152:153], v108 offset:34816\n"
  "ds_read_b64_tr_b16 v[154:155], v108 offset:35072\n"
  "v_mfma_f32_16x16x32_bf16 v[84:87], v[188:191], a[28:31], v[84:87]\n"
  "ds_read_b64_tr_b16 v[156:157], v108 offset:38912\n"
  "s_waitcnt vmcnt(9)\n"
  "v_mfma_f32_16x16x32_bf16 v[72:75], v[160:163], a[88:91], v[72:75]\n"
  "ds_read_b64_tr_b16 v[158:159], v108 offset:39168\n"
  "v_mfma_f32_16x16x32_bf16 v[72:75], v[164:167], a[92:95], v[72:75]\n"
  "s_addk_i32 s78, 4096\n"
  "buffer_load_dwordx4 a[48:51], v1, s[16:19], s78 offen sc1\n"
  "v_mfma_f32_16x16x32_bf16 v[76:79], v[168:171], a[88:91], v[76:79]\n"
  "v_mfma_f32_16x16x32_bf16 v[76:79], v[172:175], a[92:95], v[76:79]\n"
  "buffer_load_dwordx4 a[52:55], v1, s[16:19], s78 offen offset:1024 sc1\n"
  "v_mfma_f32_16x16x32_bf16 v[88:91], v[176:179], a[88:91], v[88:91]\n"
  "v_mfma_f32_16x16x32_bf16 v[88:91], v[180:183], a[92:95], v[88:91]\n"
  "buffer_load_dwordx4 a[112:115], v11, s[16:19], s78 offen sc1\n"
  "v_mfma_f32_16x16x32_bf16 v[92:95], v[184:187], a[88:91], v[92:95]\n"
  "buffer_load_dwordx4 a[116:119], v11, s[16:19], s78 offen offset:1024 sc1\n"
  "v_mfma_f32_16x16x32_bf16 v[92:95], v[188:191], a[92:95], v[92:95]\n"
  "s_waitcnt vmcnt(11) lgkmcnt(0)\n"
  "v_mfma_f32_16x16x32_bf16 v[64:67], v[128:131], a[32:35], v[64:67]\n"
  "ds_read_b64_tr_b16 v[160:161], v96 offset:40960\n"
  "ds_read_b64_tr_b16 v[162:163], v96 offset:41216\n"
  "v_mfma_f32_16x16x32_bf16 v[64:67], v[132:135], a[36:39], v[64:67]\n"
  "ds_read_b64_tr_b16 v[164:165], v96 offset:45056\n"
  "ds_read_b64_tr_b16 v[166:167], v96 offset:45312\n"
  "v_mfma_f32_16x16x32_bf16 v[68:71], v[136:139], a[32:35], v[68:71]\n"
  "ds_read_b64_tr_b16 v[168:169], v108 offset:40960\n"
  "ds_read_b64_tr_b16 v[170:171], v108 offset:41216\n"
  "v_mfma_f32_16x16x32_bf16 v[68:71], v[140:143], a[36:39], v[68:71]\n"
  "ds_read_b64_tr_b16 v[172:173], v108 offset:45056\n"
  "ds_read_b64_tr_b16 v[174:175], v108 offset:45312\n"
  "v_mfma_f32_16x16x32_bf16 v[80:83], v[144:147], a[32:35], v[80:83]\n"
  "ds_read_b64_tr_b16 v[176:177], v96 offset:43008\n"
  "ds_read_b64_tr_b16 v[178:179], v96 offset:43264\n"
  "v_mfma_f32_16x16x32_bf16 v[80:83], v[148:151], a[36:39], v[80:83]\n"
  "ds_read_b64_tr_b16 v[180:181], v96 offset:47104\n"
  "ds_read_b64_tr_b16 v[182:183], v96 offset:47360\n"
  "v_mfma_f32_16x16x32_bf16 v[84:87], v[152:155], a[32:35], v[84:87]\n"
  "ds_read_b64_tr_b16 v[184:185], v108 offset:43008\n"
  "ds_read_b64_tr_b16 v[186:187], v108 offset:43264\n"
  "v_mfma_f32_16x16x32_bf16 v[84:87], v[156:159], a[36:39], v[84:87]\n"
  "ds_read_b64_tr_b16 v[188:189], v108 offset:47104\n"
  "s_waitcnt vmcnt(9)\n"
  "v_mfma_f32_16x16x32_bf16 v[72:75], v[128:131], a[96:99], v[72:75]\n"
  "ds_read_b64_tr_b16 v[190:191], v108 offset:47360\n"
  "v_mfma_f32_16x16x32_bf16 v[72:75], v[132:135], a[100:103], v[72:75]\n"
  "buffer_load_dwordx4 a[56:59], v1, s[16:19], s78 offen offset:2048 sc1\n"
  "v_mfma_f32_16x16x32_bf16 v[76:79], v[136:139], a[96:99], v[76:79]\n"
  "v_mfma_f32_16x16x32_bf16 v[76:79], v[140:143], a[100:103], v[76:79]\n"
  "buffer_load_dwordx4 a[60:63], v1, s[16:19], s78 offen offset:3072 sc1\n"
  "v_mfma_f32_16x16x32_bf16 v[88:91], v[144:147], a[96:99], v[88:91]\n"
  "v_mfma_f32_16x16x32_bf16 v[88:91], v[148:151], a[100:103], v[88:91]\n"
  "buffer_load_dwordx4 a[120:123], v11, s[16:19], s78 offen offset:2048 sc1\n"
  "v_mfma_f32_16x16x32_bf16 v[92:95], v[152:155], a[96:99], v[92:95]\n"
  "buffer_load_dwordx4 a[124:127], v11, s[16:19], s78 offen offset:3072 sc1\n"
  "v_mfma_f32_16x16x32_bf16 v[92:95], v[156:159], a[100:103], v[92:95]\n"
  "s_waitcnt vmcnt(10)\n"
  "v_cmp_ne_u64 vcc, s[88:89], v[110:111]\n"
  "s_cbranch_vccz .Lhave_12\n"
  "s_mov_b32 s94, 0x40000\n"
  ".Lpoll_13:\n"
  "buffer_load_dwordx2 v[110:111], v114, s[4:7], 0 offen sc1\n"
  "s_waitcnt vmcnt(0)\n"
  "v_cmp_ne_u64 vcc, s[88:89], v[110:111]\n"
  "s_cbranch_vccz .Ldone_14\n"
  "s_sleep 8\n"
  "s_sub_u32 s94, s94, 1\n"
  "s_cbranch_scc0 .Lpoll_13\n"
  ".Ldone_14:\n"
  ".Lhave_12:\n"
  "s_waitcnt lgkmcnt(0)\n"
  "v_mfma_f32_16x16x32_bf16 v[64:67], v[160:163], a[40:43], v[64:67]\n"
  "ds_read_b64_tr_b16 v[128:129], v96 offset:49152\n"
  "ds_read_b64_tr_b16 v[130:131], v96 offset:49408\n"
  "v_mfma_f32_16x16x32_bf16 v[64:67], v[164:167], a[44:47], v[64:67]\n"
  "ds_read_b64_tr_b16 v[132:133], v96 offset:53248\n"
  "ds_read_b64_tr_b16 v[134:135], v96 offset:53504\n"
  "v_mfma_f32_16x16x32_bf16 v[68:71], v[168:171], a[40:43], v[68:71]\n"
  "ds_read_b64_tr_b16 v[136:137], v108 offset:49152\n"
  "ds_read_b64_tr_b16 v[138:139], v108 offset:49408\n"
  "v_mfma_f32_16x16x32_bf16 v[68:71], v[172:175], a[44:47], v[68:71]\n"
  "ds_read_b64_tr_b16 v[140:141], v108 offset:53248\n"
  "ds_read_b64_tr_b16 v[142:143], v108 offset:53504\n"
  "v_mfma_f32_16x16x32_bf16 v[80:83], v[176:179], a[40:43], v[80:83]\n"
  "ds_read_b64_tr_b16 v[144:145], v96 offset:51200\n"
  "ds_read_b64_tr_b16 v[146:147], v96 offset:51456\n"
  "v_mfma_f32_16x16x32_bf16 v[80:83], v[180:183], a[44:47], v[80:83]\n"
  "ds_read_b64_tr_b16 v[148:149], v96 offset:55296\n"
  "ds_read_b64_tr_b16 v[150:151], v96 offset:55552\n"
  "v_mfma_f32_16x16x32_bf16 v[84:87], v[184:187], a[40:43], v[84:87]\n"
  "ds_read_b64_tr_b16 v[152:153], v108 offset:51200\n"
  "ds_read_b64_tr_b16 v[154:155], v108 offset:51456\n"
  "v_mfma_f32_16x16x32_bf16 v[84:87], v[188:191], a[44:47], v[84:87]\n"
  "ds_read_b64_tr_b16 v[156:157], v108 offset:55296\n"
  "s_waitcnt vmcnt(8)\n"
  "v_mfma_f32_16x16x32_bf16 v[72:75], v[160:163], a[104:107], v[72:75]\n"
  "ds_read_b64_tr_b16 v[158:159], v108 offset:55552\n"
  "v_mfma_f32_16x16x32_bf16 v[72:75], v[164:167], a[108:111], v[72:75]\n"
  "s_add_u32 m0, s81, 65536\n"
  "s_add_u32 s95, s49, s81\n"
  "buffer_load_dwordx4 v1, s[4:7], s95 offen offset:0 sc1 lds\n"
  "v_mfma_f32_16x16x32_bf16 v[76:79], v[168:171], a[104:107], v[76:79]\n"
  "v_mfma_f32_16x16x32_bf16 v[76:79], v[172:175], a[108:111], v[76:79]\n"
  "buffer_load_dwordx4 v1, s[4:7], s95 offen offset:1024 sc1 lds\n"
  "v_mfma_f32_16x16x32_bf16 v[88:91], v[176:179], a[104:107], v[88:91]\n"
  "v_mfma_f32_16x16x32_bf16 v[88:91], v[180:183], a[108:111], v[88:91]\n"
  "buffer_load_dwordx4 v1, s[4:7], s95 offen offset:2048 sc1 lds\n"
  "v_mfma_f32_16x16x32_bf16 v[92:95], v[184:187], a[104:107], v[92:95]\n"
  "buffer_load_dwordx4 v1, s[4:7], s95 offen offset:3072 sc1 lds\n"
  "v_mfma_f32_16x16x32_bf16 v[92:95], v[188:191], a[108:111], v[92:95]\n"
  "s_waitcnt vmcnt(10) lgkmcnt(0)\n"
  "v_mfma_f32_16x16x32_bf16 v[64:67], v[128:131], a[48:51], v[64:67]\n"
  "ds_read_b64_tr_b16 v[160:161], v96 offset:57344\n"
  "ds_read_b64_tr_b16 v[162:163], v96 offset:57600\n"
  "v_mfma_f32_16x16x32_bf16 v[64:67], v[132:135], a[52:55], v[64:67]\n"
  "ds_read_b64_tr_b16 v[164:165], v96 offset:61440\n"
  "ds_read_b64_tr_b16 v[166:167], v96 offset:61696\n"
  "v_mfma_f32_16x16x32_bf16 v[68:71], v[136:139], a[48:51], v[68:71]\n"
  "ds_read_b64_tr_b16 v[168:169], v108 offset:57344\n"
  "ds_read_b64_tr_b16 v[170:171], v108 offset:57600\n"
  "v_mfma_f32_16x16x32_bf16 v[68:71], v[140:143], a[52:55], v[68:71]\n"
  "ds_read_b64_tr_b16 v[172:173], v108 offset:61440\n"
  "ds_read_b64_tr_b16 v[174:175], v108 offset:61696\n"
  "v_mfma_f32_16x16x32_bf16 v[80:83], v[144:147], a[48:51], v[80:83]\n"
  "ds_read_b64_tr_b16 v[176:177], v96 offset:59392\n"
  "ds_read_b64_tr_b16 v[178:179], v96 offset:59648\n"
  "v_mfma_f32_16x16x32_bf16 v[80:83], v[148:151], a[52:55], v[80:83]\n"
  "ds_read_b64_tr_b16 v[180:181], v96 offset:63488\n"
  "ds_read_b64_tr_b16 v[182:183], v96 offset:63744\n"
  "v_mfma_f32_16x16x32_bf16 v[84:87], v[152:155], a[48:51], v[84:87]\n"
  "ds_read_b64_tr_b16 v[184:185], v108 offset:59392\n"
  "ds_read_b64_tr_b16 v[186:187], v108 offset:59648\n"
  "v_mfma_f32_16x16x32_bf16 v[84:87], v[156:159], a[52:55], v[84:87]\n"
  "ds_read_b64_tr_b16 v[188:189], v108 offset:63488\n"
  "s_waitcnt vmcnt(8)\n"
  "v_mfma_f32_16x16x32_bf16 v[72:75], v[128:131], a[112:115], v[72:75]\n"
  "ds_read_b64_tr_b16 v[190:191], v108 offset:63744\n"
  "v_mfma_f32_16x16x32_bf16 v[72:75], v[132:135], a[116:119], v[72:75]\n"
  "s_add_u32 m0, s81, 81920\n"
  "s_add_u32 s95, s49, s81\n"
  "s_add_u32 s95, s95, 16384\n"
  "buffer_load_dwordx4 v1, s[4:7], s95 offen offset:0 sc1 lds\n"
  "v_mfma_f32_16x16x32_bf16 v[76:79], v[136:139], a[112:115], v[76:79]\n"
  "v_mfma_f32_16x16x32_bf16 v[76:79], v[140:143], a[116:119], v[76:79]\n"
  "buffer_load_dwordx4 v1, s[4:7], s95 offen offset:1024 sc1 lds\n"
  "v_mfma_f32_16x16x32_bf16 v[88:91], v[144:147], a[112:115], v[88:91]\n"
  "v_mfma_f32_16x16x32_bf16 v[88:91], v[148:151], a[116:119], v[88:91]\n"
  "buffer_load_dwordx4 v1, s[4:7], s95 offen offset:2048 sc1 lds\n"
  "v_mfma_f32_16x16x32_bf16 v[92:95], v[152:155], a[112:115], v[92:95]\n"
  "buffer_load_dwordx4 v1, s[4:7], s95 offen offset:3072 sc1 lds\n"
  "v_mfma_f32_16x16x32_bf16 v[92:95], v[156:159], a[116:119], v[92:95]\n"
  "s_waitcnt vmcnt(10) lgkmcnt(0)\n"
  "v_mfma_f32_16x16x32_bf16 v[64:67], v[160:163], a[56:59], v[64:67]\n"
  "v_mfma_f32_16x16x32_bf16 v[64:67], v[164:167], a[60:63], v[64:67]\n"
  "v_mfma_f32_16x16x32_bf16 v[68:71], v[168:171], a[56:59], v[68:71]\n"
  "v_mfma_f32_16x16x32_bf16 v[68:71], v[172:175], a[60:63], v[68:71]\n"
  "v_mfma_f32_16x16x32_bf16 v[80:83], v[176:179], a[56:59], v[80:83]\n"
  "v_mfma_f32_16x16x32_bf16 v[80:83], v[180:183], a[60:63], v[80:83]\n"
  "v_mfma_f32_16x16x32_bf16 v[84:87], v[184:187], a[56:59], v[84:87]\n"
  "v_mfma_f32_16x16x32_bf16 v[84:87], v[188:191], a[60:63], v[84:87]\n"
  "s_waitcnt vmcnt(8)\n"
  "v_mfma_f32_16x16x32_bf16 v[72:75], v[160:163], a[120:123], v[72:75]\n"
  "v_mfma_f32_16x16x32_bf16 v[72:75], v[164:167], a[124:127], v[72:75]\n"
  "s_mov_b32 m0, s58\n"
  "s_nop 0\n"
  "buffer_load_dwordx4 v104, s[32:35], 0 offen lds\n"
  "v_mfma_f32_16x16x32_bf16 v[76:79], v[168:171], a[120:123], v[76:79]\n"
  "v_mfma_f32_16x16x32_bf16 v[76:79], v[172:175], a[124:127], v[76:79]\n"
  "s_addk_i32 m0, 1024\n"
  "s_nop 0\n"
  "buffer_load_dwordx4 v105, s[32:35], 0 offen lds\n"
  "v_mfma_f32_16x16x32_bf16 v[88:91], v[176:179], a[120:123], v[88:91]\n"
  "v_mfma_f32_16x16x32_bf16 v[88:91], v[180:183], a[124:127], v[88:91]\n"
  "s_addk_i32 m0, 1024\n"
  "s_nop 0\n"
  "buffer_load_dwordx4 v106, s[32:35], 0 offen lds\n"
  "v_mfma_f32_16x16x32_bf16 v[92:95], v[184:187], a[120:123], v[92:95]\n"
  "s_mov_b32 m0, s61\n"
  "s_nop 0\n"
  "buffer_load_dwordx4 v13, s[40:43], s59 offen lds\n"
  "v_mfma_f32_16x16x32_bf16 v[92:95], v[188:191], a[124:127], v[92:95]\n"
  "v_cvt_pk_bf16_f32 v128, v64, v65\n"
  "v_cvt_pk_bf16_f32 v129, v66, v67\n"
  "v_cvt_pk_bf16_f32 v130, v72, v73\n"
  "v_cvt_pk_bf16_f32 v131, v74, v75\n"
  "v_cvt_pk_bf16_f32 v132, v68, v69\n"
  "v_cvt_pk_bf16_f32 v133, v70, v71\n"
  "v_cvt_pk_bf16_f32 v134, v76, v77\n"
  "v_cvt_pk_bf16_f32 v135, v78, v79\n"
  "v_permlane16_swap_b32 v128, v130\n"
  "v_permlane16_swap_b32 v129, v131\n"
  "v_permlane16_swap_b32 v132, v134\n"
  "v_permlane16_swap_b32 v133, v135\n"
  "v_mfma_f32_32x32x16_bf16 v[136:151], v[224:227], v[128:131], 0\n"
  "s_nop 0\n"
  "v_mfma_f32_32x32x16_bf16 v[136:151], v[228:231], v[132:135], v[136:151]\n"
  "s_load_dwordx16 s[64:79], s[98:99], 0x100\n"
  "v_cvt_pk_bf16_f32 v64, v80, v81\n"
  "v_cvt_pk_bf16_f32 v65, v82, v83\n"
  "v_cvt_pk_bf16_f32 v66, v84, v85\n"
  "v_cvt_pk_bf16_f32 v67, v86, v87\n"
  "v_cvt_pk_bf16_f32 v68, v88, v89\n"
  "v_cvt_pk_bf16_f32 v69, v90, v91\n"
  "v_cvt_pk_bf16_f32 v70, v92, v93\n"
  "v_cvt_pk_bf16_f32 v71, v94, v95\n"
  "ds_write_b64 v98, v[64:65] offset:128\n"
  "ds_write_b64 v98, v[66:67] offset:160\n"
  "ds_write_b64 v98, v[68:69] offset:3712\n"
  "ds_write_b64 v98, v[70:71] offset:3744\n"
  "s_cmp_lt_u32 s86, 11\n"
  "s_cbranch_scc0 .Lskip_15\n"
  "s_addk_i32 m0, 4096\n"
  "s_nop 0\n"
  "buffer_load_dwordx4 v13, s[40:43], s60 offen lds\n"
  ".Lskip_15:\n"
  "v_max3_f32 v184, v136, v137, v138\n"
  "v_min3_f32 v185, v136, v137, v138\n"
  "v_max3_f32 v184, v184, v139, v140\n"
  "v_min3_f32 v185, v185, v139, v140\n"
  "v_max3_f32 v184, v184, v141, v142\n"
  "v_min3_f32 v185, v185, v141, v142\n"
  "v_max3_f32 v184, v184, v143, v144\n"
  "v_min3_f32 v185, v185, v143, v144\n"
  "v_max3_f32 v184, v184, v145, v146\n"
  "v_min3_f32 v185, v185, v145, v146\n"
  "v_max3_f32 v184, v184, v147, v148\n"
  "v_min3_f32 v185, v185, v147, v148\n"
  "v_max3_f32 v184, v184, v149, v150\n"
  "v_min3_f32 v185, v185, v149, v150\n"
  "v_max_f32 v184, v184, v151\n"
  "v_min_f32 v185, v185, v151\n"
  "v_pk_mul_f32 v[184:185], v[184:185], s[92:93] op_sel_hi:[1,0] neg_lo:[1,0]\n"
  "v_pk_fma_f32 v[152:153], v[136:137], s[92:93], v[184:185] op_sel_hi:[1,0,0]\n"
  "v_pk_fma_f32 v[168:169], v[136:137], s[92:93], v[184:185] op_sel:[0,0,1] op_sel_hi:[1,0,1] neg_lo:[1,0,0] neg_hi:[1,0,0]\n"
  "v_pk_fma_f32 v[154:155], v[138:139], s[92:93], v[184:185] op_sel_hi:[1,0,0]\n"
  "v_pk_fma_f32 v[170:171], v[138:139], s[92:93], v[184:185] op_sel:[0,0,1] op_sel_hi:[1,0,1] neg_lo:[1,0,0] neg_hi:[1,0,0]\n"
  "v_pk_fma_f32 v[156:157], v[140:141], s[92:93], v[184:185] op_sel_hi:[1,0,0]\n"
  "v_pk_fma_f32 v[172:173], v[140:141], s[92:93], v[184:185] op_sel:[0,0,1] op_sel_hi:[1,0,1] neg_lo:[1,0,0] neg_hi:[1,0,0]\n"
  "v_pk_fma_f32 v[158:159], v[142:143], s[92:93], v[184:185] op_sel_hi:[1,0,0]\n"
  "v_pk_fma_f32 v[174:175], v[142:143], s[92:93], v[184:185] op_sel:[0,0,1] op_sel_hi:[1,0,1] neg_lo:[1,0,0] neg_hi:[1,0,0]\n"
  "v_pk_fma_f32 v[160:161], v[144:145], s[92:93], v[184:185] op_sel_hi:[1,0,0]\n"
  "v_pk_fma_f32 v[176:177], v[144:145], s[92:93], v[184:185] op_sel:[0,0,1] op_sel_hi:[1,0,1] neg_lo:[1,0,0] neg_hi:[1,0,0]\n"
  "v_pk_fma_f32 v[162:163], v[146:147], s[92:93], v[184:185] op_sel_hi:[1,0,0]\n"
  "v_pk_fma_f32 v[178:179], v[146:147], s[92:93], v[184:185] op_sel:[0,0,1] op_sel_hi:[1,0,1] neg_lo:[1,0,0] neg_hi:[1,0,0]\n"
  "v_pk_fma_f32 v[164:165], v[148:149], s[92:93], v[184:185] op_sel_hi:[1,0,0]\n"
  "v_pk_fma_f32 v[180:181], v[148:149], s[92:93], v[184:185] op_sel:[0,0,1] op_sel_hi:[1,0,1] neg_lo:[1,0,0] neg_hi:[1,0,0]\n"
  "v_pk_fma_f32 v[166:167], v[150:151], s[92:93], v[184:185] op_sel_hi:[1,0,0]\n"
  "v_pk_fma_f32 v[182:183], v[150:151], s[92:93], v[184:185] op_sel:[0,0,1] op_sel_hi:[1,0,1] neg_lo:[1,0,0] neg_hi:[1,0,0]\n"
  "v_exp_f32 v152, v152\n"
  "v_exp_f32 v153, v153\n"
  "v_exp_f32 v168, v168\n"
  "v_exp_f32 v169, v169\n"
  "v_exp_f32 v154, v154\n"
  "v_exp_f32 v155, v155\n"
  "v_exp_f32 v170, v170\n"
  "v_exp_f32 v171, v171\n"
  "v_exp_f32 v156, v156\n"
  "v_exp_f32 v157, v157\n"
  "v_exp_f32 v172, v172\n"
  "v_exp_f32 v173, v173\n"
  "v_pk_add_f32 v[186:187], v[152:153], v[154:155]\n"
  "v_pk_add_f32 v[188:189], v[168:169], v[170:171]\n"
  "v_exp_f32 v158, v158\n"
  "v_exp_f32 v159, v159\n"
  "v_exp_f32 v174, v174\n"
  "v_exp_f32 v175, v175\n"
  "v_pk_add_f32 v[186:187], v[186:187], v[156:157]\n"
  "v_pk_add_f32 v[188:189], v[188:189], v[172:173]\n"
  "v_exp_f32 v160, v160\n"
  "v_exp_f32 v161, v161\n"
  "v_exp_f32 v176, v176\n"
  "v_exp_f32 v177, v177\n"
  "v_pk_add_f32 v[186:187], v[186:187], v[158:159]\n"
  "v_pk_add_f32 v[188:189], v[188:189], v[174:175]\n"
  "v_exp_f32 v162, v162\n"
  "v_exp_f32 v163, v163\n"
  "v_exp_f32 v178, v178\n"
  "v_exp_f32 v179, v179\n"
  "v_pk_add_f32 v[186:187], v[186:187], v[160:161]\n"
  "v_pk_add_f32 v[188:189], v[188:189], v[176:177]\n"
  "v_exp_f32 v164, v164\n"
  "v_exp_f32 v165, v165\n"
  "v_exp_f32 v180, v180\n"
  "v_exp_f32 v181, v181\n"
  "v_pk_add_f32 v[186:187], v[186:187], v[162:163]\n"
  "v_pk_add_f32 v[188:189], v[188:189], v[178:179]\n"
  "v_exp_f32 v166, v166\n"
  "v_exp_f32 v167, v167\n"
  "v_exp_f32 v182, v182\n"
  "v_exp_f32 v183, v183\n"
  "v_pk_add_f32 v[186:187], v[186:187], v[164:165]\n"
  "v_pk_add_f32 v[188:189], v[188:189], v[180:181]\n"
  "v_pk_add_f32 v[186:187], v[186:187], v[166:167]\n"
  "v_pk_add_f32 v[188:189], v[188:189], v[182:183]\n"
  "v_add_f32 v186, v186, v187\n"
  "v_add_f32 v188, v188, v189\n"
  "v_rcp_f32 v186, v186\n"
  "v_rcp_f32 v188, v188\n"
  "v_pk_mul_f32 v[152:153], v[152:153], v[186:187] op_sel_hi:[1,0]\n"
  "v_pk_mul_f32 v[154:155], v[154:155], v[186:187] op_sel_hi:[1,0]\n"
  "v_pk_mul_f32 v[156:157], v[156:157], v[186:187] op_sel_hi:[1,0]\n"
  "v_pk_mul_f32 v[158:159], v[158:159], v[186:187] op_sel_hi:[1,0]\n"
  "v_cvt_pk_bf16_f32 v136, v152, v153\n"
  "v_cvt_pk_bf16_f32 v137, v154, v155\n"
  "v_cvt_pk_bf16_f32 v138, v156, v157\n"
  "v_cvt_pk_bf16_f32 v139, v158, v159\n"
  "ds_write_b128 v97, v[136:139] offset:0\n"
  "v_pk_mul_f32 v[160:161], v[160:161], v[186:187] op_sel_hi:[1,0]\n"
  "v_pk_mul_f32 v[162:163], v[162:163], v[186:187] op_sel_hi:[1,0]\n"
  "v_pk_mul_f32 v[164:165], v[164:165], v[186:187] op_sel_hi:[1,0]\n"
  "v_pk_mul_f32 v[166:167], v[166:167], v[186:187] op_sel_hi:[1,0]\n"
  "v_cvt_pk_bf16_f32 v140, v160, v161\n"
  "v_cvt_pk_bf16_f32 v141, v162, v163\n"
  "v_cvt_pk_bf16_f32 v142, v164, v165\n"
  "v_cvt_pk_bf16_f32 v143, v166, v167\n"
  "ds_write_b128 v97, v[140:143] offset:16\n"
  "v_pk_mul_f32 v[168:169], v[168:169], v[188:189] op_sel_hi:[1,0]\n"
  "v_pk_mul_f32 v[170:171], v[170:171], v[188:189] op_sel_hi:[1,0]\n"
  "v_pk_mul_f32 v[172:173], v[172:173], v[188:189] op_sel_hi:[1,0]\n"
  "v_pk_mul_f32 v[174:175], v[174:175], v[188:189] op_sel_hi:[1,0]\n"
  "v_cvt_pk_bf16_f32 v144, v168, v169\n"
  "v_cvt_pk_bf16_f32 v145, v170, v171\n"
  "v_cvt_pk_bf16_f32 v146, v172, v173\n"
  "v_cvt_pk_bf16_f32 v147, v174, v175\n"
  "ds_write_b128 v97, v[144:147] offset:32\n"
  "v_pk_mul_f32 v[176:177], v[176:177], v[188:189] op_sel_hi:[1,0]\n"
  "v_pk_mul_f32 v[178:179], v[178:179], v[188:189] op_sel_hi:[1,0]\n"
  "v_pk_mul_f32 v[180:181], v[180:181], v[188:189] op_sel_hi:[1,0]\n"
  "v_pk_mul_f32 v[182:183], v[182:183], v[188:189] op_sel_hi:[1,0]\n"
  "v_cvt_pk_bf16_f32 v148, v176, v177\n"
  "v_cvt_pk_bf16_f32 v149, v178, v179\n"
  "v_cvt_pk_bf16_f32 v150, v180, v181\n"
  "v_cvt_pk_bf16_f32 v151, v182, v183\n"
  "ds_write_b128 v97, v[148:151] offset:48\n"
  "s_waitcnt vmcnt(4)\n"
  "s_waitcnt lgkmcnt(0)\n"
  "s_barrier\n"
  "ds_read_b64_tr_b16 v[64:65], v99 offset:0\n"
  "ds_read_b64_tr_b16 v[66:67], v99 offset:3584\n"
  "ds_read_b64_tr_b16 v[68:69], v100 offset:0\n"
  "ds_read_b64_tr_b16 v[70:71], v100 offset:3584\n"
  "ds_read_b64_tr_b16 v[72:73], v99 offset:7168\n"
  "ds_read_b64_tr_b16 v[74:75], v99 offset:10752\n"
  "ds_read_b64_tr_b16 v[76:77], v100 offset:7168\n"
  "ds_read_b64_tr_b16 v[78:79], v100 offset:10752\n"
  "ds_read_b64_tr_b16 v[80:81], v99 offset:14336\n"
  "ds_read_b64_tr_b16 v[82:83], v99 offset:17920\n"
  "ds_read_b64_tr_b16 v[84:85], v100 offset:14336\n"
  "ds_read_b64_tr_b16 v[86:87], v100 offset:17920\n"
  "ds_read_b64_tr_b16 v[88:89], v99 offset:21504\n"
  "ds_read_b64_tr_b16 v[90:91], v99 offset:25088\n"
  "ds_read_b64_tr_b16 v[92:93], v100 offset:21504\n"
  "ds_read_b64_tr_b16 v[94:95], v100 offset:25088\n"
  "s_waitcnt lgkmcnt(12)\n"
  "v_mfma_f32_16x16x32_bf16 v[120:123], v[64:67], v[68:71], 0\n"
  "v_mfma_f32_16x16x32_bf16 v[124:127], v[64:67], v[116:119], 0\n"
  "s_waitcnt lgkmcnt(8)\n"
  "v_mfma_f32_16x16x32_bf16 v[120:123], v[72:75], v[76:79], v[120:123]\n"
  "v_mfma_f32_16x16x32_bf16 v[124:127], v[72:75], v[116:119], v[124:127]\n"
  "s_waitcnt lgkmcnt(4)\n"
  "v_mfma_f32_16x16x32_bf16 v[120:123], v[80:83], v[84:87], v[120:123]\n"
  "v_mfma_f32_16x16x32_bf16 v[124:127], v[80:83], v[116:119], v[124:127]\n"
  "s_waitcnt lgkmcnt(0)\n"
  "v_mfma_f32_16x16x32_bf16 v[120:123], v[88:91], v[92:95], v[120:123]\n"
  "v_mfma_f32_16x16x32_bf16 v[124:127], v[88:91], v[116:119], v[124:127]\n"
  "ds_read_b128 v[128:131], v109 offset:0\n"
  "ds_read_b128 v[132:135], v109 offset:2048\n"
  "ds_read_b128 v[136:139], v109 offset:1024\n"
  "ds_read_b128 v[140:143], v109 offset:3072\n"
  "ds_read_b128 v[144:147], v109 offset:4096\n"
  "ds_read_b128 v[148:151], v109 offset:6144\n"
  "s_nop 0\n"
  "v_cvt_pk_bf16_f32 v120, v120, v121\n"
  "v_cvt_pk_bf16_f32 v121, v122, v123\n"
  "buffer_store_dwordx2 v[120:121], v8, s[4:7], s51 offen sc1\n"
  "buffer_store_dwordx2 v[120:121], v8, s[4:7], s52 offen sc1\n"
  "s_mov_b32 exec_lo, 0x10001\n"
  "s_mov_b32 exec_hi, 0x10001\n"
  "buffer_store_dwordx4 v[124:127], v2, s[4:7], s53 offen sc1\n"
  "buffer_store_dwordx4 v[124:127], v2, s[4:7], s54 offen sc1\n"
  "s_mov_b64 exec, -1\n"
  "ds_read_b128 v[152:155], v109 offset:5120\n"
  "ds_read_b128 v[156:159], v109 offset:7168\n"
  "s_waitcnt lgkmcnt(4)\n"
  "v_mfma_f32_16x16x32_bf16 v[64:67], v[128:131], a[0:3], 0\n"
  "ds_read_b128 v[160:163], v109 offset:8192\n"
  "v_mfma_f32_16x16x32_bf16 v[64:67], v[132:135], a[4:7], v[64:67]\n"
  "ds_read_b128 v[164:167], v109 offset:10240\n"
  "v_mfma_f32_16x16x32_bf16 v[68:71], v[136:139], a[0:3], 0\n"
  "ds_read_b128 v[168:171], v109 offset:9216\n"
  "v_mfma_f32_16x16x32_bf16 v[68:71], v[140:143], a[4:7], v[68:71]\n"
  "ds_read_b128 v[172:175], v109 offset:11264\n"
  "v_mfma_f32_16x16x32_bf16 v[72:75], v[128:131], a[64:67], 0\n"
  "s_add_u32 s93, s82, 0\n"
  "s_and_b32 s93, s93, 0xffff\n"
  "s_add_u32 m0, s93, 0\n"
  "s_add_u32 s95, s93, s50\n"
  "buffer_load_dwordx4 v1, s[4:7], s95 offen offset:0 sc1 lds\n"
  "v_mfma_f32_16x16x32_bf16 v[72:75], v[132:135], a[68:71], v[72:75]\n"
  "v_mfma_f32_16x16x32_bf16 v[76:79], v[136:139], a[64:67], 0\n"
  "buffer_load_dwordx4 v1, s[4:7], s95 offen offset:1024 sc1 lds\n"
  "v_mfma_f32_16x16x32_bf16 v[76:79], v[140:143], a[68:71], v[76:79]\n"
  "s_waitcnt lgkmcnt(4)\n"
  "v_mfma_f32_16x16x32_bf16 v[64:67], v[144:147], a[8:11], v[64:67]\n"
  "ds_read_b128 v[128:131], v109 offset:12288\n"
  "v_mfma_f32_16x16x32_bf16 v[64:67], v[148:151], a[12:15], v[64:67]\n"
  "ds_read_b128 v[132:135], v109 offset:14336\n"
  "v_mfma_f32_16x16x32_bf16 v[68:71], v[152:155], a[8:11], v[68:71]\n"
  "ds_read_b128 v[136:139], v109 offset:13312\n"
  "v_mfma_f32_16x16x32_bf16 v[68:71], v[156:159], a[12:15], v[68:71]\n"
  "ds_read_b128 v[140:143], v109 offset:15360\n"
  "v_mfma_f32_16x16x32_bf16 v[72:75], v[144:147], a[72:75], v[72:75]\n"
  "buffer_load_dwordx4 v1, s[4:7], s95 offen offset:2048 sc1 lds\n"
  "v_mfma_f32_16x16x32_bf16 v[72:75], v[148:151], a[76:79], v[72:75]\n"
  "v_mfma_f32_16x16x32_bf16 v[76:79], v[152:155], a[72:75], v[76:79]\n"
  "buffer_load_dwordx4 v1, s[4:7], s95 offen offset:3072 sc1 lds\n"
  "v_mfma_f32_16x16x32_bf16 v[76:79], v[156:159], a[76:79], v[76:79]\n"
  "s_waitcnt lgkmcnt(4)\n"
  "v_mfma_f32_16x16x32_bf16 v[64:67], v[160:163], a[16:19], v[64:67]\n"
  "ds_read_b128 v[144:147], v109 offset:16384\n"
  "v_mfma_f32_16x16x32_bf16 v[64:67], v[164:167], a[20:23], v[64:67]\n"
  "ds_read_b128 v[148:151], v109 offset:18432\n"
  "v_mfma_f32_16x16x32_bf16 v[68:71], v[168:171], a[16:19], v[68:71]\n"
  "ds_read_b128 v[152:155], v109 offset:17408\n"
  "v_mfma_f32_16x16x32_bf16 v[68:71], v[172:175], a[20:23], v[68:71]\n"
  "ds_read_b128 v[156:159], v109 offset:19456\n"
  "v_mfma_f32_16x16x32_bf16 v[72:75], v[160:163], a[80:83], v[72:75]\n"
  "s_add_u32 s93, s82, 16384\n"
  "s_and_b32 s93, s93, 0xffff\n"
  "s_add_u32 m0, s93, 0\n"
  "s_add_u32 s95, s93, s50\n"
  "buffer_load_dwordx4 v1, s[4:7], s95 offen offset:0 sc1 lds\n"
  "v_mfma_f32_16x16x32_bf16 v[72:75], v[164:167], a[84:87], v[72:75]\n"
  "v_mfma_f32_16x16x32_bf16 v[76:79], v[168:171], a[80:83], v[76:79]\n"
  "buffer_load_dwordx4 v1, s[4:7], s95 offen offset:1024 sc1 lds\n"
  "v_mfma_f32_16x16x32_bf16 v[76:79], v[172:175], a[84:87], v[76:79]\n"
  "s_waitcnt lgkmcnt(4)\n"
  "v_mfma_f32_16x16x32_bf16 v[64:67], v[128:131], a[24:27], v[64:67]\n"
  "ds_read_b128 v[160:163], v109 offset:20480\n"
  "v_mfma_f32_16x16x32_bf16 v[64:67], v[132:135], a[28:31], v[64:67]\n"
  "ds_read_b128 v[164:167], v109 offset:22528\n"
  "v_mfma_f32_16x16x32_bf16 v[68:71], v[136:139], a[24:27], v[68:71]\n"
  "ds_read_b128 v[168:171], v109 offset:21504\n"
  "v_mfma_f32_16x16x32_bf16 v[68:71], v[140:143], a[28:31], v[68:71]\n"
  "ds_read_b128 v[172:175], v109 offset:23552\n"
  "v_mfma_f32_16x16x32_bf16 v[72:75], v[128:131], a[88:91], v[72:75]\n"
  "buffer_load_dwordx4 v1, s[4:7], s95 offen offset:2048 sc1 lds\n"
  "v_mfma_f32_16x16x32_bf16 v[72:75], v[132:135], a[92:95], v[72:75]\n"
  "v_mfma_f32_16x16x32_bf16 v[76:79], v[136:139], a[88:91], v[76:79]\n"
  "buffer_load_dwordx4 v1, s[4:7], s95 offen offset:3072 sc1 lds\n"
  "v_mfma_f32_16x16x32_bf16 v[76:79], v[140:143], a[92:95], v[76:79]\n"
  "s_waitcnt vmcnt(8)\n"
  "s_barrier\n"
  "s_mov_b32 exec_lo, s85\n"
  "s_mov_b32 exec_hi, 0\n"
  "buffer_store_dwordx4 v[4:7], v14, s[4:7], s55 offen sc1\n"
  "s_mov_b64 exec, -1\n"
  "s_waitcnt lgkmcnt(4)\n"
  "v_mfma_f32_16x16x32_bf16 v[64:67], v[144:147], a[32:35], v[64:67]\n"
  "ds_read_b128 v[128:131], v109 offset:24576\n"
  "v_mfma_f32_16x16x32_bf16 v[64:67], v[148:151], a[36:39], v[64:67]\n"
  "ds_read_b128 v[132:135], v109 offset:26624\n"
  "v_mfma_f32_16x16x32_bf16 v[68:71], v[152:155], a[32:35], v[68:71]\n"
  "ds_read_b128 v[136:139], v109 offset:25600\n"
  "v_mfma_f32_16x16x32_bf16 v[68:71], v[156:159], a[36:39], v[68:71]\n"
  "ds_read_b128 v[140:143], v109 offset:27648\n"
  "v_mfma_f32_16x16x32_bf16 v[72:75], v[144:147], a[96:99], v[72:75]\n"
  "s_add_u32 s93, s82, 32768\n"
  "s_and_b32 s93, s93, 0xffff\n"
  "s_add_u32 m0, s93, 0\n"
  "s_add_u32 s95, s93, s50\n"
  "buffer_load_dwordx4 v1, s[4:7], s95 offen offset:0 sc1 lds\n"
  "v_mfma_f32_16x16x32_bf16 v[72:75], v[148:151], a[100:103], v[72:75]\n"
  "v_mfma_f32_16x16x32_bf16 v[76:79], v[152:155], a[96:99], v[76:79]\n"
  "buffer_load_dwordx4 v1, s[4:7], s95 offen offset:1024 sc1 lds\n"
  "v_mfma_f32_16x16x32_bf16 v[76:79], v[156:159], a[100:103], v[76:79]\n"
  "s_waitcnt lgkmcnt(4)\n"
  "v_mfma_f32_16x16x32_bf16 v[64:67], v[160:163], a[40:43], v[64:67]\n"
  "ds_read_b128 v[144:147], v109 offset:28672\n"
  "v_mfma_f32_16x16x32_bf16 v[64:67], v[164:167], a[44:47], v[64:67]\n"
  "ds_read_b128 v[148:151], v109 offset:30720\n"
  "v_mfma_f32_16x16x32_bf16 v[68:71], v[168:171], a[40:43], v[68:71]\n"
  "ds_read_b128 v[152:155], v109 offset:29696\n"
  "v_mfma_f32_16x16x32_bf16 v[68:71], v[172:175], a[44:47], v[68:71]\n"
  "ds_read_b128 v[156:159], v109 offset:31744\n"
  "v_mfma_f32_16x16x32_bf16 v[72:75], v[160:163], a[104:107], v[72:75]\n"
  "buffer_load_dwordx4 v1, s[4:7], s95 offen offset:2048 sc1 lds\n"
  "v_mfma_f32_16x16x32_bf16 v[72:75], v[164:167], a[108:111], v[72:75]\n"
  "v_mfma_f32_16x16x32_bf16 v[76:79], v[168:171], a[104:107], v[76:79]\n"
  "buffer_load_dwordx4 v1, s[4:7], s95 offen offset:3072 sc1 lds\n"
  "v_mfma_f32_16x16x32_bf16 v[76:79], v[172:175], a[108:111], v[76:79]\n"
  "s_waitcnt lgkmcnt(4)\n"
  "v_mfma_f32_16x16x32_bf16 v[64:67], v[128:131], a[48:51], v[64:67]\n"
  "ds_read_b64_tr_b16 v[192:193], v101 offset:0\n"
  "v_mfma_f32_16x16x32_bf16 v[64:67], v[132:135], a[52:55], v[64:67]\n"
  "ds_read_b64_tr_b16 v[194:195], v101 offset:1536\n"
  "v_mfma_f32_16x16x32_bf16 v[68:71], v[136:139], a[48:51], v[68:71]\n"
  "ds_read_b64_tr_b16 v[196:197], v101 offset:32\n"
  "v_mfma_f32_16x16x32_bf16 v[68:71], v[140:143], a[52:55], v[68:71]\n"
  "ds_read_b64_tr_b16 v[198:199], v101 offset:1568\n"
  "v_mfma_f32_16x16x32_bf16 v[72:75], v[128:131], a[112:115], v[72:75]\n"
  "s_add_u32 s93, s82, 49152\n"
  "s_and_b32 s93, s93, 0xffff\n"
  "s_add_u32 m0, s93, 0\n"
  "s_add_u32 s95, s93, s50\n"
  "buffer_load_dwordx4 v1, s[4:7], s95 offen offset:0 sc1 lds\n"
  "ds_read_b64_tr_b16 v[200:201], v101 offset:64\n"
  "v_mfma_f32_16x16x32_bf16 v[72:75], v[132:135], a[116:119], v[72:75]\n"
  "ds_read_b64_tr_b16 v[202:203], v101 offset:1600\n"
  "v_mfma_f32_16x16x32_bf16 v[76:79], v[136:139], a[112:115], v[76:79]\n"
  "buffer_load_dwordx4 v1, s[4:7], s95 offen offset:1024 sc1 lds\n"
  "ds_read_b64_tr_b16 v[204:205], v112 offset:0\n"
  "v_mfma_f32_16x16x32_bf16 v[76:79], v[140:143], a[116:119], v[76:79]\n"
  "ds_read_b64_tr_b16 v[206:207], v113 offset:0\n"
  "s_waitcnt lgkmcnt(8)\n"
  "v_mfma_f32_16x16x32_bf16 v[64:67], v[144:147], a[56:59], v[64:67]\n"
  "ds_read_b64_tr_b16 v[208:209], v107 offset:4096\n"
  "v_mfma_f32_16x16x32_bf16 v[64:67], v[148:151], a[60:63], v[64:67]\n"
  "ds_read_b64_tr_b16 v[210:211], v107 offset:5120\n"
  "v_mfma_f32_16x16x32_bf16 v[68:71], v[152:155], a[56:59], v[68:71]\n"
  "v_mfma_f32_16x16x32_bf16 v[68:71], v[156:159], a[60:63], v[68:71]\n"
  "v_mfma_f32_16x16x32_bf16 v[72:75], v[144:147], a[120:123], v[72:75]\n"
  "buffer_load_dwordx4 v1, s[4:7], s95 offen offset:2048 sc1 lds\n"
  "v_mfma_f32_16x16x32_bf16 v[72:75], v[148:151], a[124:127], v[72:75]\n"
  "v_mfma_f32_16x16x32_bf16 v[76:79], v[152:155], a[120:123], v[76:79]\n"
  "buffer_load_dwordx4 v1, s[4:7], s95 offen offset:3072 sc1 lds\n"
  "v_mfma_f32_16x16x32_bf16 v[76:79], v[156:159], a[124:127], v[76:79]\n"
  "v_cvt_pk_bf16_f32 v128, v64, v65\n"
  "v_cvt_pk_bf16_f32 v129, v66, v67\n"
  "s_nop 2\n"
  "v_cvt_pk_bf16_f32 v130, v72, v73\n"
  "v_cvt_pk_bf16_f32 v131, v74, v75\n"
  "v_cvt_pk_bf16_f32 v132, v68, v69\n"
  "v_cvt_pk_bf16_f32 v133, v70, v71\n"
  "v_cvt_pk_bf16_f32 v134, v76, v77\n"
  "v_cvt_pk_bf16_f32 v135, v78, v79\n"
  "v_permlane16_swap_b32 v128, v130\n"
  "v_permlane16_swap_b32 v129, v131\n"
  "v_permlane16_swap_b32 v132, v134\n"
  "v_permlane16_swap_b32 v133, v135\n"
  "v_mfma_f32_32x32x16_bf16 v[136:151], v[224:227], v[128:131], 0\n"
  "s_nop 0\n"
  "v_mfma_f32_32x32x16_bf16 v[136:151], v[228:231], v[132:135], v[136:151]\n"
  "s_nop 11\n"
  "v_max3_f32 v184, v136, v137, v138\n"
  "v_min3_f32 v185, v136, v137, v138\n"
  "v_max3_f32 v184, v184, v139, v140\n"
  "v_min3_f32 v185, v185, v139, v140\n"
  "v_max3_f32 v184, v184, v141, v142\n"
  "v_min3_f32 v185, v185, v141, v142\n"
  "v_max3_f32 v184, v184, v143, v144\n"
  "v_min3_f32 v185, v185, v143, v144\n"
  "v_max3_f32 v184, v184, v145, v146\n"
  "v_min3_f32 v185, v185, v145, v146\n"
  "v_max3_f32 v184, v184, v147, v148\n"
  "v_min3_f32 v185, v185, v147, v148\n"
  "v_max3_f32 v184, v184, v149, v150\n"
  "v_min3_f32 v185, v185, v149, v150\n"
  "v_max_f32 v184, v184, v151\n"
  "v_min_f32 v185, v185, v151\n"
  "v_pk_mul_f32 v[184:185], v[184:185], s[92:93] op_sel_hi:[1,0] neg_lo:[1,0]\n"
  "s_cmp_eq_u32 s80, 0\n"
  "s_cbranch_scc0 .Lskip_16\n"
  "s_mov_b64 exec, 0xffff\n"
  "s_bitset0_b64 exec, s84\n"
  "v_mov_b64 v[18:19], 0\n"
  "buffer_atomic_or_x2 v[18:19], v10, s[16:19], s56 offen sc0 sc1\n"
  "s_mov_b64 exec, -1\n"
  ".Lskip_16:\n"
  "v_pk_fma_f32 v[152:153], v[136:137], s[92:93], v[184:185] op_sel_hi:[1,0,0]\n"
  "v_pk_fma_f32 v[168:169], v[136:137], s[92:93], v[184:185] op_sel:[0,0,1] op_sel_hi:[1,0,1] neg_lo:[1,0,0] neg_hi:[1,0,0]\n"
  "v_pk_fma_f32 v[154:155], v[138:139], s[92:93], v[184:185] op_sel_hi:[1,0,0]\n"
  "v_pk_fma_f32 v[170:171], v[138:139], s[92:93], v[184:185] op_sel:[0,0,1] op_sel_hi:[1,0,1] neg_lo:[1,0,0] neg_hi:[1,0,0]\n"
  "v_pk_fma_f32 v[156:157], v[140:141], s[92:93], v[184:185] op_sel_hi:[1,0,0]\n"
  "v_pk_fma_f32 v[172:173], v[140:141], s[92:93], v[184:185] op_sel:[0,0,1] op_sel_hi:[1,0,1] neg_lo:[1,0,0] neg_hi:[1,0,0]\n"
  "v_pk_fma_f32 v[158:159], v[142:143], s[92:93], v[184:185] op_sel_hi:[1,0,0]\n"
  "v_pk_fma_f32 v[174:175], v[142:143], s[92:93], v[184:185] op_sel:[0,0,1] op_sel_hi:[1,0,1] neg_lo:[1,0,0] neg_hi:[1,0,0]\n"
  "v_pk_fma_f32 v[160:161], v[144:145], s[92:93], v[184:185] op_sel_hi:[1,0,0]\n"
  "v_pk_fma_f32 v[176:177], v[144:145], s[92:93], v[184:185] op_sel:[0,0,1] op_sel_hi:[1,0,1] neg_lo:[1,0,0] neg_hi:[1,0,0]\n"
  "v_pk_fma_f32 v[162:163], v[146:147], s[92:93], v[184:185] op_sel_hi:[1,0,0]\n"
  "v_pk_fma_f32 v[178:179], v[146:147], s[92:93], v[184:185] op_sel:[0,0,1] op_sel_hi:[1,0,1] neg_lo:[1,0,0] neg_hi:[1,0,0]\n"
  "v_pk_fma_f32 v[164:165], v[148:149], s[92:93], v[184:185] op_sel_hi:[1,0,0]\n"
  "v_pk_fma_f32 v[180:181], v[148:149], s[92:93], v[184:185] op_sel:[0,0,1] op_sel_hi:[1,0,1] neg_lo:[1,0,0] neg_hi:[1,0,0]\n"
  "v_pk_fma_f32 v[166:167], v[150:151], s[92:93], v[184:185] op_sel_hi:[1,0,0]\n"
  "v_pk_fma_f32 v[182:183], v[150:151], s[92:93], v[184:185] op_sel:[0,0,1] op_sel_hi:[1,0,1] neg_lo:[1,0,0] neg_hi:[1,0,0]\n"
  "v_exp_f32 v152, v152\n"
  "v_exp_f32 v153, v153\n"
  "v_exp_f32 v168, v168\n"
  "v_exp_f32 v169, v169\n"
  "v_exp_f32 v154, v154\n"
  "v_exp_f32 v155, v155\n"
  "v_exp_f32 v170, v170\n"
  "v_exp_f32 v171, v171\n"
  "v_exp_f32 v156, v156\n"
  "v_exp_f32 v157, v157\n"
  "v_exp_f32 v172, v172\n"
  "v_exp_f32 v173, v173\n"
  "v_pk_add_f32 v[186:187], v[152:153], v[154:155]\n"
  "v_pk_add_f32 v[188:189], v[168:169], v[170:171]\n"
  "v_exp_f32 v158, v158\n"
  "v_exp_f32 v159, v159\n"
  "v_exp_f32 v174, v174\n"
  "v_exp_f32 v175, v175\n"
  "v_pk_add_f32 v[186:187], v[186:187], v[156:157]\n"
  "v_pk_add_f32 v[188:189], v[188:189], v[172:173]\n"
  "v_exp_f32 v160, v160\n"
  "v_exp_f32 v161, v161\n"
  "v_exp_f32 v176, v176\n"
  "v_exp_f32 v177, v177\n"
  "v_pk_add_f32 v[186:187], v[186:187], v[158:159]\n"
  "v_pk_add_f32 v[188:189], v[188:189], v[174:175]\n"
  "v_exp_f32 v162, v162\n"
  "v_exp_f32 v163, v163\n"
  "v_exp_f32 v178, v178\n"
  "v_exp_f32 v179, v179\n"
  "v_pk_add_f32 v[186:187], v[186:187], v[160:161]\n"
  "v_pk_add_f32 v[188:189], v[188:189], v[176:177]\n"
  "v_exp_f32 v164, v164\n"
  "v_exp_f32 v165, v165\n"
  "v_exp_f32 v180, v180\n"
  "v_exp_f32 v181, v181\n"
  "v_pk_add_f32 v[186:187], v[186:187], v[162:163]\n"
  "v_pk_add_f32 v[188:189], v[188:189], v[178:179]\n"
  "v_exp_f32 v166, v166\n"
  "v_exp_f32 v167, v167\n"
  "v_exp_f32 v182, v182\n"
  "v_exp_f32 v183, v183\n"
  "v_pk_add_f32 v[186:187], v[186:187], v[164:165]\n"
  "v_pk_add_f32 v[188:189], v[188:189], v[180:181]\n"
  "v_pk_add_f32 v[186:187], v[186:187], v[166:167]\n"
  "v_pk_add_f32 v[188:189], v[188:189], v[182:183]\n"
  "v_add_f32 v186, v186, v187\n"
  "v_add_f32 v188, v188, v189\n"
  "v_rcp_f32 v186, v186\n"
  "v_rcp_f32 v188, v188\n"
  "v_pk_mul_f32 v[152:153], v[152:153], v[186:187] op_sel_hi:[1,0]\n"
  "v_pk_mul_f32 v[154:155], v[154:155], v[186:187] op_sel_hi:[1,0]\n"
  "v_pk_mul_f32 v[156:157], v[156:157], v[186:187] op_sel_hi:[1,0]\n"
  "v_pk_mul_f32 v[158:159], v[158:159], v[186:187] op_sel_hi:[1,0]\n"
  "v_cvt_pk_bf16_f32 v24, v152, v153\n"
  "v_cvt_pk_bf16_f32 v25, v154, v155\n"
  "v_cvt_pk_bf16_f32 v26, v156, v157\n"
  "v_cvt_pk_bf16_f32 v27, v158, v159\n"
  "v_pk_mul_f32 v[160:161], v[160:161], v[186:187] op_sel_hi:[1,0]\n"
  "v_pk_mul_f32 v[162:163], v[162:163], v[186:187] op_sel_hi:[1,0]\n"
  "v_pk_mul_f32 v[164:165], v[164:165], v[186:187] op_sel_hi:[1,0]\n"
  "v_pk_mul_f32 v[166:167], v[166:167], v[186:187] op_sel_hi:[1,0]\n"
  "v_cvt_pk_bf16_f32 v28, v160, v161\n"
  "v_cvt_pk_bf16_f32 v29, v162, v163\n"
  "v_cvt_pk_bf16_f32 v30, v164, v165\n"
  "v_cvt_pk_bf16_f32 v31, v166, v167\n"
  "v_pk_mul_f32 v[168:169], v[168:169], v[188:189] op_sel_hi:[1,0]\n"
  "v_pk_mul_f32 v[170:171], v[170:171], v[188:189] op_sel_hi:[1,0]\n"
  "v_pk_mul_f32 v[172:173], v[172:173], v[188:189] op_sel_hi:[1,0]\n"
  "v_pk_mul_f32 v[174:175], v[174:175], v[188:189] op_sel_hi:[1,0]\n"
  "v_cvt_pk_bf16_f32 v32, v168, v169\n"
  "v_cvt_pk_bf16_f32 v33, v170, v171\n"
  "v_cvt_pk_bf16_f32 v34, v172, v173\n"
  "v_cvt_pk_bf16_f32 v35, v174, v175\n"
  "v_pk_mul_f32 v[176:177], v[176:177], v[188:189] op_sel_hi:[1,0]\n"
  "v_pk_mul_f32 v[178:179], v[178:179], v[188:189] op_sel_hi:[1,0]\n"
  "v_pk_mul_f32 v[180:181], v[180:181], v[188:189] op_sel_hi:[1,0]\n"
  "v_pk_mul_f32 v[182:183], v[182:183], v[188:189] op_sel_hi:[1,0]\n"
  "v_cvt_pk_bf16_f32 v36, v176, v177\n"
  "v_cvt_pk_bf16_f32 v37, v178, v179\n"
  "v_cvt_pk_bf16_f32 v38, v180, v181\n"
  "v_cvt_pk_bf16_f32 v39, v182, v183\n"
  "v_mov_b32 v60, 0x3f80\n"
  "v_mov_b32 v61, 1.0\n"
  "s_cmp_eq_u32 s80, 0\n"
  "s_cbranch_scc0 .Lskip_17\n"
  "s_mov_b64 exec, 0xffff\n"
  "s_bitset0_b64 exec, s84\n"
  "s_waitcnt vmcnt(0)\n"
  "v_cmp_ne_u64 vcc, s[88:89], v[18:19]\n"
  "s_mov_b64 exec, -1\n"
  "s_cbranch_vccz .Lhave_18\n"
  "s_mov_b64 exec, 0xffff\n"
  "s_bitset0_b64 exec, s84\n"
  "s_mov_b32 s94, 0x40000\n"
  ".Lpoll_19:\n"
  "v_mov_b64 v[18:19], 0\n"
  "buffer_atomic_or_x2 v[18:19], v10, s[16:19], s56 offen sc0 sc1\n"
  "s_waitcnt vmcnt(0)\n"
  "v_cmp_ne_u64 vcc, s[88:89], v[18:19]\n"
  "s_cbranch_vccz .Ldone_20\n"
  "s_sub_u32 s94, s94, 1\n"
  "s_cbranch_scc0 .Lpoll_19\n"
  ".Ldone_20:\n"
  "s_mov_b64 exec, -1\n"
  ".Lhave_18:\n"
  ".Lskip_17:\n"
  "s_barrier\n"
  "buffer_load_dwordx4 v[128:131], v1, s[16:19], s9 offen sc1\n"
  "buffer_load_dwordx4 v[64:67], v1, s[16:19], s8 offen sc1\n"
  "buffer_load_dwordx4 v[68:71], v1, s[16:19], s8 offen offset:1024 sc1\n"
  "buffer_load_dwordx4 v[72:75], v1, s[16:19], s8 offen offset:2048 sc1\n"
  "buffer_load_dwordx4 v[76:79], v1, s[16:19], s8 offen offset:3072 sc1\n"
  "s_addk_i32 s8, 4096\n"
  "buffer_load_dwordx4 v[80:83], v1, s[16:19], s8 offen sc1\n"
  "buffer_load_dwordx4 v[84:87], v1, s[16:19], s8 offen offset:1024 sc1\n"
  "buffer_load_dwordx4 v[88:91], v1, s[16:19], s8 offen offset:2048 sc1\n"
  "buffer_load_dwordx4 v[92:95], v1, s[16:19], s8 offen offset:3072 sc1\n"
  "s_waitcnt vmcnt(8)\n"
  "v_add_f32_dpp v128, v128, v128 row_ror:4 row_mask:0xf bank_mask:0xf\n"
  "v_add_f32_dpp v129, v129, v129 row_ror:4 row_mask:0xf bank_mask:0xf\n"
  "v_add_f32_dpp v130, v130, v130 row_ror:4 row_mask:0xf bank_mask:0xf\n"
  "v_add_f32_dpp v131, v131, v131 row_ror:4 row_mask:0xf bank_mask:0xf\n"
  "v_add_f32_dpp v128, v128, v128 row_ror:8 row_mask:0xf bank_mask:0xf\n"
  "v_add_f32_dpp v129, v129, v129 row_ror:8 row_mask:0xf bank_mask:0xf\n"
  "v_add_f32_dpp v130, v130, v130 row_ror:8 row_mask:0xf bank_mask:0xf\n"
  "v_add_f32_dpp v131, v131, v131 row_ror:8 row_mask:0xf bank_mask:0xf\n"
  "v_mov_b32 v132, v128\n"
  "v_mov_b32 v133, v129\n"
  "v_mov_b32 v134, v130\n"
  "v_mov_b32 v135, v131\n"
  "v_permlane16_swap_b32 v128, v132\n"
  "v_permlane16_swap_b32 v129, v133\n"
  "v_permlane16_swap_b32 v130, v134\n"
  "v_permlane16_swap_b32 v131, v135\n"
  "v_pk_add_f32 v[128:129], v[128:129], v[132:133]\n"
  "v_pk_add_f32 v[130:131], v[130:131], v[134:135]\n"
  "s_waitcnt lgkmcnt(8)\n"
  "buffer_store_dwordx4 v[192:195], v1, s[4:7], s11 offen sc1\n"
  "v_mov_b32 v132, v128\n"
  "v_mov_b32 v133, v129\n"
  "v_mov_b32 v134, v130\n"
  "v_mov_b32 v135, v131\n"
  "v_permlane32_swap_b32 v128, v132\n"
  "v_permlane32_swap_b32 v129, v133\n"
  "v_permlane32_swap_b32 v130, v134\n"
  "v_permlane32_swap_b32 v131, v135\n"
  "v_pk_add_f32 v[128:129], v[128:129], v[132:133]\n"
  "v_pk_add_f32 v[130:131], v[130:131], v[134:135]\n"
  "s_waitcnt lgkmcnt(6)\n"
  "buffer_store_dwordx4 v[196:199], v1, s[4:7], s11 offen offset:2048 sc1\n"
  "v_cvt_pk_bf16_f32 v128, v128, v129\n"
  "v_cvt_pk_bf16_f32 v129, v130, v131\n"
  "ds_write_b64 v103, v[128:129]\n"
  "s_waitcnt vmcnt(9)\n"
  "v_dot2_f32_bf16 v112, v64, v60, 0\n"
  "v_dot2_f32_bf16 v113, v64, v61, 0\n"
  "v_dot2_f32_bf16 v116, v66, v60, 0\n"
  "v_dot2_f32_bf16 v117, v66, v61, 0\n"
  "v_dot2_f32_bf16 v114, v65, v60, 0\n"
  "v_dot2_f32_bf16 v115, v65, v61, 0\n"
  "v_dot2_f32_bf16 v118, v67, v60, 0\n"
  "v_dot2_f32_bf16 v119, v67, v61, 0\n"
  "s_waitcnt vmcnt(8)\n"
  "v_dot2_f32_bf16 v112, v68, v60, v112\n"
  "v_dot2_f32_bf16 v113, v68, v61, v113\n"
  "v_dot2_f32_bf16 v116, v70, v60, v116\n"
  "v_dot2_f32_bf16 v117, v70, v61, v117\n"
  "v_dot2_f32_bf16 v114, v69, v60, v114\n"
  "v_dot2_f32_bf16 v115, v69, v61, v115\n"
  "v_dot2_f32_bf16 v118, v71, v60, v118\n"
  "v_dot2_f32_bf16 v119, v71, v61, v119\n"
  "s_waitcnt lgkmcnt(5)\n"
  "buffer_store_dwordx4 v[200:203], v1, s[4:7], s12 offen sc1\n"
  "s_waitcnt vmcnt(8)\n"
  "v_dot2_f32_bf16 v112, v72, v60, v112\n"
  "v_dot2_f32_bf16 v113, v72, v61, v113\n"
  "v_dot2_f32_bf16 v116, v74, v60, v116\n"
  "v_dot2_f32_bf16 v117, v74, v61, v117\n"
  "v_dot2_f32_bf16 v114, v73, v60, v114\n"
  "v_dot2_f32_bf16 v115, v73, v61, v115\n"
  "v_dot2_f32_bf16 v118, v75, v60, v118\n"
  "v_dot2_f32_bf16 v119, v75, v61, v119\n"
  "s_waitcnt vmcnt(7)\n"
  "v_dot2_f32_bf16 v112, v76, v60, v112\n"
  "v_dot2_f32_bf16 v113, v76, v61, v113\n"
  "v_dot2_f32_bf16 v116, v78, v60, v116\n"
  "v_dot2_f32_bf16 v117, v78, v61, v117\n"
  "v_dot2_f32_bf16 v114, v77, v60, v114\n"
  "v_dot2_f32_bf16 v115, v77, v61, v115\n"
  "v_dot2_f32_bf16 v118, v79, v60, v118\n"
  "v_dot2_f32_bf16 v119, v79, v61, v119\n"
  "s_waitcnt lgkmcnt(3)\n"
  "buffer_store_dwordx4 v[204:207], v1, s[4:7], s14 offen sc1\n"
  "s_waitcnt vmcnt(7)\n"
  "v_dot2_f32_bf16 v112, v80, v60, v112\n"
  "v_dot2_f32_bf16 v113, v80, v61, v113\n"
  "v_dot2_f32_bf16 v116, v82, v60, v116\n"
  "v_dot2_f32_bf16 v117, v82, v61, v117\n"
  "v_dot2_f32_bf16 v114, v81, v60, v114\n"
  "v_dot2_f32_bf16 v115, v81, v61, v115\n"
  "v_dot2_f32_bf16 v118, v83, v60, v118\n"
  "v_dot2_f32_bf16 v119, v83, v61, v119\n"
  "s_waitcnt vmcnt(6)\n"
  "v_dot2_f32_bf16 v112, v84, v60, v112\n"
  "v_dot2_f32_bf16 v113, v84, v61, v113\n"
  "v_dot2_f32_bf16 v116, v86, v60, v116\n"
  "v_dot2_f32_bf16 v117, v86, v61, v117\n"
  "v_dot2_f32_bf16 v114, v85, v60, v114\n"
  "v_dot2_f32_bf16 v115, v85, v61, v115\n"
  "v_dot2_f32_bf16 v118, v87, v60, v118\n"
  "v_dot2_f32_bf16 v119, v87, v61, v119\n"
  "s_waitcnt vmcnt(5)\n"
  "v_dot2_f32_bf16 v112, v88, v60, v112\n"
  "v_dot2_f32_bf16 v113, v88, v61, v113\n"
  "v_dot2_f32_bf16 v116, v90, v60, v116\n"
  "v_dot2_f32_bf16 v117, v90, v61, v117\n"
  "v_dot2_f32_bf16 v114, v89, v60, v114\n"
  "v_dot2_f32_bf16 v115, v89, v61, v115\n"
  "v_dot2_f32_bf16 v118, v91, v60, v118\n"
  "v_dot2_f32_bf16 v119, v91, v61, v119\n"
  "s_waitcnt vmcnt(4)\n"
  "v_dot2_f32_bf16 v112, v92, v60, v112\n"
  "v_dot2_f32_bf16 v113, v92, v61, v113\n"
  "v_dot2_f32_bf16 v116, v94, v60, v116\n"
  "v_dot2_f32_bf16 v117, v94, v61, v117\n"
  "v_dot2_f32_bf16 v114, v93, v60, v114\n"
  "v_dot2_f32_bf16 v115, v93, v61, v115\n"
  "v_dot2_f32_bf16 v118, v95, v60, v118\n"
  "v_dot2_f32_bf16 v119, v95, v61, v119\n"
  "v_permlane32_swap_b32 v112, v116\n"
  "v_permlane32_swap_b32 v113, v117\n"
  "v_permlane32_swap_b32 v114, v118\n"
  "v_permlane32_swap_b32 v115, v119\n"
  "v_pk_add_f32 v[112:113], v[112:113], v[116:117]\n"
  "v_pk_add_f32 v[114:115], v[114:115], v[118:119]\n"
  "v_cvt_pk_bf16_f32 v112, v112, v113\n"
  "v_cvt_pk_bf16_f32 v113, v114, v115\n"
  "ds_write_b64 v216, v[112:113]\n"
  "s_cmp_lt_u32 s86, 11\n"
  "s_cbranch_scc0 .Lskip_21\n"
  "s_waitcnt lgkmcnt(2)\n"
  "buffer_store_dwordx4 v[208:211], v1, s[4:7], s15 offen sc1\n"
  ".Lskip_21:\n"
  "s_waitcnt lgkmcnt(0)\n"
  "s_barrier\n"
  "ds_read_b128 v[64:67], v218 offset:0\n"
  "ds_read_b128 v[68:71], v218 offset:16\n"
  "ds_read_b128 v[72:75], v218 offset:32\n"
  "ds_read_b128 v[76:79], v218 offset:48\n"
  "ds_read_b128 v[80:83], v217 offset:0\n"
  "ds_read_b128 v[84:87], v217 offset:16\n"
  "ds_read_b128 v[88:91], v217 offset:32\n"
  "ds_read_b128 v[92:95], v217 offset:48\n"
  "buffer_load_dwordx4 v[40:43], v1, s[4:7], s29 offen sc1\n"
  "s_waitcnt lgkmcnt(7)\n"
  "v_mfma_f32_32x32x16_bf16 v[112:127], v[64:67], v[24:27], 0\n"
  "s_waitcnt lgkmcnt(6)\n"
  "v_mfma_f32_32x32x16_bf16 v[112:127], v[68:71], v[28:31], v[112:127]\n"
  "buffer_load_dwordx4 v[44:47], v1, s[4:7], s29 offen offset:1024 sc1\n"
  "s_waitcnt lgkmcnt(5)\n"
  "v_mfma_f32_32x32x16_bf16 v[112:127], v[72:75], v[32:35], v[112:127]\n"
  "s_waitcnt lgkmcnt(4)\n"
  "v_mfma_f32_32x32x16_bf16 v[112:127], v[76:79], v[36:39], v[112:127]\n"
  "buffer_load_dwordx4 v[48:51], v1, s[4:7], s29 offen offset:2048 sc1\n"
  "s_waitcnt lgkmcnt(3)\n"
  "v_mfma_f32_32x32x16_bf16 v[96:111], v[80:83], v[24:27], 0\n"
  "s_waitcnt lgkmcnt(2)\n"
  "v_mfma_f32_32x32x16_bf16 v[96:111], v[84:87], v[28:31], v[96:111]\n"
  "buffer_load_dwordx4 v[52:55], v1, s[4:7], s29 offen offset:3072 sc1\n"
  "s_waitcnt lgkmcnt(1)\n"
  "v_mfma_f32_32x32x16_bf16 v[96:111], v[88:91], v[32:35], v[96:111]\n"
  "s_waitcnt lgkmcnt(0)\n"
  "v_mfma_f32_32x32x16_bf16 v[96:111], v[92:95], v[36:39], v[96:111]\n"
  "s_nop 1\n"
  "v_add_f32 v112, 0x358637bd, v112\n"
  "v_rcp_f32 v112, v112\n"
  "s_nop 7\n"
  "v_pk_mul_f32 v[96:97], v[96:97], v[112:113] op_sel_hi:[1,0]\n"
  "v_pk_mul_f32 v[98:99], v[98:99], v[112:113] op_sel_hi:[1,0]\n"
  "v_pk_mul_f32 v[100:101], v[100:101], v[112:113] op_sel_hi:[1,0]\n"
  "v_pk_mul_f32 v[102:103], v[102:103], v[112:113] op_sel_hi:[1,0]\n"
  "v_cvt_pk_bf16_f32 v64, v96, v97\n"
  "v_cvt_pk_bf16_f32 v65, v98, v99\n"
  "v_cvt_pk_bf16_f32 v66, v100, v101\n"
  "v_cvt_pk_bf16_f32 v67, v102, v103\n"
  "buffer_store_dwordx4 v[64:67], v219, s[16:19], s26 offen sc1\n"
  "v_pk_mul_f32 v[104:105], v[104:105], v[112:113] op_sel_hi:[1,0]\n"
  "v_pk_mul_f32 v[106:107], v[106:107], v[112:113] op_sel_hi:[1,0]\n"
  "v_pk_mul_f32 v[108:109], v[108:109], v[112:113] op_sel_hi:[1,0]\n"
  "v_pk_mul_f32 v[110:111], v[110:111], v[112:113] op_sel_hi:[1,0]\n"
  "v_cvt_pk_bf16_f32 v68, v104, v105\n"
  "v_cvt_pk_bf16_f32 v69, v106, v107\n"
  "v_cvt_pk_bf16_f32 v70, v108, v109\n"
  "v_cvt_pk_bf16_f32 v71, v110, v111\n"
  "buffer_store_dwordx4 v[68:71], v219, s[16:19], s26 offen offset:256 sc1\n"
  "ds_read_b128 a[0:3], v1 offset:0\n"
  "ds_read_b128 a[4:7], v1 offset:1024\n"
  "ds_read_b128 a[8:11], v1 offset:2048\n"
  "ds_read_b128 a[12:15], v1 offset:3072\n"
  "ds_read_b128 a[16:19], v1 offset:4096\n"
  "ds_read_b128 a[20:23], v1 offset:5120\n"
  "ds_read_b128 a[24:27], v1 offset:6144\n"
  "ds_read_b128 a[28:31], v1 offset:7168\n"
  "ds_read_b128 a[32:35], v1 offset:8192\n"
  "ds_read_b128 a[36:39], v1 offset:9216\n"
  "ds_read_b128 a[40:43], v1 offset:10240\n"
  "ds_read_b128 a[44:47], v1 offset:11264\n"
  "ds_read_b128 a[48:51], v1 offset:12288\n"
  "ds_read_b128 a[52:55], v1 offset:13312\n"
  "ds_read_b128 a[56:59], v1 offset:14336\n"
  "ds_read_b128 a[60:63], v1 offset:15360\n"
  "ds_read_b128 a[64:67], v1 offset:16384\n"
  "ds_read_b128 a[68:71], v1 offset:17408\n"
  "ds_read_b128 a[72:75], v1 offset:18432\n"
  "ds_read_b128 a[76:79], v1 offset:19456\n"
  "s_waitcnt vmcnt(0)\n"
  "s_barrier\n"
  "s_mov_b32 exec_lo, 0xffff\n"
  "s_mov_b32 exec_hi, 0\n"
  "buffer_store_dwordx4 v[4:7], v14, s[16:19], s27 offen sc1\n"
  "s_mov_b64 exec, -1\n"
  "s_mov_b64 exec, 1\n"
  "buffer_store_dwordx2 v[4:5], off, s[4:7], s25 sc1\n"
  "s_mov_b64 exec, -1\n"
  "ds_read_b128 a[80:83], v1 offset:20480\n"
  "ds_read_b128 a[84:87], v1 offset:21504\n"
  "ds_read_b128 a[88:91], v1 offset:22528\n"
  "ds_read_b128 a[92:95], v1 offset:23552\n"
  "ds_read_b128 a[96:99], v1 offset:24576\n"
  "ds_read_b128 a[100:103], v1 offset:25600\n"
  "ds_read_b128 a[104:107], v1 offset:26624\n"
  "ds_read_b128 a[108:111], v1 offset:27648\n"
  "ds_read_b128 a[112:115], v1 offset:28672\n"
  "ds_read_b128 a[116:119], v1 offset:29696\n"
  "ds_read_b128 a[120:123], v1 offset:30720\n"
  "ds_read_b128 a[124:127], v1 offset:31744\n"
  "ds_read_b128 a[128:131], v1 offset:32768\n"
  "ds_read_b128 a[132:135], v1 offset:33792\n"
  "ds_read_b128 a[136:139], v1 offset:34816\n"
  "ds_read_b128 a[140:143], v1 offset:35840\n"
  "s_cmp_eq_u32 s80, 0\n"
  "s_cbranch_scc0 .Lskip_22\n"
  "s_mov_b64 exec, 0xffff\n"
  "v_mov_b64 v[18:19], 0\n"
  "buffer_atomic_or_x2 v[18:19], v10, s[16:19], s28 offen sc0 sc1\n"
  "s_mov_b64 exec, -1\n"
  ".Lskip_22:\n"
  "v_xor_b32 v7, s88, v41\n"
  "v_xor_b32 v23, s90, v40\n"
  "v_bitop3_b32 v7, s88, v43, v7 bitop3:0xbe\n"
  "v_bitop3_b32 v23, s90, v42, v23 bitop3:0xbe\n"
  "v_bitop3_b32 v7, s88, v45, v7 bitop3:0xbe\n"
  "v_bitop3_b32 v23, s90, v44, v23 bitop3:0xbe\n"
  "v_bitop3_b32 v7, s88, v47, v7 bitop3:0xbe\n"
  "v_bitop3_b32 v23, s90, v46, v23 bitop3:0xbe\n"
  "v_bitop3_b32 v7, s88, v49, v7 bitop3:0xbe\n"
  "v_bitop3_b32 v23, s90, v48, v23 bitop3:0xbe\n"
  "v_bitop3_b32 v7, s88, v51, v7 bitop3:0xbe\n"
  "v_bitop3_b32 v23, s90, v50, v23 bitop3:0xbe\n"
  "v_bitop3_b32 v7, s88, v53, v7 bitop3:0xbe\n"
  "v_bitop3_b32 v23, s90, v52, v23 bitop3:0xbe\n"
  "v_bitop3_b32 v7, s88, v55, v7 bitop3:0xbe\n"
  "v_bitop3_b32 v23, s90, v54, v23 bitop3:0xbe\n"
  "v_lshl_or_b32 v7, v23, 24, v7\n"
  "v_cmp_ne_u32 vcc, 0, v7\n"
  "s_cbranch_vccz .Lhave_23\n"
  "s_mov_b32 s94, 0x40000\n"
  ".Lpoll_24:\n"
  "buffer_load_dwordx4 v[40:43], v1, s[4:7], s29 offen sc1\n"
  "buffer_load_dwordx4 v[44:47], v1, s[4:7], s29 offen offset:1024 sc1\n"
  "buffer_load_dwordx4 v[48:51], v1, s[4:7], s29 offen offset:2048 sc1\n"
  "buffer_load_dwordx4 v[52:55], v1, s[4:7], s29 offen offset:3072 sc1\n"
  "s_waitcnt vmcnt(3)\n"
  "v_xor_b32 v7, s88, v41\n"
  "v_xor_b32 v23, s90, v40\n"
  "v_bitop3_b32 v7, s88, v43, v7 bitop3:0xbe\n"
  "v_bitop3_b32 v23, s90, v42, v23 bitop3:0xbe\n"
  "s_waitcnt vmcnt(2)\n"
  "v_bitop3_b32 v7, s88, v45, v7 bitop3:0xbe\n"
  "v_bitop3_b32 v23, s90, v44, v23 bitop3:0xbe\n"
  "v_bitop3_b32 v7, s88, v47, v7 bitop3:0xbe\n"
  "v_bitop3_b32 v23, s90, v46, v23 bitop3:0xbe\n"
  "s_waitcnt vmcnt(1)\n"
  "v_bitop3_b32 v7, s88, v49, v7 bitop3:0xbe\n"
  "v_bitop3_b32 v23, s90, v48, v23 bitop3:0xbe\n"
  "v_bitop3_b32 v7, s88, v51, v7 bitop3:0xbe\n"
  "v_bitop3_b32 v23, s90, v50, v23 bitop3:0xbe\n"
  "s_waitcnt vmcnt(0)\n"
  "v_bitop3_b32 v7, s88, v53, v7 bitop3:0xbe\n"
  "v_bitop3_b32 v23, s90, v52, v23 bitop3:0xbe\n"
  "v_bitop3_b32 v7, s88, v55, v7 bitop3:0xbe\n"
  "v_bitop3_b32 v23, s90, v54, v23 bitop3:0xbe\n"
  "v_lshl_or_b32 v7, v23, 24, v7\n"
  "v_cmp_ne_u32 vcc, 0, v7\n"
  "s_cbranch_vccz .Ldone_25\n"
  "s_sleep 2\n"
  "s_sub_u32 s94, s94, 1\n"
  "s_cbranch_scc0 .Lpoll_24\n"
  ".Ldone_25:\n"
  ".Lhave_23:\n"
  "s_lshl_b32 s36, s80, 8\n"
  "s_add_u32 s36, s36, 155648\n"
  "v_add_u32 v12, s36, v17\n"
  "ds_read_b128 a[144:147], v1 offset:36864\n"
  "v_and_b32 v40, s3, v40\n"
  "ds_read_b128 a[148:151], v1 offset:37888\n"
  "v_and_b32 v41, s3, v42\n"
  "ds_read_b128 a[152:155], v1 offset:38912\n"
  "v_and_b32 v44, s3, v44\n"
  "ds_read_b128 a[156:159], v1 offset:39936\n"
  "v_and_b32 v45, s3, v46\n"
  "ds_read_b128 a[160:163], v1 offset:40960\n"
  "v_and_b32 v48, s3, v48\n"
  "ds_read_b128 a[164:167], v1 offset:41984\n"
  "v_and_b32 v49, s3, v50\n"
  "ds_read_b128 a[168:171], v1 offset:43008\n"
  "v_and_b32 v52, s3, v52\n"
  "ds_read_b128 a[172:175], v1 offset:44032\n"
  "v_and_b32 v53, s3, v54\n"
  "ds_read_b128 a[176:179], v1 offset:45056\n"
  "v_pk_add_f32 v[40:41], v[40:41], v[44:45]\n"
  "ds_read_b128 a[180:183], v1 offset:46080\n"
  "v_pk_add_f32 v[48:49], v[48:49], v[52:53]\n"
  "ds_read_b128 a[184:187], v1 offset:47104\n"
  "v_pk_add_f32 v[40:41], v[40:41], v[48:49]\n"
  "ds_read_b128 a[188:191], v1 offset:48128\n"
  "v_mov_b32 v42, v40\n"
  "ds_read_b128 a[192:195], v1 offset:49152\n"
  "v_mov_b32 v43, v41\n"
  "ds_read_b128 a[196:199], v1 offset:50176\n"
  "v_permlane32_swap_b32 v40, v42\n"
  "ds_read_b128 a[200:203], v1 offset:51200\n"
  "v_permlane32_swap_b32 v41, v43\n"
  "ds_read_b128 a[204:207], v1 offset:52224\n"
  "v_pk_add_f32 v[40:41], v[40:41], v[42:43]\n"
  "ds_read_b128 a[208:211], v1 offset:53248\n"
  "v_cvt_pk_bf16_f32 v40, 0, v40\n"
  "ds_read_b128 a[212:215], v1 offset:54272\n"
  "v_cvt_pk_bf16_f32 v41, 0, v41\n"
  "ds_read_b128 a[216:219], v1 offset:55296\n"
  "v_add_f32 v40, s69, v40\n"
  "ds_read_b128 a[220:223], v1 offset:56320\n"
  "v_add_f32 v41, s69, v41\n"
  "ds_read_b128 a[224:227], v1 offset:57344\n"
  "v_cvt_pk_bf16_f32 v40, 0, v40\n"
  "ds_read_b128 a[228:231], v1 offset:58368\n"
  "v_cvt_pk_bf16_f32 v41, 0, v41\n"
  "ds_read_b128 a[232:235], v1 offset:59392\n"
  "ds_write_b64 v12, v[40:41]\n"
  "ds_read_b128 a[236:239], v1 offset:60416\n"
  "ds_read_b128 a[240:243], v1 offset:61440\n"
  "ds_read_b128 a[244:247], v1 offset:62464\n"
  "ds_read_b128 a[248:251], v1 offset:63488\n"
  "ds_read_b128 a[252:255], v1 offset:64512\n"
  "s_cmp_lg_u32 s31, 0\n"
  "s_cbranch_scc0 .Lskip_26\n"
  "v_cvt_pk_bf16_f32 v42, v40, v41\n"
  "v_mov_b32 v43, s88\n"
  "s_mov_b32 exec_hi, 0\n"
  "buffer_store_dwordx2 v[42:43], v17, s[4:7], s31 offen sc1\n"
  "s_mov_b64 exec, -1\n"
  ".Lskip_26:\n"
  "s_cmp_eq_u32 s80, 0\n"
  "s_cbranch_scc0 .Lskip_27\n"
  "s_mov_b64 exec, 0xffff\n"
  "s_waitcnt vmcnt(0)\n"
  "v_cmp_ne_u64 vcc, s[88:89], v[18:19]\n"
  "s_mov_b64 exec, -1\n"
  "s_cbranch_vccz .Lhave_28\n"
  "s_mov_b64 exec, 0xffff\n"
  "s_mov_b32 s94, 0x40000\n"
  ".Lpoll_29:\n"
  "v_mov_b64 v[18:19], 0\n"
  "buffer_atomic_or_x2 v[18:19], v10, s[16:19], s28 offen sc0 sc1\n"
  "s_waitcnt vmcnt(0)\n"
  "v_cmp_ne_u64 vcc, s[88:89], v[18:19]\n"
  "s_cbranch_vccz .Ldone_30\n"
  "s_sub_u32 s94, s94, 1\n"
  "s_cbranch_scc0 .Lpoll_29\n"
  ".Ldone_30:\n"
  "s_mov_b64 exec, -1\n"
  ".Lhave_28:\n"
  ".Lskip_27:\n"
  "s_waitcnt lgkmcnt(0)\n"
  "s_barrier\n"
  "buffer_load_dwordx4 v[64:67], v1, s[16:19], s30 offen sc1\n"
  "buffer_load_dwordx4 v[68:71], v1, s[16:19], s30 offen offset:1024 sc1\n"
  "buffer_load_dwordx4 v[72:75], v1, s[16:19], s30 offen offset:2048 sc1\n"
  "v_lshrrev_b32 v3, 4, v2\n"
  "v_lshlrev_b32 v14, 4, v3\n"
  "v_add_u32 v14, 155648, v14\n"
  "ds_read_b128 v[160:163], v14 offset:0\n"
  "buffer_load_dwordx4 v[76:79], v1, s[16:19], s30 offen offset:3072 sc1\n"
  "ds_read_b128 v[176:179], v14 offset:512\n"
  "ds_read_b128 v[164:167], v14 offset:64\n"
  "ds_read_b128 v[180:183], v14 offset:576\n"
  "ds_read_b128 v[168:171], v14 offset:128\n"
  "s_addk_i32 s30, 4096\n"
  "buffer_load_dwordx4 v[80:83], v1, s[16:19], s30 offen sc1\n"
  "ds_read_b128 v[184:187], v14 offset:640\n"
  "ds_read_b128 v[172:175], v14 offset:192\n"
  "ds_read_b128 v[188:191], v14 offset:704\n"
  "v_lshlrev_b32 v144, 16, v232\n"
  "buffer_load_dwordx2 v[60:61], v222, s[4:7], s24 offen sc1\n"
  "buffer_load_dwordx4 v[84:87], v1, s[16:19], s30 offen offset:1024 sc1\n"
  "v_and_b32 v145, s91, v232\n"
  "v_lshlrev_b32 v146, 16, v233\n"
  "v_and_b32 v147, s91, v233\n"
  "v_lshlrev_b32 v148, 16, v234\n"
  "buffer_load_dwordx4 v[88:91], v1, s[16:19], s30 offen offset:2048 sc1\n"
  "v_and_b32 v149, s91, v234\n"
  "v_lshlrev_b32 v150, 16, v235\n"
  "v_and_b32 v151, s91, v235\n"
  "v_lshlrev_b32 v152, 16, v236\n"
  "buffer_load_dwordx4 v[92:95], v1, s[16:19], s30 offen offset:3072 sc1\n"
  "v_and_b32 v153, s91, v236\n"
  "v_lshlrev_b32 v154, 16, v237\n"
  "v_and_b32 v155, s91, v237\n"
  "v_lshlrev_b32 v156, 16, v238\n"
  "s_addk_i32 s30, 12288\n"
  "buffer_load_dwordx4 v[96:99], v1, s[16:19], s30 offen sc1\n"
  "v_and_b32 v157, s91, v238\n"
  "v_lshlrev_b32 v158, 16, v239\n"
  "v_and_b32 v159, s91, v239\n"
  "v_lshrrev_b32 v3, 2, v2\n"
  "buffer_load_dwordx4 v[100:103], v1, s[16:19], s30 offen offset:1024 sc1\n"
  "v_lshlrev_b32 v21, 9, v3\n"
  "v_and_b32 v3, 3, v2\n"
  "v_lshl_or_b32 v21, v3, 4, v21\n"
  "v_lshlrev_b32 v17, 3, v17\n"
  "s_waitcnt vmcnt(9)\n"
  "v_mfma_f32_16x16x32_bf16 v[128:131], a[0:3], v[64:67], 0\n"
  "s_xor_b32 s36, s80, 2\n"
  "s_lshl_b32 s37, s36, 9\n"
  "v_mfma_f32_16x16x32_bf16 v[128:131], a[16:19], v[68:71], v[128:131]\n"
  "buffer_load_dwordx4 v[104:107], v1, s[16:19], s30 offen offset:2048 sc1\n"
  "s_add_u32 s37, s37, 161792\n"
  "v_mfma_f32_16x16x32_bf16 v[132:135], a[4:7], v[64:67], 0\n"
  "v_lshrrev_b32 v3, 3, v2\n"
  "v_lshlrev_b32 v216, 6, v3\n"
  "v_mfma_f32_16x16x32_bf16 v[132:135], a[20:23], v[68:71], v[132:135]\n"
  "v_and_b32 v3, 3, v2\n"
  "v_lshl_or_b32 v216, v3, 4, v216\n"
  "v_mfma_f32_16x16x32_bf16 v[136:139], a[8:11], v[64:67], 0\n"
  "v_bfe_u32 v3, v2, 2, 1\n"
  "v_lshl_or_b32 v216, v3, 3, v216\n"
  "v_mfma_f32_16x16x32_bf16 v[136:139], a[24:27], v[68:71], v[136:139]\n"
  "buffer_load_dwordx4 v[108:111], v1, s[16:19], s30 offen offset:3072 sc1\n"
  "v_add_u32 v216, s37, v216\n"
  "v_mfma_f32_16x16x32_bf16 v[140:143], a[12:15], v[64:67], 0\n"
  "v_add_u32 v15, 57344, v1\n"
  "v_add_u32 v16, 122880, v1\n"
  "v_mfma_f32_16x16x32_bf16 v[140:143], a[28:31], v[68:71], v[140:143]\n"
  "v_lshl_add_u32 v219, s81, 2, v15\n"
  "v_lshl_add_u32 v220, s81, 1, v16\n"
  "s_waitcnt vmcnt(9)\n"
  "v_mfma_f32_16x16x32_bf16 v[128:131], a[32:35], v[72:75], v[128:131]\n"
  "s_lshl_b32 s36, s80, 8\n"
  "s_add_u32 s36, s36, 161792\n"
  "v_mfma_f32_16x16x32_bf16 v[128:131], a[48:51], v[76:79], v[128:131]\n"
  "s_addk_i32 s30, 4096\n"
  "buffer_load_dwordx4 v[112:115], v1, s[16:19], s30 offen sc1\n"
  "v_bfe_u32 v3, v2, 2, 1\n"
  "v_mfma_f32_16x16x32_bf16 v[132:135], a[36:39], v[72:75], v[132:135]\n"
  "v_lshlrev_b32 v217, 10, v3\n"
  "v_lshrrev_b32 v3, 4, v2\n"
  "v_mfma_f32_16x16x32_bf16 v[132:135], a[52:55], v[76:79], v[132:135]\n"
  "v_lshl_or_b32 v217, v3, 4, v217\n"
  "v_add_u32 v217, s36, v217\n"
  "v_mfma_f32_16x16x32_bf16 v[136:139], a[40:43], v[72:75], v[136:139]\n"
  "v_and_b32 v3, 31, v2\n"
  "v_lshlrev_b32 v22, 4, v3\n"
  "v_mfma_f32_16x16x32_bf16 v[136:139], a[56:59], v[76:79], v[136:139]\n"
  "buffer_load_dwordx4 v[116:119], v1, s[16:19], s30 offen offset:1024 sc1\n"
  "v_add_u32 v22, 156672, v22\n"
  "v_mfma_f32_16x16x32_bf16 v[140:143], a[44:47], v[72:75], v[140:143]\n"
  "v_lshl_add_u32 v218, s80, 2, v22\n"
  "v_add_u32 v12, 65536, v1\n"
  "v_mfma_f32_16x16x32_bf16 v[140:143], a[60:63], v[76:79], v[140:143]\n"
  "v_add_u32 v13, 98304, v1\n"
  "v_lshrrev_b32 v3, 5, v2\n"
  "s_waitcnt vmcnt(8)\n"
  "v_mfma_f32_16x16x32_bf16 v[128:131], a[64:67], v[80:83], v[128:131]\n"
  "v_lshlrev_b32 v14, 17, v3\n"
  "v_and_b32 v3, 31, v2\n"
  "v_mfma_f32_16x16x32_bf16 v[128:131], a[80:83], v[84:87], v[128:131]\n"
  "buffer_load_dwordx4 v[120:123], v1, s[16:19], s30 offen offset:2048 sc1\n"
  "v_lshl_or_b32 v14, v3, 4, v14\n"
  "v_mfma_f32_16x16x32_bf16 v[132:135], a[68:71], v[80:83], v[132:135]\n"
  "v_mfma_f32_16x16x32_bf16 v[132:135], a[84:87], v[84:87], v[132:135]\n"
  "v_mfma_f32_16x16x32_bf16 v[136:139], a[72:75], v[80:83], v[136:139]\n"
  "v_mfma_f32_16x16x32_bf16 v[136:139], a[88:91], v[84:87], v[136:139]\n"
  "buffer_load_dwordx4 v[124:127], v1, s[16:19], s30 offen offset:3072 sc1\n"
  "v_mfma_f32_16x16x32_bf16 v[140:143], a[76:79], v[80:83], v[140:143]\n"
  "v_mfma_f32_16x16x32_bf16 v[140:143], a[92:95], v[84:87], v[140:143]\n"
  "s_waitcnt vmcnt(8)\n"
  "v_cmp_ne_u64 vcc, s[88:89], v[60:61]\n"
  "s_cbranch_vccz .Lhave_31\n"
  "s_mov_b32 s94, 0x40000\n"
  ".Lpoll_32:\n"
  "buffer_load_dwordx2 v[60:61], v222, s[4:7], s24 offen sc1\n"
  "s_waitcnt vmcnt(0)\n"
  "v_cmp_ne_u64 vcc, s[88:89], v[60:61]\n"
  "s_cbranch_vccz .Ldone_33\n"
  "s_sleep 8\n"
  "s_sub_u32 s94, s94, 1\n"
  "s_cbranch_scc0 .Lpoll_32\n"
  ".Ldone_33:\n"
  ".Lhave_31:\n"
  "v_mfma_f32_16x16x32_bf16 v[128:131], a[96:99], v[88:91], v[128:131]\n"
  "v_mfma_f32_16x16x32_bf16 v[128:131], a[112:115], v[92:95], v[128:131]\n"
  "s_add_u32 m0, s81, 57344\n"
  "s_add_u32 s95, s10, s81\n"
  "buffer_load_dwordx4 v1, s[4:7], s95 offen offset:0 sc1 lds\n"
  "v_mfma_f32_16x16x32_bf16 v[132:135], a[100:103], v[88:91], v[132:135]\n"
  "v_mfma_f32_16x16x32_bf16 v[132:135], a[116:119], v[92:95], v[132:135]\n"
  "v_mfma_f32_16x16x32_bf16 v[136:139], a[104:107], v[88:91], v[136:139]\n"
  "v_mfma_f32_16x16x32_bf16 v[136:139], a[120:123], v[92:95], v[136:139]\n"
  "buffer_load_dwordx4 v1, s[4:7], s95 offen offset:1024 sc1 lds\n"
  "v_mfma_f32_16x16x32_bf16 v[140:143], a[108:111], v[88:91], v[140:143]\n"
  "v_mfma_f32_16x16x32_bf16 v[140:143], a[124:127], v[92:95], v[140:143]\n"
  "s_waitcnt vmcnt(8)\n"
  "v_mfma_f32_16x16x32_bf16 v[128:131], a[128:131], v[96:99], v[128:131]\n"
  "v_mfma_f32_16x16x32_bf16 v[128:131], a[144:147], v[100:103], v[128:131]\n"
  "buffer_load_dwordx4 v1, s[4:7], s95 offen offset:2048 sc1 lds\n"
  "v_mfma_f32_16x16x32_bf16 v[132:135], a[132:135], v[96:99], v[132:135]\n"
  "v_mfma_f32_16x16x32_bf16 v[132:135], a[148:151], v[100:103], v[132:135]\n"
  "v_mfma_f32_16x16x32_bf16 v[136:139], a[136:139], v[96:99], v[136:139]\n"
  "v_mfma_f32_16x16x32_bf16 v[136:139], a[152:155], v[100:103], v[136:139]\n"
  "buffer_load_dwordx4 v1, s[4:7], s95 offen offset:3072 sc1 lds\n"
  "v_mfma_f32_16x16x32_bf16 v[140:143], a[140:143], v[96:99], v[140:143]\n"
  "v_mfma_f32_16x16x32_bf16 v[140:143], a[156:159], v[100:103], v[140:143]\n"
  "s_waitcnt vmcnt(8)\n"
  "v_mfma_f32_16x16x32_bf16 v[128:131], a[160:163], v[104:107], v[128:131]\n"
  "v_mfma_f32_16x16x32_bf16 v[128:131], a[176:179], v[108:111], v[128:131]\n"
  "s_add_u32 m0, s81, 73728\n"
  "s_add_u32 s95, s10, s81\n"
  "s_add_u32 s95, s95, 16384\n"
  "buffer_load_dwordx4 v1, s[4:7], s95 offen offset:0 sc1 lds\n"
  "v_mfma_f32_16x16x32_bf16 v[132:135], a[164:167], v[104:107], v[132:135]\n"
  "v_mfma_f32_16x16x32_bf16 v[132:135], a[180:183], v[108:111], v[132:135]\n"
  "v_mfma_f32_16x16x32_bf16 v[136:139], a[168:171], v[104:107], v[136:139]\n"
  "v_mfma_f32_16x16x32_bf16 v[136:139], a[184:187], v[108:111], v[136:139]\n"
  "buffer_load_dwordx4 v1, s[4:7], s95 offen offset:1024 sc1 lds\n"
  "v_mfma_f32_16x16x32_bf16 v[140:143], a[172:175], v[104:107], v[140:143]\n"
  "v_mfma_f32_16x16x32_bf16 v[140:143], a[188:191], v[108:111], v[140:143]\n"
  "s_waitcnt vmcnt(8)\n"
  "v_mfma_f32_16x16x32_bf16 v[128:131], a[192:195], v[112:115], v[128:131]\n"
  "v_mfma_f32_16x16x32_bf16 v[128:131], a[208:211], v[116:119], v[128:131]\n"
  "buffer_load_dwordx4 v1, s[4:7], s95 offen offset:2048 sc1 lds\n"
  "v_mfma_f32_16x16x32_bf16 v[132:135], a[196:199], v[112:115], v[132:135]\n"
  "v_mfma_f32_16x16x32_bf16 v[132:135], a[212:215], v[116:119], v[132:135]\n"
  "v_mfma_f32_16x16x32_bf16 v[136:139], a[200:203], v[112:115], v[136:139]\n"
  "v_mfma_f32_16x16x32_bf16 v[136:139], a[216:219], v[116:119], v[136:139]\n"
  "buffer_load_dwordx4 v1, s[4:7], s95 offen offset:3072 sc1 lds\n"
  "v_mfma_f32_16x16x32_bf16 v[140:143], a[204:207], v[112:115], v[140:143]\n"
  "v_mfma_f32_16x16x32_bf16 v[140:143], a[220:223], v[116:119], v[140:143]\n"
  "s_waitcnt vmcnt(8)\n"
  "v_mfma_f32_16x16x32_bf16 v[128:131], a[224:227], v[120:123], v[128:131]\n"
  "v_mfma_f32_16x16x32_bf16 v[128:131], a[240:243], v[124:127], v[128:131]\n"
  "v_mfma_f32_16x16x32_bf16 v[132:135], a[228:231], v[120:123], v[132:135]\n"
  "v_mfma_f32_16x16x32_bf16 v[132:135], a[244:247], v[124:127], v[132:135]\n"
  "v_mfma_f32_16x16x32_bf16 v[136:139], a[232:235], v[120:123], v[136:139]\n"
  "v_mfma_f32_16x16x32_bf16 v[136:139], a[248:251], v[124:127], v[136:139]\n"
  "v_mfma_f32_16x16x32_bf16 v[140:143], a[236:239], v[120:123], v[140:143]\n"
  "v_mfma_f32_16x16x32_bf16 v[140:143], a[252:255], v[124:127], v[140:143]\n"
  "buffer_load_dwordx2 v[62:63], v17, s[4:7], s67 offen sc1\n"
  "buffer_load_dwordx4 v[212:215], v1, s[4:7], s68 offen sc1\n"
  "s_waitcnt lgkmcnt(7)\n"
  "v_pk_fma_f32 v[240:241], v[128:129], v[160:161], v[144:145]\n"
  "v_pk_fma_f32 v[242:243], v[130:131], v[162:163], v[146:147]\n"
  "s_waitcnt lgkmcnt(5)\n"
  "v_pk_fma_f32 v[244:245], v[132:133], v[164:165], v[148:149]\n"
  "v_pk_fma_f32 v[246:247], v[134:135], v[166:167], v[150:151]\n"
  "v_pk_mul_f32 v[176:177], v[240:241], v[176:177]\n"
  "v_pk_mul_f32 v[178:179], v[242:243], v[178:179]\n"
  "s_waitcnt lgkmcnt(4)\n"
  "v_pk_mul_f32 v[180:181], v[244:245], v[180:181]\n"
  "v_pk_mul_f32 v[182:183], v[246:247], v[182:183]\n"
  "v_cvt_pk_bf16_f32 v64, v176, v177\n"
  "v_cvt_pk_bf16_f32 v65, v178, v179\n"
  "v_cvt_pk_bf16_f32 v66, v180, v181\n"
  "v_cvt_pk_bf16_f32 v67, v182, v183\n"
  "buffer_store_dwordx4 v[64:67], v1, s[16:19], s64 offen sc1\n"
  "s_waitcnt lgkmcnt(3)\n"
  "v_pk_fma_f32 v[248:249], v[136:137], v[168:169], v[152:153]\n"
  "v_pk_fma_f32 v[250:251], v[138:139], v[170:171], v[154:155]\n"
  "s_waitcnt lgkmcnt(1)\n"
  "v_pk_fma_f32 v[252:253], v[140:141], v[172:173], v[156:157]\n"
  "v_pk_fma_f32 v[254:255], v[142:143], v[174:175], v[158:159]\n"
  "v_pk_mul_f32 v[184:185], v[248:249], v[184:185]\n"
  "v_pk_mul_f32 v[186:187], v[250:251], v[186:187]\n"
  "s_waitcnt lgkmcnt(0)\n"
  "v_pk_mul_f32 v[188:189], v[252:253], v[188:189]\n"
  "v_pk_mul_f32 v[190:191], v[254:255], v[190:191]\n"
  "v_cvt_pk_bf16_f32 v68, v184, v185\n"
  "v_cvt_pk_bf16_f32 v69, v186, v187\n"
  "v_cvt_pk_bf16_f32 v70, v188, v189\n"
  "v_cvt_pk_bf16_f32 v71, v190, v191\n"
  "buffer_store_dwordx4 v[68:71], v1, s[16:19], s64 offen offset:1024 sc1\n"
  "v_pk_add_f32 v[72:73], v[240:241], v[242:243]\n"
  "v_pk_mul_f32 v[74:75], v[240:241], v[240:241]\n"
  "v_pk_fma_f32 v[74:75], v[242:243], v[242:243], v[74:75]\n"
  "v_pk_add_f32 v[72:73], v[72:73], v[244:245]\n"
  "v_pk_fma_f32 v[74:75], v[244:245], v[244:245], v[74:75]\n"
  "v_pk_add_f32 v[72:73], v[72:73], v[246:247]\n"
  "v_pk_fma_f32 v[74:75], v[246:247], v[246:247], v[74:75]\n"
  "v_pk_add_f32 v[72:73], v[72:73], v[248:249]\n"
  "v_pk_fma_f32 v[74:75], v[248:249], v[248:249], v[74:75]\n"
  "v_pk_add_f32 v[72:73], v[72:73], v[250:251]\n"
  "v_pk_fma_f32 v[74:75], v[250:251], v[250:251], v[74:75]\n"
  "v_pk_add_f32 v[72:73], v[72:73], v[252:253]\n"
  "v_pk_fma_f32 v[74:75], v[252:253], v[252:253], v[74:75]\n"
  "v_pk_add_f32 v[72:73], v[72:73], v[254:255]\n"
  "v_pk_fma_f32 v[74:75], v[254:255], v[254:255], v[74:75]\n"
  "v_add_f32 v72, v72, v73\n"
  "v_add_f32 v73, v74, v75\n"
  "s_nop 1\n"
  "v_permlane32_swap_b32 v72, v73\n"
  "v_add_f32 v72, v72, v73\n"
  "v_mov_b32 v73, v72\n"
  "s_nop 1\n"
  "v_permlane16_swap_b32 v72, v73\n"
  "v_add_f32 v72, v72, v73\n"
  "v_mov_b32 v73, v72\n"
  "s_nop 1\n"
  "v_permlane32_swap_b32 v72, v73\n"
  "s_mov_b64 exec, 0xffff\n"
  "buffer_store_dwordx2 v[72:73], v8, s[16:19], s65 offen sc1\n"
  "s_mov_b64 exec, -1\n"
  "s_add_u32 s93, s82, 0\n"
  "s_and_b32 s93, s93, 0xffff\n"
  "s_add_u32 m0, s93, 90112\n"
  "s_add_u32 s95, s93, s10\n"
  "s_add_u32 s95, s95, 32768\n"
  "buffer_load_dwordx4 v1, s[4:7], s95 offen offset:0 sc1 lds\n"
  "buffer_load_dwordx4 v1, s[4:7], s95 offen offset:1024 sc1 lds\n"
  "buffer_load_dwordx4 v1, s[4:7], s95 offen offset:2048 sc1 lds\n"
  "buffer_load_dwordx4 v1, s[4:7], s95 offen offset:3072 sc1 lds\n"
  "s_add_u32 s93, s82, 16384\n"
  "s_and_b32 s93, s93, 0xffff\n"
  "s_add_u32 m0, s93, 90112\n"
  "s_add_u32 s95, s93, s10\n"
  "s_add_u32 s95, s95, 32768\n"
  "buffer_load_dwordx4 v1, s[4:7], s95 offen offset:0 sc1 lds\n"
  "buffer_load_dwordx4 v1, s[4:7], s95 offen offset:1024 sc1 lds\n"
  "s_waitcnt vmcnt(6)\n"
  "s_barrier\n"
  "s_mov_b32 exec_lo, s79\n"
  "s_mov_b32 exec_hi, 0\n"
  "buffer_store_dwordx4 v[4:7], v21, s[16:19], s66 offen sc1\n"
  "s_mov_b64 exec, -1\n"
  "buffer_load_dwordx4 v1, s[4:7], s95 offen offset:2048 sc1 lds\n"
  "buffer_load_dwordx4 v1, s[4:7], s95 offen offset:3072 sc1 lds\n"
  "s_add_u32 s93, s82, 32768\n"
  "s_and_b32 s93, s93, 0xffff\n"
  "s_add_u32 m0, s93, 90112\n"
  "s_add_u32 s95, s93, s10\n"
  "s_add_u32 s95, s95, 32768\n"
  "buffer_load_dwordx4 v1, s[4:7], s95 offen offset:0 sc1 lds\n"
  "buffer_load_dwordx4 v1, s[4:7], s95 offen offset:1024 sc1 lds\n"
  "buffer_load_dwordx4 v1, s[4:7], s95 offen offset:2048 sc1 lds\n"
  "buffer_load_dwordx4 v1, s[4:7], s95 offen offset:3072 sc1 lds\n"
  "s_add_u32 s93, s82, 49152\n"
  "s_and_b32 s93, s93, 0xffff\n"
  "s_add_u32 m0, s93, 90112\n"
  "s_add_u32 s95, s93, s10\n"
  "s_add_u32 s95, s95, 32768\n"
  "buffer_load_dwordx4 v1, s[4:7], s95 offen offset:0 sc1 lds\n"
  "buffer_load_dwordx4 v1, s[4:7], s95 offen offset:1024 sc1 lds\n"
  "buffer_load_dwordx4 v1, s[4:7], s95 offen offset:2048 sc1 lds\n"
  "buffer_load_dwordx4 v1, s[4:7], s95 offen offset:3072 sc1 lds\n"
  "s_mov_b64 exec, 0xff\n"
  "v_mov_b64 v[18:19], 0\n"
  "buffer_atomic_or_x2 v[18:19], v10, s[16:19], s70 offen sc0 sc1\n"
  "s_mov_b64 exec, -1\n"
  "v_cmp_ne_u64 vcc, s[88:89], v[62:63]\n"
  "s_cbranch_vccz .Lhave_34\n"
  "s_mov_b32 s94, 0x40000\n"
  ".Lpoll_35:\n"
  "buffer_load_dwordx2 v[62:63], v17, s[4:7], s67 offen sc1\n"
  "s_waitcnt vmcnt(0)\n"
  "v_cmp_ne_u64 vcc, s[88:89], v[62:63]\n"
  "s_cbranch_vccz .Ldone_36\n"
  "s_sleep 8\n"
  "s_sub_u32 s94, s94, 1\n"
  "s_cbranch_scc0 .Lpoll_35\n"
  ".Ldone_36:\n"
  ".Lhave_34:\n"
  "v_xor_b32 v7, s88, v213\n"
  "v_bitop3_b32 v7, s88, v215, v7 bitop3:0xbe\n"
  "v_cmp_ne_u32 vcc, 0, v7\n"
  "s_cbranch_vccz .Lhave_37\n"
  "s_mov_b32 s94, 0x40000\n"
  ".Lpoll_38:\n"
  "buffer_load_dwordx4 v[212:215], v1, s[4:7], s68 offen sc1\n"
  "s_waitcnt vmcnt(0)\n"
  "v_xor_b32 v7, s88, v213\n"
  "v_bitop3_b32 v7, s88, v215, v7 bitop3:0xbe\n"
  "v_cmp_ne_u32 vcc, 0, v7\n"
  "s_cbranch_vccz .Ldone_39\n"
  "s_sleep 2\n"
  "s_sub_u32 s94, s94, 1\n"
  "s_cbranch_scc0 .Lpoll_38\n"
  ".Ldone_39:\n"
  ".Lhave_37:\n"
  "v_mov_b32 v213, v214\n"
  "ds_write_b64 v216, v[212:213]\n"
  "s_or_b32 s36, s2, s80\n"
  "s_cbranch_scc1 .Lskip_40\n"
  "s_mov_b64 exec, 1\n"
  "buffer_store_dwordx2 v[4:5], off, s[4:7], 0 sc1\n"
  "s_mov_b64 exec, -1\n"
  ".Lskip_40:\n"
  "v_bfe_u32 v3, v2, 4, 1\n"
  "v_lshlrev_b32 v21, 14, v3\n"
  "v_bfe_u32 v3, v2, 2, 2\n"
  "v_lshl_or_b32 v21, v3, 11, v21\n"
  "v_and_b32 v3, 3, v2\n"
  "v_lshl_or_b32 v21, v3, 4, v21\n"
  "s_cmp_lt_u32 s87, 29\n"
  "s_cbranch_scc0 .Lno3_41\n"
  "v_mov_b64 v[36:37], 0\n"
  "v_mov_b64 v[42:43], 0\n"
  "v_mov_b64 v[46:47], 0\n"
  "v_mov_b64 v[48:49], 0\n"
  "v_mov_b64 v[50:51], 0\n"
  "v_mov_b64 v[52:53], 0\n"
  "v_mov_b64 v[54:55], 0\n"
  "s_mov_b64 exec, 0xff\n"
  "s_waitcnt vmcnt(0)\n"
  "v_cmp_ne_u64 vcc, s[88:89], v[18:19]\n"
  "s_mov_b64 exec, -1\n"
  "s_cbranch_vccz .Lhave_43\n"
  "s_mov_b64 exec, 0xff\n"
  "s_mov_b32 s94, 0x40000\n"
  ".Lpoll_44:\n"
  "v_mov_b64 v[18:19], 0\n"
  "buffer_atomic_or_x2 v[18:19], v10, s[16:19], s70 offen sc0 sc1\n"
  "s_waitcnt vmcnt(0)\n"
  "v_cmp_ne_u64 vcc, s[88:89], v[18:19]\n"
  "s_cbranch_vccz .Ldone_45\n"
  "s_sub_u32 s94, s94, 1\n"
  "s_cbranch_scc0 .Lpoll_44\n"
  ".Ldone_45:\n"
  "s_mov_b64 exec, -1\n"
  ".Lhave_43:\n"
  "buffer_load_dwordx4 a[0:3], v1, s[16:19], s71 offen sc1\n"
  "buffer_load_dwordx4 a[4:7], v1, s[16:19], s71 offen offset:1024 sc1\n"
  "buffer_load_dwordx4 a[64:67], v11, s[16:19], s71 offen sc1\n"
  "s_waitcnt lgkmcnt(0)\n"
  "s_barrier\n"
  "ds_read_b128 v[136:139], v217 offset:0\n"
  "ds_read_b128 v[88:91], v219 offset:0\n"
  "ds_read_b128 v[92:95], v219 offset:1024\n"
  "ds_read_b128 v[96:99], v219 offset:2048\n"
  "ds_read_b128 v[100:103], v219 offset:3072\n"
  "ds_read_b128 v[104:107], v220 offset:0\n"
  "ds_read_b128 v[108:111], v220 offset:1024\n"
  "ds_read_b128 v[140:143], v217 offset:64\n"
  "ds_read_b128 v[112:115], v219 offset:4096\n"
  "ds_read_b128 v[116:119], v219 offset:5120\n"
  "s_waitcnt lgkmcnt(8)\n"
  "v_mfma_f32_16x16x32_bf16 v[64:67], v[136:139], v[88:91], 0\n"
  "buffer_load_dwordx4 a[68:71], v11, s[16:19], s71 offen offset:1024 sc1\n"
  "ds_read_b128 v[120:123], v219 offset:6144\n"
  "s_waitcnt lgkmcnt(8)\n"
  "v_mfma_f32_16x16x32_bf16 v[68:71], v[136:139], v[92:95], 0\n"
  "ds_read_b128 v[124:127], v219 offset:7168\n"
  "s_waitcnt lgkmcnt(8)\n"
  "v_mfma_f32_16x16x32_bf16 v[72:75], v[136:139], v[96:99], 0\n"
  "ds_read_b128 v[128:131], v220 offset:2048\n"
  "s_waitcnt lgkmcnt(8)\n"
  "v_mfma_f32_16x16x32_bf16 v[76:79], v[136:139], v[100:103], 0\n"
  "buffer_load_dwordx4 a[128:131], v12, s[16:19], s71 offen sc1\n"
  "ds_read_b128 v[132:135], v220 offset:3072\n"
  "s_waitcnt lgkmcnt(8)\n"
  "v_mfma_f32_16x16x32_bf16 v[80:83], v[136:139], v[104:107], 0\n"
  "ds_read_b128 v[144:147], v217 offset:128\n"
  "s_waitcnt lgkmcnt(8)\n"
  "v_mfma_f32_16x16x32_bf16 v[84:87], v[136:139], v[108:111], 0\n"
  "ds_read_b128 v[148:151], v217 offset:192\n"
  "s_waitcnt lgkmcnt(7)\n"
  "v_mfma_f32_16x16x32_bf16 v[64:67], v[140:143], v[112:115], v[64:67]\n"
  "buffer_load_dwordx4 a[132:135], v12, s[16:19], s71 offen offset:1024 sc1\n"
  "ds_read_b128 v[88:91], v219 offset:8192\n"
  "s_waitcnt lgkmcnt(7)\n"
  "v_mfma_f32_16x16x32_bf16 v[68:71], v[140:143], v[116:119], v[68:71]\n"
  "ds_read_b128 v[92:95], v219 offset:9216\n"
  "s_waitcnt lgkmcnt(7)\n"
  "v_mfma_f32_16x16x32_bf16 v[72:75], v[140:143], v[120:123], v[72:75]\n"
  "ds_read_b128 v[96:99], v219 offset:10240\n"
  "s_waitcnt lgkmcnt(7)\n"
  "v_mfma_f32_16x16x32_bf16 v[76:79], v[140:143], v[124:127], v[76:79]\n"
  "buffer_load_dwordx4 a[192:195], v13, s[16:19], s71 offen sc1\n"
  "ds_read_b128 v[100:103], v219 offset:11264\n"
  "s_waitcnt lgkmcnt(7)\n"
  "v_mfma_f32_16x16x32_bf16 v[80:83], v[140:143], v[128:131], v[80:83]\n"
  "ds_read_b128 v[104:107], v220 offset:4096\n"
  "s_waitcnt lgkmcnt(7)\n"
  "v_mfma_f32_16x16x32_bf16 v[84:87], v[140:143], v[132:135], v[84:87]\n"
  "ds_read_b128 v[108:111], v220 offset:5120\n"
  "s_waitcnt lgkmcnt(5)\n"
  "v_mfma_f32_16x16x32_bf16 v[64:67], v[144:147], v[88:91], v[64:67]\n"
  "buffer_load_dwordx4 a[196:199], v13, s[16:19], s71 offen offset:1024 sc1\n"
  "ds_read_b128 v[112:115], v219 offset:12288\n"
  "s_waitcnt lgkmcnt(5)\n"
  "v_mfma_f32_16x16x32_bf16 v[68:71], v[144:147], v[92:95], v[68:71]\n"
  "ds_read_b128 v[116:119], v219 offset:13312\n"
  "s_waitcnt lgkmcnt(5)\n"
  "v_mfma_f32_16x16x32_bf16 v[72:75], v[144:147], v[96:99], v[72:75]\n"
  "ds_read_b128 v[120:123], v219 offset:14336\n"
  "s_waitcnt lgkmcnt(5)\n"
  "v_mfma_f32_16x16x32_bf16 v[76:79], v[144:147], v[100:103], v[76:79]\n"
  "buffer_load_dwordx4 a[8:11], v1, s[16:19], s71 offen offset:2048 sc1\n"
  "ds_read_b128 v[124:127], v219 offset:15360\n"
  "s_waitcnt lgkmcnt(5)\n"
  "v_mfma_f32_16x16x32_bf16 v[80:83], v[144:147], v[104:107], v[80:83]\n"
  "ds_read_b128 v[128:131], v220 offset:6144\n"
  "s_waitcnt lgkmcnt(5)\n"
  "v_mfma_f32_16x16x32_bf16 v[84:87], v[144:147], v[108:111], v[84:87]\n"
  "ds_read_b128 v[132:135], v220 offset:7168\n"
  "s_waitcnt lgkmcnt(5)\n"
  "v_mfma_f32_16x16x32_bf16 v[64:67], v[148:151], v[112:115], v[64:67]\n"
  "buffer_load_dwordx4 a[12:15], v1, s[16:19], s71 offen offset:3072 sc1\n"
  "ds_read_b128 v[160:163], v15 offset:0\n"
  "s_waitcnt lgkmcnt(5)\n"
  "v_mfma_f32_16x16x32_bf16 v[68:71], v[148:151], v[116:119], v[68:71]\n"
  "ds_read_b128 v[164:167], v15 offset:4096\n"
  "s_waitcnt lgkmcnt(5)\n"
  "v_mfma_f32_16x16x32_bf16 v[72:75], v[148:151], v[120:123], v[72:75]\n"
  "ds_read_b128 v[168:171], v15 offset:1024\n"
  "s_waitcnt lgkmcnt(5)\n"
  "v_mfma_f32_16x16x32_bf16 v[76:79], v[148:151], v[124:127], v[76:79]\n"
  "buffer_load_dwordx4 a[72:75], v11, s[16:19], s71 offen offset:2048 sc1\n"
  "ds_read_b128 v[172:175], v15 offset:5120\n"
  "s_waitcnt lgkmcnt(5)\n"
  "v_mfma_f32_16x16x32_bf16 v[80:83], v[148:151], v[128:131], v[80:83]\n"
  "ds_read_b128 v[176:179], v15 offset:2048\n"
  "s_waitcnt lgkmcnt(5)\n"
  "v_mfma_f32_16x16x32_bf16 v[84:87], v[148:151], v[132:135], v[84:87]\n"
  "ds_read_b128 v[180:183], v15 offset:6144\n"
  "ds_read_b128 v[184:187], v15 offset:3072\n"
  "ds_read_b128 v[188:191], v15 offset:7168\n"
  "buffer_load_dwordx4 a[76:79], v11, s[16:19], s71 offen offset:3072 sc1\n"
  "buffer_load_dwordx4 a[136:139], v12, s[16:19], s71 offen offset:2048 sc1\n"
  "ds_write_b32 v218, v64 offset:0\n"
  "ds_write_b32 v218, v68 offset:512\n"
  "ds_write_b32 v218, v72 offset:1024\n"
  "ds_write_b32 v218, v76 offset:1536\n"
  "ds_write_b32 v218, v80 offset:2048\n"
  "ds_write_b32 v218, v84 offset:2560\n"
  "s_waitcnt vmcnt(11) lgkmcnt(0)\n"
  "v_mfma_f32_16x16x32_bf16 v[64:67], v[160:163], a[0:3], 0\n"
  "v_mfma_f32_16x16x32_bf16 v[64:67], v[164:167], a[4:7], v[64:67]\n"
  "ds_read_b128 v[192:195], v16 offset:0\n"
  "buffer_load_dwordx4 a[140:143], v12, s[16:19], s71 offen offset:3072 sc1\n"
  "v_mfma_f32_16x16x32_bf16 v[68:71], v[168:171], a[0:3], 0\n"
  "v_mfma_f32_16x16x32_bf16 v[68:71], v[172:175], a[4:7], v[68:71]\n"
  "ds_read_b128 v[196:199], v16 offset:2048\n"
  "v_mfma_f32_16x16x32_bf16 v[72:75], v[176:179], a[0:3], 0\n"
  "v_mfma_f32_16x16x32_bf16 v[72:75], v[180:183], a[4:7], v[72:75]\n"
  "ds_read_b128 v[200:203], v16 offset:1024\n"
  "v_mfma_f32_16x16x32_bf16 v[76:79], v[184:187], a[0:3], 0\n"
  "v_mfma_f32_16x16x32_bf16 v[76:79], v[188:191], a[4:7], v[76:79]\n"
  "ds_read_b128 v[204:207], v16 offset:3072\n"
  "buffer_load_dwordx4 a[200:203], v13, s[16:19], s71 offen offset:2048 sc1\n"
  "s_waitcnt vmcnt(11)\n"
  "v_mfma_f32_16x16x32_bf16 v[80:83], v[160:163], a[64:67], 0\n"
  "v_mfma_f32_16x16x32_bf16 v[80:83], v[164:167], a[68:71], v[80:83]\n"
  "v_mfma_f32_16x16x32_bf16 v[84:87], v[168:171], a[64:67], 0\n"
  "v_mfma_f32_16x16x32_bf16 v[84:87], v[172:175], a[68:71], v[84:87]\n"
  "v_mfma_f32_16x16x32_bf16 v[88:91], v[176:179], a[64:67], 0\n"
  "v_mfma_f32_16x16x32_bf16 v[88:91], v[180:183], a[68:71], v[88:91]\n"
  "buffer_load_dwordx4 a[204:207], v13, s[16:19], s71 offen offset:3072 sc1\n"
  "v_mfma_f32_16x16x32_bf16 v[92:95], v[184:187], a[64:67], 0\n"
  "v_mfma_f32_16x16x32_bf16 v[92:95], v[188:191], a[68:71], v[92:95]\n"
  "s_waitcnt vmcnt(10)\n"
  "v_mfma_f32_16x16x32_bf16 v[96:99], v[160:163], a[128:131], 0\n"
  "v_mfma_f32_16x16x32_bf16 v[96:99], v[164:167], a[132:135], v[96:99]\n"
  "v_mfma_f32_16x16x32_bf16 v[100:103], v[168:171], a[128:131], 0\n"
  "v_mfma_f32_16x16x32_bf16 v[100:103], v[172:175], a[132:135], v[100:103]\n"
  "s_addk_i32 s71, 4096\n"
  "buffer_load_dwordx4 a[16:19], v1, s[16:19], s71 offen sc1\n"
  "v_mfma_f32_16x16x32_bf16 v[104:107], v[176:179], a[128:131], 0\n"
  "v_mfma_f32_16x16x32_bf16 v[104:107], v[180:183], a[132:135], v[104:107]\n"
  "v_mfma_f32_16x16x32_bf16 v[108:111], v[184:187], a[128:131], 0\n"
  "v_mfma_f32_16x16x32_bf16 v[108:111], v[188:191], a[132:135], v[108:111]\n"
  "s_waitcnt vmcnt(9)\n"
  "v_mfma_f32_16x16x32_bf16 v[112:115], v[160:163], a[192:195], 0\n"
  "v_mfma_f32_16x16x32_bf16 v[112:115], v[164:167], a[196:199], v[112:115]\n"
  "buffer_load_dwordx4 a[20:23], v1, s[16:19], s71 offen offset:1024 sc1\n"
  "v_mfma_f32_16x16x32_bf16 v[116:119], v[168:171], a[192:195], 0\n"
  "v_mfma_f32_16x16x32_bf16 v[116:119], v[172:175], a[196:199], v[116:119]\n"
  "v_mfma_f32_16x16x32_bf16 v[120:123], v[176:179], a[192:195], 0\n"
  "v_mfma_f32_16x16x32_bf16 v[120:123], v[180:183], a[196:199], v[120:123]\n"
  "v_mfma_f32_16x16x32_bf16 v[124:127], v[184:187], a[192:195], 0\n"
  "v_mfma_f32_16x16x32_bf16 v[124:127], v[188:191], a[196:199], v[124:127]\n"
  "buffer_load_dwordx4 a[80:83], v11, s[16:19], s71 offen sc1\n"
  "s_barrier\n"
  "s_waitcnt lgkmcnt(0)\n"
  "v_mfma_f32_16x16x32_bf16 v[128:131], v[192:195], a[0:3], 0\n"
  "ds_read_b128 v[160:163], v15 offset:8192\n"
  "v_mfma_f32_16x16x32_bf16 v[128:131], v[196:199], a[4:7], v[128:131]\n"
  "ds_read_b128 v[164:167], v15 offset:12288\n"
  "v_mfma_f32_16x16x32_bf16 v[132:135], v[200:203], a[0:3], 0\n"
  "ds_read_b128 v[168:171], v15 offset:9216\n"
  "v_mfma_f32_16x16x32_bf16 v[132:135], v[204:207], a[4:7], v[132:135]\n"
  "ds_read_b128 v[172:175], v15 offset:13312\n"
  "v_mfma_f32_16x16x32_bf16 v[136:139], v[192:195], a[64:67], 0\n"
  "ds_read_b128 v[176:179], v15 offset:10240\n"
  "v_mfma_f32_16x16x32_bf16 v[136:139], v[196:199], a[68:71], v[136:139]\n"
  "ds_read_b128 v[180:183], v15 offset:14336\n"
  "buffer_load_dwordx4 a[84:87], v11, s[16:19], s71 offen offset:1024 sc1\n"
  "v_mfma_f32_16x16x32_bf16 v[140:143], v[200:203], a[64:67], 0\n"
  "ds_read_b128 v[184:187], v15 offset:11264\n"
  "v_mfma_f32_16x16x32_bf16 v[140:143], v[204:207], a[68:71], v[140:143]\n"
  "ds_read_b128 v[188:191], v15 offset:15360\n"
  "v_mfma_f32_16x16x32_bf16 v[144:147], v[192:195], a[128:131], 0\n"
  "s_add_u32 m0, s81, 0\n"
  "s_add_u32 s95, s13, s81\n"
  "s_add_u32 s95, s95, 118784\n"
  "buffer_load_dwordx4 v1, s[4:7], s95 offen offset:0 sc1 lds\n"
  "v_mfma_f32_16x16x32_bf16 v[144:147], v[196:199], a[132:135], v[144:147]\n"
  "v_mfma_f32_16x16x32_bf16 v[148:151], v[200:203], a[128:131], 0\n"
  "v_mfma_f32_16x16x32_bf16 v[148:151], v[204:207], a[132:135], v[148:151]\n"
  "buffer_load_dwordx4 a[144:147], v12, s[16:19], s71 offen sc1\n"
  "v_mfma_f32_16x16x32_bf16 v[152:155], v[192:195], a[192:195], 0\n"
  "v_mfma_f32_16x16x32_bf16 v[152:155], v[196:199], a[196:199], v[152:155]\n"
  "v_mfma_f32_16x16x32_bf16 v[156:159], v[200:203], a[192:195], 0\n"
  "v_mfma_f32_16x16x32_bf16 v[156:159], v[204:207], a[196:199], v[156:159]\n"
  "s_waitcnt vmcnt(12) lgkmcnt(0)\n"
  "v_mfma_f32_16x16x32_bf16 v[64:67], v[160:163], a[8:11], v[64:67]\n"
  "v_mfma_f32_16x16x32_bf16 v[64:67], v[164:167], a[12:15], v[64:67]\n"
  "ds_read_b128 v[192:195], v16 offset:4096\n"
  "buffer_load_dwordx4 a[148:151], v12, s[16:19], s71 offen offset:1024 sc1\n"
  "v_mfma_f32_16x16x32_bf16 v[68:71], v[168:171], a[8:11], v[68:71]\n"
  "v_mfma_f32_16x16x32_bf16 v[68:71], v[172:175], a[12:15], v[68:71]\n"
  "ds_read_b128 v[196:199], v16 offset:6144\n"
  "v_mfma_f32_16x16x32_bf16 v[72:75], v[176:179], a[8:11], v[72:75]\n"
  "buffer_load_dwordx4 v1, s[4:7], s95 offen offset:1024 sc1 lds\n"
  "v_mfma_f32_16x16x32_bf16 v[72:75], v[180:183], a[12:15], v[72:75]\n"
  "ds_read_b128 v[200:203], v16 offset:5120\n"
  "v_mfma_f32_16x16x32_bf16 v[76:79], v[184:187], a[8:11], v[76:79]\n"
  "v_mfma_f32_16x16x32_bf16 v[76:79], v[188:191], a[12:15], v[76:79]\n"
  "ds_read_b128 v[204:207], v16 offset:7168\n"
  "buffer_load_dwordx4 a[208:211], v13, s[16:19], s71 offen sc1\n"
  "s_waitcnt vmcnt(13)\n"
  "v_mfma_f32_16x16x32_bf16 v[80:83], v[160:163], a[72:75], v[80:83]\n"
  "ds_read_b128 v[224:227], v22 offset:0\n"
  "v_mfma_f32_16x16x32_bf16 v[80:83], v[164:167], a[76:79], v[80:83]\n"
  "ds_read_b128 v[228:231], v22 offset:512\n"
  "v_mfma_f32_16x16x32_bf16 v[84:87], v[168:171], a[72:75], v[84:87]\n"
  "ds_read_b128 v[232:235], v22 offset:1024\n"
  "v_mfma_f32_16x16x32_bf16 v[84:87], v[172:175], a[76:79], v[84:87]\n"
  "ds_read_b128 v[236:239], v22 offset:1536\n"
  "v_mfma_f32_16x16x32_bf16 v[88:91], v[176:179], a[72:75], v[88:91]\n"
  "ds_read_b128 v[56:59], v22 offset:2048\n"
  "v_mfma_f32_16x16x32_bf16 v[88:91], v[180:183], a[76:79], v[88:91]\n"
  "buffer_load_dwordx4 a[212:215], v13, s[16:19], s71 offen offset:1024 sc1\n"
  "ds_read_b128 v[60:63], v22 offset:2560\n"
  "v_mfma_f32_16x16x32_bf16 v[92:95], v[184:187], a[72:75], v[92:95]\n"
  "s_waitcnt lgkmcnt(5)\n"
  "v_add_f32 v224, v224, v226\n"
  "v_mfma_f32_16x16x32_bf16 v[92:95], v[188:191], a[76:79], v[92:95]\n"
  "v_add_f32 v225, v225, v227\n"
  "s_waitcnt vmcnt(12)\n"
  "v_mfma_f32_16x16x32_bf16 v[96:99], v[160:163], a[136:139], v[96:99]\n"
  "buffer_load_dwordx4 v1, s[4:7], s95 offen offset:2048 sc1 lds\n"
  "v_mfma_f32_16x16x32_bf16 v[96:99], v[164:167], a[140:143], v[96:99]\n"
  "s_waitcnt lgkmcnt(4)\n"
  "v_add_f32 v228, v228, v230\n"
  "v_mfma_f32_16x16x32_bf16 v[100:103], v[168:171], a[136:139], v[100:103]\n"
  "v_add_f32 v229, v229, v231\n"
  "v_mfma_f32_16x16x32_bf16 v[100:103], v[172:175], a[140:143], v[100:103]\n"
  "buffer_load_dwordx4 a[24:27], v1, s[16:19], s71 offen offset:2048 sc1\n"
  "s_waitcnt lgkmcnt(3)\n"
  "v_add_f32 v232, v232, v234\n"
  "v_mfma_f32_16x16x32_bf16 v[104:107], v[176:179], a[136:139], v[104:107]\n"
  "v_add_f32 v233, v233, v235\n"
  "s_waitcnt lgkmcnt(2)\n"
  "v_add_f32 v236, v236, v238\n"
  "v_mfma_f32_16x16x32_bf16 v[104:107], v[180:183], a[140:143], v[104:107]\n"
  "v_add_f32 v237, v237, v239\n"
  "s_waitcnt lgkmcnt(1)\n"
  "v_add_f32 v56, v56, v58\n"
  "v_mfma_f32_16x16x32_bf16 v[108:111], v[184:187], a[136:139], v[108:111]\n"
  "v_add_f32 v57, v57, v59\n"
  "s_waitcnt lgkmcnt(0)\n"
  "v_add_f32 v60, v60, v62\n"
  "v_mfma_f32_16x16x32_bf16 v[108:111], v[188:191], a[140:143], v[108:111]\n"
  "v_add_f32 v61, v61, v63\n"
  "v_add_f32 v224, v224, v225\n"
  "s_waitcnt vmcnt(12)\n"
  "v_mfma_f32_16x16x32_bf16 v[112:115], v[160:163], a[200:203], v[112:115]\n"
  "v_add_f32 v228, v228, v229\n"
  "v_add_f32 v232, v232, v233\n"
  "v_mfma_f32_16x16x32_bf16 v[112:115], v[164:167], a[204:207], v[112:115]\n"
  "buffer_load_dwordx4 a[28:31], v1, s[16:19], s71 offen offset:3072 sc1\n"
  "v_add_f32 v236, v236, v237\n"
  "v_mfma_f32_16x16x32_bf16 v[116:119], v[168:171], a[200:203], v[116:119]\n"
  "v_add_f32 v56, v56, v57\n"
  "v_add_f32 v60, v60, v61\n"
  "v_mfma_f32_16x16x32_bf16 v[116:119], v[172:175], a[204:207], v[116:119]\n"
  "v_cvt_pk_bf16_f32 v225, 0, v224\n"
  "v_cvt_pk_bf16_f32 v229, 0, v228\n"
  "v_mfma_f32_16x16x32_bf16 v[120:123], v[176:179], a[200:203], v[120:123]\n"
  "buffer_load_dwordx4 v1, s[4:7], s95 offen offset:3072 sc1 lds\n"
  "v_mfma_f32_16x16x32_bf16 v[120:123], v[180:183], a[204:207], v[120:123]\n"
  "v_cvt_pk_bf16_f32 v233, 0, v232\n"
  "v_cvt_pk_bf16_f32 v237, 0, v236\n"
  "v_mfma_f32_16x16x32_bf16 v[124:127], v[184:187], a[200:203], v[124:127]\n"
  "v_cvt_pk_bf16_f32 v57, 0, v56\n"
  "v_cvt_pk_bf16_f32 v61, 0, v60\n"
  "v_mfma_f32_16x16x32_bf16 v[124:127], v[188:191], a[204:207], v[124:127]\n"
  "buffer_load_dwordx4 a[88:91], v11, s[16:19], s71 offen offset:2048 sc1\n"
  "v_sub_f32 v224, v224, v225\n"
  "v_mfma_f32_16x16x32_bf16 v[128:131], v[192:195], a[8:11], v[128:131]\n"
  "ds_read_b128 v[160:163], v15 offset:16384\n"
  "v_sub_f32 v228, v228, v229\n"
  "v_mfma_f32_16x16x32_bf16 v[128:131], v[196:199], a[12:15], v[128:131]\n"
  "ds_read_b128 v[164:167], v15 offset:20480\n"
  "v_sub_f32 v232, v232, v233\n"
  "v_mfma_f32_16x16x32_bf16 v[132:135], v[200:203], a[8:11], v[132:135]\n"
  "ds_read_b128 v[168:171], v15 offset:17408\n"
  "v_sub_f32 v236, v236, v237\n"
  "v_mfma_f32_16x16x32_bf16 v[132:135], v[204:207], a[12:15], v[132:135]\n"
  "ds_read_b128 v[172:175], v15 offset:21504\n"
  "v_sub_f32 v56, v56, v57\n"
  "v_mfma_f32_16x16x32_bf16 v[136:139], v[192:195], a[72:75], v[136:139]\n"
  "ds_read_b128 v[176:179], v15 offset:18432\n"
  "v_sub_f32 v60, v60, v61\n"
  "v_mfma_f32_16x16x32_bf16 v[136:139], v[196:199], a[76:79], v[136:139]\n"
  "ds_read_b128 v[180:183], v15 offset:22528\n"
  "buffer_load_dwordx4 a[92:95], v11, s[16:19], s71 offen offset:3072 sc1\n"
  "v_mfma_f32_16x16x32_bf16 v[140:143], v[200:203], a[72:75], v[140:143]\n"
  "ds_read_b128 v[184:187], v15 offset:19456\n"
  "v_cvt_pk_bf16_f32 v24, v225, v224\n"
  "v_mfma_f32_16x16x32_bf16 v[140:143], v[204:207], a[76:79], v[140:143]\n"
  "ds_read_b128 v[188:191], v15 offset:23552\n"
  "v_cvt_pk_bf16_f32 v25, v225, v224\n"
  "v_mfma_f32_16x16x32_bf16 v[144:147], v[192:195], a[136:139], v[144:147]\n"
  "s_add_u32 m0, s81, 16384\n"
  "s_add_u32 s95, s13, s81\n"
  "s_add_u32 s95, s95, 135168\n"
  "buffer_load_dwordx4 v1, s[4:7], s95 offen offset:0 sc1 lds\n"
  "v_mfma_f32_16x16x32_bf16 v[144:147], v[196:199], a[140:143], v[144:147]\n"
  "v_cvt_pk_bf16_f32 v26, v229, v228\n"
  "v_cvt_pk_bf16_f32 v27, v229, v228\n"
  "v_mfma_f32_16x16x32_bf16 v[148:151], v[200:203], a[136:139], v[148:151]\n"
  "v_cvt_pk_bf16_f32 v28, v233, v232\n"
  "v_cvt_pk_bf16_f32 v29, v233, v232\n"
  "v_mfma_f32_16x16x32_bf16 v[148:151], v[204:207], a[140:143], v[148:151]\n"
  "buffer_load_dwordx4 a[152:155], v12, s[16:19], s71 offen offset:2048 sc1\n"
  "v_cvt_pk_bf16_f32 v30, v237, v236\n"
  "v_mfma_f32_16x16x32_bf16 v[152:155], v[192:195], a[200:203], v[152:155]\n"
  "v_cvt_pk_bf16_f32 v31, v237, v236\n"
  "v_cvt_pk_bf16_f32 v32, v57, v56\n"
  "v_mfma_f32_16x16x32_bf16 v[152:155], v[196:199], a[204:207], v[152:155]\n"
  "v_cvt_pk_bf16_f32 v33, v57, v56\n"
  "v_cvt_pk_bf16_f32 v34, v61, v60\n"
  "v_mfma_f32_16x16x32_bf16 v[156:159], v[200:203], a[200:203], v[156:159]\n"
  "v_cvt_pk_bf16_f32 v35, v61, v60\n"
  "v_mfma_f32_16x16x32_bf16 v[156:159], v[204:207], a[204:207], v[156:159]\n"
  "s_waitcnt vmcnt(16) lgkmcnt(0)\n"
  "v_mfma_f32_16x16x32_bf16 v[64:67], v[160:163], a[16:19], v[64:67]\n"
  "v_mfma_f32_16x16x32_bf16 v[64:67], v[164:167], a[20:23], v[64:67]\n"
  "ds_read_b128 v[192:195], v16 offset:8192\n"
  "buffer_load_dwordx4 a[156:159], v12, s[16:19], s71 offen offset:3072 sc1\n"
  "v_mfma_f32_16x16x32_bf16 v[68:71], v[168:171], a[16:19], v[68:71]\n"
  "v_mfma_f32_16x16x32_bf16 v[68:71], v[172:175], a[20:23], v[68:71]\n"
  "ds_read_b128 v[196:199], v16 offset:10240\n"
  "v_mfma_f32_16x16x32_bf16 v[72:75], v[176:179], a[16:19], v[72:75]\n"
  "buffer_load_dwordx4 v1, s[4:7], s95 offen offset:1024 sc1 lds\n"
  "v_mfma_f32_16x16x32_bf16 v[72:75], v[180:183], a[20:23], v[72:75]\n"
  "ds_read_b128 v[200:203], v16 offset:9216\n"
  "v_mfma_f32_16x16x32_bf16 v[76:79], v[184:187], a[16:19], v[76:79]\n"
  "v_mfma_f32_16x16x32_bf16 v[76:79], v[188:191], a[20:23], v[76:79]\n"
  "ds_read_b128 v[204:207], v16 offset:11264\n"
  "buffer_load_dwordx4 a[216:219], v13, s[16:19], s71 offen offset:2048 sc1\n"
  "s_waitcnt vmcnt(17)\n"
  "v_mfma_f32_16x16x32_bf16 v[80:83], v[160:163], a[80:83], v[80:83]\n"
  "v_mfma_f32_16x16x32_bf16 v[80:83], v[164:167], a[84:87], v[80:83]\n"
  "v_mfma_f32_16x16x32_bf16 v[84:87], v[168:171], a[80:83], v[84:87]\n"
  "buffer_load_dwordx2 v[224:225], v8, s[16:19], s75 offen sc1\n"
  "v_mfma_f32_16x16x32_bf16 v[84:87], v[172:175], a[84:87], v[84:87]\n"
  "v_mfma_f32_16x16x32_bf16 v[88:91], v[176:179], a[80:83], v[88:91]\n"
  "v_mfma_f32_16x16x32_bf16 v[88:91], v[180:183], a[84:87], v[88:91]\n"
  "buffer_load_dwordx4 a[220:223], v13, s[16:19], s71 offen offset:3072 sc1\n"
  "v_mfma_f32_16x16x32_bf16 v[92:95], v[184:187], a[80:83], v[92:95]\n"
  "v_mfma_f32_16x16x32_bf16 v[92:95], v[188:191], a[84:87], v[92:95]\n"
  "s_waitcnt vmcnt(16)\n"
  "v_mfma_f32_16x16x32_bf16 v[96:99], v[160:163], a[144:147], v[96:99]\n"
  "buffer_load_dwordx4 v1, s[4:7], s95 offen offset:2048 sc1 lds\n"
  "v_mfma_f32_16x16x32_bf16 v[96:99], v[164:167], a[148:151], v[96:99]\n"
  "v_mfma_f32_16x16x32_bf16 v[100:103], v[168:171], a[144:147], v[100:103]\n"
  "v_mfma_f32_16x16x32_bf16 v[100:103], v[172:175], a[148:151], v[100:103]\n"
  "s_addk_i32 s71, 12288\n"
  "buffer_load_dwordx4 a[32:35], v1, s[16:19], s71 offen sc1\n"
  "v_mfma_f32_16x16x32_bf16 v[104:107], v[176:179], a[144:147], v[104:107]\n"
  "v_mfma_f32_16x16x32_bf16 v[104:107], v[180:183], a[148:151], v[104:107]\n"
  "v_mfma_f32_16x16x32_bf16 v[108:111], v[184:187], a[144:147], v[108:111]\n"
  "buffer_load_dwordx2 v[226:227], v8, s[16:19], s75 offen offset:512 sc1\n"
  "v_mfma_f32_16x16x32_bf16 v[108:111], v[188:191], a[148:151], v[108:111]\n"
  "s_waitcnt vmcnt(16)\n"
  "v_mfma_f32_16x16x32_bf16 v[112:115], v[160:163], a[208:211], v[112:115]\n"
  "v_mfma_f32_16x16x32_bf16 v[112:115], v[164:167], a[212:215], v[112:115]\n"
  "buffer_load_dwordx4 a[36:39], v1, s[16:19], s71 offen offset:1024 sc1\n"
  "v_mfma_f32_16x16x32_bf16 v[116:119], v[168:171], a[208:211], v[116:119]\n"
  "v_mfma_f32_16x16x32_bf16 v[116:119], v[172:175], a[212:215], v[116:119]\n"
  "v_mfma_f32_16x16x32_bf16 v[120:123], v[176:179], a[208:211], v[120:123]\n"
  "buffer_load_dwordx4 v1, s[4:7], s95 offen offset:3072 sc1 lds\n"
  "v_mfma_f32_16x16x32_bf16 v[120:123], v[180:183], a[212:215], v[120:123]\n"
  "v_mfma_f32_16x16x32_bf16 v[124:127], v[184:187], a[208:211], v[124:127]\n"
  "v_mfma_f32_16x16x32_bf16 v[124:127], v[188:191], a[212:215], v[124:127]\n"
  "buffer_load_dwordx4 a[96:99], v11, s[16:19], s71 offen sc1\n"
  "s_waitcnt lgkmcnt(0)\n"
  "v_mfma_f32_16x16x32_bf16 v[128:131], v[192:195], a[16:19], v[128:131]\n"
  "ds_read_b128 v[160:163], v15 offset:24576\n"
  "v_mfma_f32_16x16x32_bf16 v[128:131], v[196:199], a[20:23], v[128:131]\n"
  "ds_read_b128 v[164:167], v15 offset:28672\n"
  "v_mfma_f32_16x16x32_bf16 v[132:135], v[200:203], a[16:19], v[132:135]\n"
  "ds_read_b128 v[168:171], v15 offset:25600\n"
  "buffer_load_dwordx2 v[228:229], v8, s[16:19], s75 offen offset:1024 sc1\n"
  "v_mfma_f32_16x16x32_bf16 v[132:135], v[204:207], a[20:23], v[132:135]\n"
  "ds_read_b128 v[172:175], v15 offset:29696\n"
  "v_mfma_f32_16x16x32_bf16 v[136:139], v[192:195], a[80:83], v[136:139]\n"
  "ds_read_b128 v[176:179], v15 offset:26624\n"
  "v_mfma_f32_16x16x32_bf16 v[136:139], v[196:199], a[84:87], v[136:139]\n"
  "ds_read_b128 v[180:183], v15 offset:30720\n"
  "buffer_load_dwordx4 a[100:103], v11, s[16:19], s71 offen offset:1024 sc1\n"
  "v_mfma_f32_16x16x32_bf16 v[140:143], v[200:203], a[80:83], v[140:143]\n"
  "ds_read_b128 v[184:187], v15 offset:27648\n"
  "v_mfma_f32_16x16x32_bf16 v[140:143], v[204:207], a[84:87], v[140:143]\n"
  "ds_read_b128 v[188:191], v15 offset:31744\n"
  "v_mfma_f32_16x16x32_bf16 v[144:147], v[192:195], a[144:147], v[144:147]\n"
  "s_add_u32 m0, s81, 32768\n"
  "s_add_u32 s95, s13, s81\n"
  "s_add_u32 s95, s95, 151552\n"
  "buffer_load_dwordx4 v1, s[4:7], s95 offen offset:0 sc1 lds\n"
  "v_mfma_f32_16x16x32_bf16 v[144:147], v[196:199], a[148:151], v[144:147]\n"
  "v_mfma_f32_16x16x32_bf16 v[148:151], v[200:203], a[144:147], v[148:151]\n"
  "v_mfma_f32_16x16x32_bf16 v[148:151], v[204:207], a[148:151], v[148:151]\n"
  "buffer_load_dwordx4 a[160:163], v12, s[16:19], s71 offen sc1\n"
  "v_mfma_f32_16x16x32_bf16 v[152:155], v[192:195], a[208:211], v[152:155]\n"
  "v_mfma_f32_16x16x32_bf16 v[152:155], v[196:199], a[212:215], v[152:155]\n"
  "v_mfma_f32_16x16x32_bf16 v[156:159], v[200:203], a[208:211], v[156:159]\n"
  "buffer_load_dwordx2 v[230:231], v8, s[16:19], s75 offen offset:1536 sc1\n"
  "v_mfma_f32_16x16x32_bf16 v[156:159], v[204:207], a[212:215], v[156:159]\n"
  "s_waitcnt vmcnt(21) lgkmcnt(0)\n"
  "v_mfma_f32_16x16x32_bf16 v[64:67], v[160:163], a[24:27], v[64:67]\n"
  "v_mfma_f32_16x16x32_bf16 v[64:67], v[164:167], a[28:31], v[64:67]\n"
  "ds_read_b128 v[192:195], v16 offset:12288\n"
  "buffer_load_dwordx4 a[164:167], v12, s[16:19], s71 offen offset:1024 sc1\n"
  "v_mfma_f32_16x16x32_bf16 v[68:71], v[168:171], a[24:27], v[68:71]\n"
  "v_mfma_f32_16x16x32_bf16 v[68:71], v[172:175], a[28:31], v[68:71]\n"
  "ds_read_b128 v[196:199], v16 offset:14336\n"
  "v_mfma_f32_16x16x32_bf16 v[72:75], v[176:179], a[24:27], v[72:75]\n"
  "buffer_load_dwordx4 v1, s[4:7], s95 offen offset:1024 sc1 lds\n"
  "v_mfma_f32_16x16x32_bf16 v[72:75], v[180:183], a[28:31], v[72:75]\n"
  "ds_read_b128 v[200:203], v16 offset:13312\n"
  "v_mfma_f32_16x16x32_bf16 v[76:79], v[184:187], a[24:27], v[76:79]\n"
  "v_mfma_f32_16x16x32_bf16 v[76:79], v[188:191], a[28:31], v[76:79]\n"
  "ds_read_b128 v[204:207], v16 offset:15360\n"
  "buffer_load_dwordx4 a[224:227], v13, s[16:19], s71 offen sc1\n"
  "s_waitcnt vmcnt(21)\n"
  "v_mfma_f32_16x16x32_bf16 v[80:83], v[160:163], a[88:91], v[80:83]\n"
  "v_mfma_f32_16x16x32_bf16 v[80:83], v[164:167], a[92:95], v[80:83]\n"
  "v_mfma_f32_16x16x32_bf16 v[84:87], v[168:171], a[88:91], v[84:87]\n"
  "buffer_load_dwordx2 v[232:233], v8, s[16:19], s75 offen offset:2048 sc1\n"
  "v_mfma_f32_16x16x32_bf16 v[84:87], v[172:175], a[92:95], v[84:87]\n"
  "v_mfma_f32_16x16x32_bf16 v[88:91], v[176:179], a[88:91], v[88:91]\n"
  "v_mfma_f32_16x16x32_bf16 v[88:91], v[180:183], a[92:95], v[88:91]\n"
  "buffer_load_dwordx4 a[228:231], v13, s[16:19], s71 offen offset:1024 sc1\n"
  "v_mfma_f32_16x16x32_bf16 v[92:95], v[184:187], a[88:91], v[92:95]\n"
  "v_mfma_f32_16x16x32_bf16 v[92:95], v[188:191], a[92:95], v[92:95]\n"
  "s_waitcnt vmcnt(20)\n"
  "v_mfma_f32_16x16x32_bf16 v[96:99], v[160:163], a[152:155], v[96:99]\n"
  "buffer_load_dwordx4 v1, s[4:7], s95 offen offset:2048 sc1 lds\n"
  "v_mfma_f32_16x16x32_bf16 v[96:99], v[164:167], a[156:159], v[96:99]\n"
  "v_mfma_f32_16x16x32_bf16 v[100:103], v[168:171], a[152:155], v[100:103]\n"
  "v_mfma_f32_16x16x32_bf16 v[100:103], v[172:175], a[156:159], v[100:103]\n"
  "buffer_load_dwordx4 a[40:43], v1, s[16:19], s71 offen offset:2048 sc1\n"
  "v_mfma_f32_16x16x32_bf16 v[104:107], v[176:179], a[152:155], v[104:107]\n"
  "v_mfma_f32_16x16x32_bf16 v[104:107], v[180:183], a[156:159], v[104:107]\n"
  "v_mfma_f32_16x16x32_bf16 v[108:111], v[184:187], a[152:155], v[108:111]\n"
  "buffer_load_dwordx2 v[234:235], v8, s[16:19], s75 offen offset:2560 sc1\n"
  "v_mfma_f32_16x16x32_bf16 v[108:111], v[188:191], a[156:159], v[108:111]\n"
  "s_waitcnt vmcnt(19)\n"
  "v_mfma_f32_16x16x32_bf16 v[112:115], v[160:163], a[216:219], v[112:115]\n"
  "v_mfma_f32_16x16x32_bf16 v[112:115], v[164:167], a[220:223], v[112:115]\n"
  "buffer_load_dwordx4 a[44:47], v1, s[16:19], s71 offen offset:3072 sc1\n"
  "v_mfma_f32_16x16x32_bf16 v[116:119], v[168:171], a[216:219], v[116:119]\n"
  "v_mfma_f32_16x16x32_bf16 v[116:119], v[172:175], a[220:223], v[116:119]\n"
  "v_mfma_f32_16x16x32_bf16 v[120:123], v[176:179], a[216:219], v[120:123]\n"
  "buffer_load_dwordx4 v1, s[4:7], s95 offen offset:3072 sc1 lds\n"
  "v_mfma_f32_16x16x32_bf16 v[120:123], v[180:183], a[220:223], v[120:123]\n"
  "v_mfma_f32_16x16x32_bf16 v[124:127], v[184:187], a[216:219], v[124:127]\n"
  "v_mfma_f32_16x16x32_bf16 v[124:127], v[188:191], a[220:223], v[124:127]\n"
  "buffer_load_dwordx4 a[104:107], v11, s[16:19], s71 offen offset:2048 sc1\n"
  "s_waitcnt lgkmcnt(0)\n"
  "v_mfma_f32_16x16x32_bf16 v[128:131], v[192:195], a[24:27], v[128:131]\n"
  "ds_read_b128 v[160:163], v15 offset:32768\n"
  "v_mfma_f32_16x16x32_bf16 v[128:131], v[196:199], a[28:31], v[128:131]\n"
  "ds_read_b128 v[164:167], v15 offset:36864\n"
  "v_mfma_f32_16x16x32_bf16 v[132:135], v[200:203], a[24:27], v[132:135]\n"
  "ds_read_b128 v[168:171], v15 offset:33792\n"
  "buffer_load_dwordx2 v[236:237], v8, s[16:19], s75 offen offset:3072 sc1\n"
  "v_mfma_f32_16x16x32_bf16 v[132:135], v[204:207], a[28:31], v[132:135]\n"
  "ds_read_b128 v[172:175], v15 offset:37888\n"
  "v_mfma_f32_16x16x32_bf16 v[136:139], v[192:195], a[88:91], v[136:139]\n"
  "ds_read_b128 v[176:179], v15 offset:34816\n"
  "v_mfma_f32_16x16x32_bf16 v[136:139], v[196:199], a[92:95], v[136:139]\n"
  "ds_read_b128 v[180:183], v15 offset:38912\n"
  "buffer_load_dwordx4 a[108:111], v11, s[16:19], s71 offen offset:3072 sc1\n"
  "v_mfma_f32_16x16x32_bf16 v[140:143], v[200:203], a[88:91], v[140:143]\n"
  "ds_read_b128 v[184:187], v15 offset:35840\n"
  "v_mfma_f32_16x16x32_bf16 v[140:143], v[204:207], a[92:95], v[140:143]\n"
  "ds_read_b128 v[188:191], v15 offset:39936\n"
  "v_mfma_f32_16x16x32_bf16 v[144:147], v[192:195], a[152:155], v[144:147]\n"
  "s_lshl_b32 s93, s80, 11\n"
  "s_add_u32 m0, s93, 49152\n"
  "s_add_u32 s95, s93, s13\n"
  "s_add_u32 s95, s95, 167936\n"
  "buffer_load_dwordx4 v1, s[4:7], s95 offen offset:0 sc1 lds\n"
  "v_mfma_f32_16x16x32_bf16 v[144:147], v[196:199], a[156:159], v[144:147]\n"
  "v_mfma_f32_16x16x32_bf16 v[148:151], v[200:203], a[152:155], v[148:151]\n"
  "v_mfma_f32_16x16x32_bf16 v[148:151], v[204:207], a[156:159], v[148:151]\n"
  "buffer_load_dwordx4 a[168:171], v12, s[16:19], s71 offen offset:2048 sc1\n"
  "v_mfma_f32_16x16x32_bf16 v[152:155], v[192:195], a[216:219], v[152:155]\n"
  "v_mfma_f32_16x16x32_bf16 v[152:155], v[196:199], a[220:223], v[152:155]\n"
  "v_mfma_f32_16x16x32_bf16 v[156:159], v[200:203], a[216:219], v[156:159]\n"
  "buffer_load_dwordx2 v[238:239], v8, s[16:19], s75 offen offset:3584 sc1\n"
  "v_mfma_f32_16x16x32_bf16 v[156:159], v[204:207], a[220:223], v[156:159]\n"
  "s_waitcnt vmcnt(23) lgkmcnt(0)\n"
  "v_mfma_f32_16x16x32_bf16 v[64:67], v[160:163], a[32:35], v[64:67]\n"
  "v_mfma_f32_16x16x32_bf16 v[64:67], v[164:167], a[36:39], v[64:67]\n"
  "ds_read_b128 v[192:195], v16 offset:16384\n"
  "buffer_load_dwordx4 a[172:175], v12, s[16:19], s71 offen offset:3072 sc1\n"
  "v_mfma_f32_16x16x32_bf16 v[68:71], v[168:171], a[32:35], v[68:71]\n"
  "v_mfma_f32_16x16x32_bf16 v[68:71], v[172:175], a[36:39], v[68:71]\n"
  "ds_read_b128 v[196:199], v16 offset:18432\n"
  "v_mfma_f32_16x16x32_bf16 v[72:75], v[176:179], a[32:35], v[72:75]\n"
  "buffer_load_dwordx4 v1, s[4:7], s95 offen offset:1024 sc1 lds\n"
  "v_mfma_f32_16x16x32_bf16 v[72:75], v[180:183], a[36:39], v[72:75]\n"
  "ds_read_b128 v[200:203], v16 offset:17408\n"
  "v_mfma_f32_16x16x32_bf16 v[76:79], v[184:187], a[32:35], v[76:79]\n"
  "v_mfma_f32_16x16x32_bf16 v[76:79], v[188:191], a[36:39], v[76:79]\n"
  "ds_read_b128 v[204:207], v16 offset:19456\n"
  "buffer_load_dwordx4 a[232:235], v13, s[16:19], s71 offen offset:2048 sc1\n"
  "s_waitcnt vmcnt(22)\n"
  "v_mfma_f32_16x16x32_bf16 v[80:83], v[160:163], a[96:99], v[80:83]\n"
  "v_mfma_f32_16x16x32_bf16 v[80:83], v[164:167], a[100:103], v[80:83]\n"
  "v_mfma_f32_16x16x32_bf16 v[84:87], v[168:171], a[96:99], v[84:87]\n"
  "v_mfma_f32_16x16x32_bf16 v[84:87], v[172:175], a[100:103], v[84:87]\n"
  "v_mfma_f32_16x16x32_bf16 v[88:91], v[176:179], a[96:99], v[88:91]\n"
  "v_mfma_f32_16x16x32_bf16 v[88:91], v[180:183], a[100:103], v[88:91]\n"
  "buffer_load_dwordx4 a[236:239], v13, s[16:19], s71 offen offset:3072 sc1\n"
  "v_mfma_f32_16x16x32_bf16 v[92:95], v[184:187], a[96:99], v[92:95]\n"
  "v_mfma_f32_16x16x32_bf16 v[92:95], v[188:191], a[100:103], v[92:95]\n"
  "s_waitcnt vmcnt(19)\n"
  "v_mfma_f32_16x16x32_bf16 v[96:99], v[160:163], a[160:163], v[96:99]\n"
  "v_mfma_f32_16x16x32_bf16 v[96:99], v[164:167], a[164:167], v[96:99]\n"
  "v_mfma_f32_16x16x32_bf16 v[100:103], v[168:171], a[160:163], v[100:103]\n"
  "v_mfma_f32_16x16x32_bf16 v[100:103], v[172:175], a[164:167], v[100:103]\n"
  "s_addk_i32 s71, 4096\n"
  "buffer_load_dwordx4 a[48:51], v1, s[16:19], s71 offen sc1\n"
  "v_mfma_f32_16x16x32_bf16 v[104:107], v[176:179], a[160:163], v[104:107]\n"
  "v_mfma_f32_16x16x32_bf16 v[104:107], v[180:183], a[164:167], v[104:107]\n"
  "v_mfma_f32_16x16x32_bf16 v[108:111], v[184:187], a[160:163], v[108:111]\n"
  "v_mfma_f32_16x16x32_bf16 v[108:111], v[188:191], a[164:167], v[108:111]\n"
  "s_waitcnt vmcnt(16)\n"
  "v_mfma_f32_16x16x32_bf16 v[112:115], v[160:163], a[224:227], v[112:115]\n"
  "v_mfma_f32_16x16x32_bf16 v[112:115], v[164:167], a[228:231], v[112:115]\n"
  "buffer_load_dwordx4 a[52:55], v1, s[16:19], s71 offen offset:1024 sc1\n"
  "v_mfma_f32_16x16x32_bf16 v[116:119], v[168:171], a[224:227], v[116:119]\n"
  "v_mfma_f32_16x16x32_bf16 v[116:119], v[172:175], a[228:231], v[116:119]\n"
  "v_mfma_f32_16x16x32_bf16 v[120:123], v[176:179], a[224:227], v[120:123]\n"
  "v_mfma_f32_16x16x32_bf16 v[120:123], v[180:183], a[228:231], v[120:123]\n"
  "v_mfma_f32_16x16x32_bf16 v[124:127], v[184:187], a[224:227], v[124:127]\n"
  "v_mfma_f32_16x16x32_bf16 v[124:127], v[188:191], a[228:231], v[124:127]\n"
  "buffer_load_dwordx4 a[112:115], v11, s[16:19], s71 offen sc1\n"
  "s_waitcnt lgkmcnt(0)\n"
  "v_mfma_f32_16x16x32_bf16 v[128:131], v[192:195], a[32:35], v[128:131]\n"
  "ds_read_b128 v[160:163], v15 offset:40960\n"
  "v_mfma_f32_16x16x32_bf16 v[128:131], v[196:199], a[36:39], v[128:131]\n"
  "ds_read_b128 v[164:167], v15 offset:45056\n"
  "v_mfma_f32_16x16x32_bf16 v[132:135], v[200:203], a[32:35], v[132:135]\n"
  "ds_read_b128 v[168:171], v15 offset:41984\n"
  "v_mfma_f32_16x16x32_bf16 v[132:135], v[204:207], a[36:39], v[132:135]\n"
  "ds_read_b128 v[172:175], v15 offset:46080\n"
  "v_mfma_f32_16x16x32_bf16 v[136:139], v[192:195], a[96:99], v[136:139]\n"
  "ds_read_b128 v[176:179], v15 offset:43008\n"
  "v_mfma_f32_16x16x32_bf16 v[136:139], v[196:199], a[100:103], v[136:139]\n"
  "ds_read_b128 v[180:183], v15 offset:47104\n"
  "buffer_load_dwordx4 a[116:119], v11, s[16:19], s71 offen offset:1024 sc1\n"
  "v_mfma_f32_16x16x32_bf16 v[140:143], v[200:203], a[96:99], v[140:143]\n"
  "ds_read_b128 v[184:187], v15 offset:44032\n"
  "v_mfma_f32_16x16x32_bf16 v[140:143], v[204:207], a[100:103], v[140:143]\n"
  "ds_read_b128 v[188:191], v15 offset:48128\n"
  "v_mfma_f32_16x16x32_bf16 v[144:147], v[192:195], a[160:163], v[144:147]\n"
  "v_mfma_f32_16x16x32_bf16 v[144:147], v[196:199], a[164:167], v[144:147]\n"
  "v_mfma_f32_16x16x32_bf16 v[148:151], v[200:203], a[160:163], v[148:151]\n"
  "v_mfma_f32_16x16x32_bf16 v[148:151], v[204:207], a[164:167], v[148:151]\n"
  "buffer_load_dwordx4 a[176:179], v12, s[16:19], s71 offen sc1\n"
  "v_mfma_f32_16x16x32_bf16 v[152:155], v[192:195], a[224:227], v[152:155]\n"
  "v_mfma_f32_16x16x32_bf16 v[152:155], v[196:199], a[228:231], v[152:155]\n"
  "v_mfma_f32_16x16x32_bf16 v[156:159], v[200:203], a[224:227], v[156:159]\n"
  "v_mfma_f32_16x16x32_bf16 v[156:159], v[204:207], a[228:231], v[156:159]\n"
  "s_waitcnt vmcnt(16) lgkmcnt(0)\n"
  "v_mfma_f32_16x16x32_bf16 v[64:67], v[160:163], a[40:43], v[64:67]\n"
  "v_add_f32 v224, v224, v226\n"
  "v_add_f32 v225, v225, v227\n"
  "v_mfma_f32_16x16x32_bf16 v[64:67], v[164:167], a[44:47], v[64:67]\n"
  "ds_read_b128 v[192:195], v16 offset:20480\n"
  "buffer_load_dwordx4 a[180:183], v12, s[16:19], s71 offen offset:1024 sc1\n"
  "v_mfma_f32_16x16x32_bf16 v[68:71], v[168:171], a[40:43], v[68:71]\n"
  "v_add_f32 v228, v228, v230\n"
  "v_add_f32 v229, v229, v231\n"
  "v_mfma_f32_16x16x32_bf16 v[68:71], v[172:175], a[44:47], v[68:71]\n"
  "ds_read_b128 v[196:199], v16 offset:22528\n"
  "v_add_f32 v232, v232, v234\n"
  "v_mfma_f32_16x16x32_bf16 v[72:75], v[176:179], a[40:43], v[72:75]\n"
  "v_add_f32 v233, v233, v235\n"
  "s_waitcnt vmcnt(10)\n"
  "v_add_f32 v236, v236, v238\n"
  "v_mfma_f32_16x16x32_bf16 v[72:75], v[180:183], a[44:47], v[72:75]\n"
  "ds_read_b128 v[200:203], v16 offset:21504\n"
  "v_add_f32 v237, v237, v239\n"
  "v_mfma_f32_16x16x32_bf16 v[76:79], v[184:187], a[40:43], v[76:79]\n"
  "v_add_f32 v224, v224, v228\n"
  "v_add_f32 v225, v225, v229\n"
  "v_mfma_f32_16x16x32_bf16 v[76:79], v[188:191], a[44:47], v[76:79]\n"
  "ds_read_b128 v[204:207], v16 offset:23552\n"
  "buffer_load_dwordx4 a[240:243], v13, s[16:19], s71 offen sc1\n"
  "v_mfma_f32_16x16x32_bf16 v[80:83], v[160:163], a[104:107], v[80:83]\n"
  "v_add_f32 v232, v232, v236\n"
  "v_add_f32 v233, v233, v237\n"
  "v_mfma_f32_16x16x32_bf16 v[80:83], v[164:167], a[108:111], v[80:83]\n"
  "v_add_f32 v224, v224, v232\n"
  "v_add_f32 v225, v225, v233\n"
  "v_mfma_f32_16x16x32_bf16 v[84:87], v[168:171], a[104:107], v[84:87]\n"
  "v_mov_b32 v226, 0x3727c5ac\n"
  "v_mul_f32 v224, 0xbb000000, v224\n"
  "v_mfma_f32_16x16x32_bf16 v[84:87], v[172:175], a[108:111], v[84:87]\n"
  "v_mul_f32 v225, 0x3b000000, v225\n"
  "v_fma_f32 v225, -v224, v224, v225\n"
  "v_mfma_f32_16x16x32_bf16 v[88:91], v[176:179], a[104:107], v[88:91]\n"
  "v_max_f32 v225, 0, v225\n"
  "v_add_f32 v226, v226, v225\n"
  "v_mfma_f32_16x16x32_bf16 v[88:91], v[180:183], a[108:111], v[88:91]\n"
  "buffer_load_dwordx4 a[244:247], v13, s[16:19], s71 offen offset:1024 sc1\n"
  "v_mfma_f32_16x16x32_bf16 v[92:95], v[184:187], a[104:107], v[92:95]\n"
  "v_sqrt_f32 v225, v226\n"
  "v_mfma_f32_16x16x32_bf16 v[92:95], v[188:191], a[108:111], v[92:95]\n"
  "s_waitcnt vmcnt(11)\n"
  "v_cvt_pk_bf16_f32 v40, v224, v224\n"
  "v_cvt_pk_bf16_f32 v44, v225, v225\n"
  "v_mfma_f32_16x16x32_bf16 v[96:99], v[160:163], a[168:171], v[96:99]\n"
  "v_and_b32 v226, s91, v40\n"
  "v_and_b32 v227, s91, v44\n"
  "v_mfma_f32_16x16x32_bf16 v[96:99], v[164:167], a[172:175], v[96:99]\n"
  "v_sub_f32 v224, v224, v226\n"
  "v_sub_f32 v225, v225, v227\n"
  "v_mfma_f32_16x16x32_bf16 v[100:103], v[168:171], a[168:171], v[100:103]\n"
  "v_cvt_pk_bf16_f32 v41, v224, v224\n"
  "v_cvt_pk_bf16_f32 v45, v225, v225\n"
  "v_mfma_f32_16x16x32_bf16 v[100:103], v[172:175], a[172:175], v[100:103]\n"
  "buffer_load_dwordx4 a[56:59], v1, s[16:19], s71 offen offset:2048 sc1\n"
  "v_mfma_f32_16x16x32_bf16 v[104:107], v[176:179], a[168:171], v[104:107]\n"
  "v_permlane32_swap_b32 v40, v48\n"
  "v_mfma_f32_16x16x32_bf16 v[104:107], v[180:183], a[172:175], v[104:107]\n"
  "v_permlane32_swap_b32 v44, v52\n"
  "v_mfma_f32_16x16x32_bf16 v[108:111], v[184:187], a[168:171], v[108:111]\n"
  "v_permlane32_swap_b32 v41, v49\n"
  "v_mfma_f32_16x16x32_bf16 v[108:111], v[188:191], a[172:175], v[108:111]\n"
  "s_waitcnt vmcnt(9)\n"
  "v_permlane32_swap_b32 v45, v53\n"
  "v_mfma_f32_16x16x32_bf16 v[112:115], v[160:163], a[232:235], v[112:115]\n"
  "v_permlane16_swap_b32 v40, v44\n"
  "v_mfma_f32_16x16x32_bf16 v[112:115], v[164:167], a[236:239], v[112:115]\n"
  "buffer_load_dwordx4 a[60:63], v1, s[16:19], s71 offen offset:3072 sc1\n"
  "v_mfma_f32_16x16x32_bf16 v[116:119], v[168:171], a[232:235], v[116:119]\n"
  "v_permlane16_swap_b32 v48, v52\n"
  "v_mfma_f32_16x16x32_bf16 v[116:119], v[172:175], a[236:239], v[116:119]\n"
  "v_permlane16_swap_b32 v41, v45\n"
  "v_mfma_f32_16x16x32_bf16 v[120:123], v[176:179], a[232:235], v[120:123]\n"
  "v_permlane16_swap_b32 v49, v53\n"
  "v_mfma_f32_16x16x32_bf16 v[120:123], v[180:183], a[236:239], v[120:123]\n"
  "v_mfma_f32_16x16x32_bf16 v[124:127], v[184:187], a[232:235], v[124:127]\n"
  "v_mfma_f32_16x16x32_bf16 v[124:127], v[188:191], a[236:239], v[124:127]\n"
  "buffer_load_dwordx4 a[120:123], v11, s[16:19], s71 offen offset:2048 sc1\n"
  "s_waitcnt lgkmcnt(0)\n"
  "v_mfma_f32_16x16x32_bf16 v[128:131], v[192:195], a[40:43], v[128:131]\n"
  "ds_read_b128 v[160:163], v15 offset:49152\n"
  "v_mfma_f32_16x16x32_bf16 v[128:131], v[196:199], a[44:47], v[128:131]\n"
  "ds_read_b128 v[164:167], v15 offset:53248\n"
  "v_mfma_f32_16x16x32_bf16 v[132:135], v[200:203], a[40:43], v[132:135]\n"
  "ds_read_b128 v[168:171], v15 offset:50176\n"
  "v_mfma_f32_16x16x32_bf16 v[132:135], v[204:207], a[44:47], v[132:135]\n"
  "ds_read_b128 v[172:175], v15 offset:54272\n"
  "v_mfma_f32_16x16x32_bf16 v[136:139], v[192:195], a[104:107], v[136:139]\n"
  "ds_read_b128 v[176:179], v15 offset:51200\n"
  "v_mfma_f32_16x16x32_bf16 v[136:139], v[196:199], a[108:111], v[136:139]\n"
  "ds_read_b128 v[180:183], v15 offset:55296\n"
  "buffer_load_dwordx4 a[124:127], v11, s[16:19], s71 offen offset:3072 sc1\n"
  "v_mfma_f32_16x16x32_bf16 v[140:143], v[200:203], a[104:107], v[140:143]\n"
  "ds_read_b128 v[184:187], v15 offset:52224\n"
  "v_mfma_f32_16x16x32_bf16 v[140:143], v[204:207], a[108:111], v[140:143]\n"
  "ds_read_b128 v[188:191], v15 offset:56320\n"
  "v_mfma_f32_16x16x32_bf16 v[144:147], v[192:195], a[168:171], v[144:147]\n"
  "v_mfma_f32_16x16x32_bf16 v[144:147], v[196:199], a[172:175], v[144:147]\n"
  "v_mfma_f32_16x16x32_bf16 v[148:151], v[200:203], a[168:171], v[148:151]\n"
  "v_mfma_f32_16x16x32_bf16 v[148:151], v[204:207], a[172:175], v[148:151]\n"
  "buffer_load_dwordx4 a[184:187], v12, s[16:19], s71 offen offset:2048 sc1\n"
  "v_mfma_f32_16x16x32_bf16 v[152:155], v[192:195], a[232:235], v[152:155]\n"
  "v_mfma_f32_16x16x32_bf16 v[152:155], v[196:199], a[236:239], v[152:155]\n"
  "v_mfma_f32_16x16x32_bf16 v[156:159], v[200:203], a[232:235], v[156:159]\n"
  "v_mfma_f32_16x16x32_bf16 v[156:159], v[204:207], a[236:239], v[156:159]\n"
  "s_waitcnt vmcnt(11)\n"
  "s_barrier\n"
  "s_waitcnt lgkmcnt(0)\n"
  "v_mfma_f32_16x16x32_bf16 v[64:67], v[24:27], v[40:43], v[64:67]\n"
  "v_mfma_f32_16x16x32_bf16 v[64:67], v[160:163], a[48:51], v[64:67]\n"
  "ds_read_b128 v[192:195], v16 offset:24576\n"
  "buffer_load_dwordx4 a[188:191], v12, s[16:19], s71 offen offset:3072 sc1\n"
  "s_add_u32 m0, s81, 57344\n"
  "s_add_u32 s95, s13, s81\n"
  "buffer_load_dwordx4 v1, s[4:7], s95 offen offset:0 sc1 lds\n"
  "v_mfma_f32_16x16x32_bf16 v[64:67], v[164:167], a[52:55], v[64:67]\n"
  "v_mfma_f32_16x16x32_bf16 v[68:71], v[26:29], v[40:43], v[68:71]\n"
  "ds_read_b128 v[196:199], v16 offset:26624\n"
  "v_mfma_f32_16x16x32_bf16 v[68:71], v[168:171], a[48:51], v[68:71]\n"
  "v_mfma_f32_16x16x32_bf16 v[68:71], v[172:175], a[52:55], v[68:71]\n"
  "ds_read_b128 v[200:203], v16 offset:25600\n"
  "buffer_load_dwordx4 v1, s[4:7], s95 offen offset:1024 sc1 lds\n"
  "v_mfma_f32_16x16x32_bf16 v[72:75], v[28:31], v[40:43], v[72:75]\n"
  "v_mfma_f32_16x16x32_bf16 v[72:75], v[176:179], a[48:51], v[72:75]\n"
  "ds_read_b128 v[204:207], v16 offset:27648\n"
  "buffer_load_dwordx4 a[248:251], v13, s[16:19], s71 offen offset:2048 sc1\n"
  "v_mfma_f32_16x16x32_bf16 v[72:75], v[180:183], a[52:55], v[72:75]\n"
  "v_mfma_f32_16x16x32_bf16 v[76:79], v[30:33], v[40:43], v[76:79]\n"
  "buffer_load_dwordx4 v1, s[4:7], s95 offen offset:2048 sc1 lds\n"
  "v_mfma_f32_16x16x32_bf16 v[76:79], v[184:187], a[48:51], v[76:79]\n"
  "v_mfma_f32_16x16x32_bf16 v[76:79], v[188:191], a[52:55], v[76:79]\n"
  "s_waitcnt vmcnt(14)\n"
  "v_mfma_f32_16x16x32_bf16 v[80:83], v[24:27], v[44:47], v[80:83]\n"
  "v_mfma_f32_16x16x32_bf16 v[80:83], v[160:163], a[112:115], v[80:83]\n"
  "buffer_load_dwordx4 a[252:255], v13, s[16:19], s71 offen offset:3072 sc1\n"
  "buffer_load_dwordx4 v1, s[4:7], s95 offen offset:3072 sc1 lds\n"
  "v_mfma_f32_16x16x32_bf16 v[80:83], v[164:167], a[116:119], v[80:83]\n"
  "v_mfma_f32_16x16x32_bf16 v[84:87], v[26:29], v[44:47], v[84:87]\n"
  "v_mfma_f32_16x16x32_bf16 v[84:87], v[168:171], a[112:115], v[84:87]\n"
  "v_mfma_f32_16x16x32_bf16 v[84:87], v[172:175], a[116:119], v[84:87]\n"
  "s_add_u32 m0, s81, 73728\n"
  "s_add_u32 s95, s13, s81\n"
  "s_add_u32 s95, s95, 16384\n"
  "buffer_load_dwordx4 v1, s[4:7], s95 offen offset:0 sc1 lds\n"
  "v_mfma_f32_16x16x32_bf16 v[88:91], v[28:31], v[44:47], v[88:91]\n"
  "v_mfma_f32_16x16x32_bf16 v[88:91], v[176:179], a[112:115], v[88:91]\n"
  "v_mfma_f32_16x16x32_bf16 v[88:91], v[180:183], a[116:119], v[88:91]\n"
  "v_mfma_f32_16x16x32_bf16 v[92:95], v[30:33], v[44:47], v[92:95]\n"
  "buffer_load_dwordx4 v1, s[4:7], s95 offen offset:1024 sc1 lds\n"
  "v_mfma_f32_16x16x32_bf16 v[92:95], v[184:187], a[112:115], v[92:95]\n"
  "v_mfma_f32_16x16x32_bf16 v[92:95], v[188:191], a[116:119], v[92:95]\n"
  "s_waitcnt vmcnt(16)\n"
  "v_mfma_f32_16x16x32_bf16 v[96:99], v[24:27], v[48:51], v[96:99]\n"
  "v_mfma_f32_16x16x32_bf16 v[96:99], v[160:163], a[176:179], v[96:99]\n"
  "buffer_load_dwordx4 v1, s[4:7], s95 offen offset:2048 sc1 lds\n"
  "v_mfma_f32_16x16x32_bf16 v[96:99], v[164:167], a[180:183], v[96:99]\n"
  "v_mfma_f32_16x16x32_bf16 v[100:103], v[26:29], v[48:51], v[100:103]\n"
  "v_mfma_f32_16x16x32_bf16 v[100:103], v[168:171], a[176:179], v[100:103]\n"
  "v_mfma_f32_16x16x32_bf16 v[100:103], v[172:175], a[180:183], v[100:103]\n"
  "buffer_load_dwordx4 v1, s[4:7], s95 offen offset:3072 sc1 lds\n"
  "v_mfma_f32_16x16x32_bf16 v[104:107], v[28:31], v[48:51], v[104:107]\n"
  "v_mfma_f32_16x16x32_bf16 v[104:107], v[176:179], a[176:179], v[104:107]\n"
  "v_mfma_f32_16x16x32_bf16 v[104:107], v[180:183], a[180:183], v[104:107]\n"
  "v_mfma_f32_16x16x32_bf16 v[108:111], v[30:33], v[48:51], v[108:111]\n"
  "s_add_u32 m0, s81, 90112\n"
  "s_add_u32 s95, s13, s81\n"
  "s_add_u32 s95, s95, 32768\n"
  "buffer_load_dwordx4 v1, s[4:7], s95 offen offset:0 sc1 lds\n"
  "v_mfma_f32_16x16x32_bf16 v[108:111], v[184:187], a[176:179], v[108:111]\n"
  "v_mfma_f32_16x16x32_bf16 v[108:111], v[188:191], a[180:183], v[108:111]\n"
  "s_waitcnt vmcnt(17)\n"
  "v_mfma_f32_16x16x32_bf16 v[112:115], v[24:27], v[52:55], v[112:115]\n"
  "v_mfma_f32_16x16x32_bf16 v[112:115], v[160:163], a[240:243], v[112:115]\n"
  "buffer_load_dwordx4 v1, s[4:7], s95 offen offset:1024 sc1 lds\n"
  "v_mfma_f32_16x16x32_bf16 v[112:115], v[164:167], a[244:247], v[112:115]\n"
  "v_mfma_f32_16x16x32_bf16 v[116:119], v[26:29], v[52:55], v[116:119]\n"
  "v_mfma_f32_16x16x32_bf16 v[116:119], v[168:171], a[240:243], v[116:119]\n"
  "v_mfma_f32_16x16x32_bf16 v[116:119], v[172:175], a[244:247], v[116:119]\n"
  "buffer_load_dwordx4 v1, s[4:7], s95 offen offset:2048 sc1 lds\n"
  "v_mfma_f32_16x16x32_bf16 v[120:123], v[28:31], v[52:55], v[120:123]\n"
  "v_mfma_f32_16x16x32_bf16 v[120:123], v[176:179], a[240:243], v[120:123]\n"
  "v_mfma_f32_16x16x32_bf16 v[120:123], v[180:183], a[244:247], v[120:123]\n"
  "v_mfma_f32_16x16x32_bf16 v[124:127], v[30:33], v[52:55], v[124:127]\n"
  "buffer_load_dwordx4 v1, s[4:7], s95 offen offset:3072 sc1 lds\n"
  "v_mfma_f32_16x16x32_bf16 v[124:127], v[184:187], a[240:243], v[124:127]\n"
  "v_mfma_f32_16x16x32_bf16 v[124:127], v[188:191], a[244:247], v[124:127]\n"
  "s_waitcnt lgkmcnt(0)\n"
  "v_mfma_f32_16x16x32_bf16 v[128:131], v[32:35], v[40:43], v[128:131]\n"
  "ds_read_b128 v[160:163], v16 offset:28672\n"
  "v_mfma_f32_16x16x32_bf16 v[128:131], v[192:195], a[48:51], v[128:131]\n"
  "ds_read_b128 v[164:167], v16 offset:30720\n"
  "s_add_u32 m0, s81, 122880\n"
  "s_add_u32 s95, s13, s81\n"
  "s_add_u32 s95, s95, 65536\n"
  "buffer_load_dwordx4 v1, s[4:7], s95 offen offset:0 sc1 lds\n"
  "v_mfma_f32_16x16x32_bf16 v[128:131], v[196:199], a[52:55], v[128:131]\n"
  "ds_read_b128 v[168:171], v16 offset:29696\n"
  "v_mfma_f32_16x16x32_bf16 v[132:135], v[34:37], v[40:43], v[132:135]\n"
  "ds_read_b128 v[172:175], v16 offset:31744\n"
  "v_mfma_f32_16x16x32_bf16 v[132:135], v[200:203], a[48:51], v[132:135]\n"
  "v_mfma_f32_16x16x32_bf16 v[132:135], v[204:207], a[52:55], v[132:135]\n"
  "buffer_load_dwordx4 v1, s[4:7], s95 offen offset:1024 sc1 lds\n"
  "v_mfma_f32_16x16x32_bf16 v[136:139], v[32:35], v[44:47], v[136:139]\n"
  "v_mfma_f32_16x16x32_bf16 v[136:139], v[192:195], a[112:115], v[136:139]\n"
  "v_mfma_f32_16x16x32_bf16 v[136:139], v[196:199], a[116:119], v[136:139]\n"
  "v_mfma_f32_16x16x32_bf16 v[140:143], v[34:37], v[44:47], v[140:143]\n"
  "buffer_load_dwordx4 v1, s[4:7], s95 offen offset:2048 sc1 lds\n"
  "v_mfma_f32_16x16x32_bf16 v[140:143], v[200:203], a[112:115], v[140:143]\n"
  "v_mfma_f32_16x16x32_bf16 v[140:143], v[204:207], a[116:119], v[140:143]\n"
  "v_mfma_f32_16x16x32_bf16 v[144:147], v[32:35], v[48:51], v[144:147]\n"
  "v_mfma_f32_16x16x32_bf16 v[144:147], v[192:195], a[176:179], v[144:147]\n"
  "buffer_load_dwordx4 v1, s[4:7], s95 offen offset:3072 sc1 lds\n"
  "v_mfma_f32_16x16x32_bf16 v[144:147], v[196:199], a[180:183], v[144:147]\n"
  "v_mfma_f32_16x16x32_bf16 v[148:151], v[34:37], v[48:51], v[148:151]\n"
  "v_mfma_f32_16x16x32_bf16 v[148:151], v[200:203], a[176:179], v[148:151]\n"
  "v_mfma_f32_16x16x32_bf16 v[148:151], v[204:207], a[180:183], v[148:151]\n"
  "v_mfma_f32_16x16x32_bf16 v[152:155], v[32:35], v[52:55], v[152:155]\n"
  "v_mfma_f32_16x16x32_bf16 v[152:155], v[192:195], a[240:243], v[152:155]\n"
  "v_mfma_f32_16x16x32_bf16 v[152:155], v[196:199], a[244:247], v[152:155]\n"
  "v_mfma_f32_16x16x32_bf16 v[156:159], v[34:37], v[52:55], v[156:159]\n"
  "v_mfma_f32_16x16x32_bf16 v[156:159], v[200:203], a[240:243], v[156:159]\n"
  "v_mfma_f32_16x16x32_bf16 v[156:159], v[204:207], a[244:247], v[156:159]\n"
  "s_waitcnt vmcnt(22) lgkmcnt(0)\n"
  "v_mfma_f32_16x16x32_bf16 v[128:131], v[160:163], a[56:59], v[128:131]\n"
  "ds_read_b128 v[192:195], v15 offset:57344\n"
  "v_mfma_f32_16x16x32_bf16 v[128:131], v[164:167], a[60:63], v[128:131]\n"
  "ds_read_b128 v[196:199], v15 offset:61440\n"
  "v_mfma_f32_16x16x32_bf16 v[132:135], v[168:171], a[56:59], v[132:135]\n"
  "ds_read_b128 v[200:203], v15 offset:58368\n"
  "v_mfma_f32_16x16x32_bf16 v[132:135], v[172:175], a[60:63], v[132:135]\n"
  "ds_read_b128 v[204:207], v15 offset:62464\n"
  "s_waitcnt vmcnt(20)\n"
  "v_mfma_f32_16x16x32_bf16 v[136:139], v[160:163], a[120:123], v[136:139]\n"
  "ds_read_b128 v[208:211], v15 offset:59392\n"
  "v_mfma_f32_16x16x32_bf16 v[136:139], v[164:167], a[124:127], v[136:139]\n"
  "ds_read_b128 v[212:215], v15 offset:63488\n"
  "v_mfma_f32_16x16x32_bf16 v[140:143], v[168:171], a[120:123], v[140:143]\n"
  "ds_read_b128 v[216:219], v15 offset:60416\n"
  "v_mfma_f32_16x16x32_bf16 v[140:143], v[172:175], a[124:127], v[140:143]\n"
  "ds_read_b128 v[220:223], v15 offset:64512\n"
  "s_waitcnt vmcnt(18)\n"
  "v_mfma_f32_16x16x32_bf16 v[144:147], v[160:163], a[184:187], v[144:147]\n"
  "v_max_f32 v128, 0, v128\n"
  "v_max_f32 v129, 0, v129\n"
  "v_mfma_f32_16x16x32_bf16 v[144:147], v[164:167], a[188:191], v[144:147]\n"
  "v_max_f32 v130, 0, v130\n"
  "v_max_f32 v131, 0, v131\n"
  "v_mfma_f32_16x16x32_bf16 v[148:151], v[168:171], a[184:187], v[148:151]\n"
  "v_mul_f32 v128, v128, v132\n"
  "v_mul_f32 v129, v129, v133\n"
  "v_mfma_f32_16x16x32_bf16 v[148:151], v[172:175], a[188:191], v[148:151]\n"
  "s_waitcnt vmcnt(13)\n"
  "v_mul_f32 v130, v130, v134\n"
  "v_mul_f32 v131, v131, v135\n"
  "v_mfma_f32_16x16x32_bf16 v[152:155], v[160:163], a[248:251], v[152:155]\n"
  "v_cvt_pk_bf16_f32 v56, v128, v129\n"
  "v_cvt_pk_bf16_f32 v57, v130, v131\n"
  "v_mfma_f32_16x16x32_bf16 v[152:155], v[164:167], a[252:255], v[152:155]\n"
  "v_max_f32 v136, 0, v136\n"
  "v_max_f32 v137, 0, v137\n"
  "v_mfma_f32_16x16x32_bf16 v[156:159], v[168:171], a[248:251], v[156:159]\n"
  "v_max_f32 v138, 0, v138\n"
  "v_max_f32 v139, 0, v139\n"
  "v_mfma_f32_16x16x32_bf16 v[156:159], v[172:175], a[252:255], v[156:159]\n"
  "v_mul_f32 v136, v136, v140\n"
  "v_mul_f32 v137, v137, v141\n"
  "s_waitcnt lgkmcnt(0)\n"
  "v_mfma_f32_16x16x32_bf16 v[64:67], v[192:195], a[56:59], v[64:67]\n"
  "v_mul_f32 v138, v138, v142\n"
  "v_mul_f32 v139, v139, v143\n"
  "v_mfma_f32_16x16x32_bf16 v[64:67], v[196:199], a[60:63], v[64:67]\n"
  "v_cvt_pk_bf16_f32 v58, v136, v137\n"
  "v_cvt_pk_bf16_f32 v59, v138, v139\n"
  "v_mfma_f32_16x16x32_bf16 v[68:71], v[200:203], a[56:59], v[68:71]\n"
  "v_permlane32_swap_b32 v56, v58\n"
  "v_mfma_f32_16x16x32_bf16 v[68:71], v[204:207], a[60:63], v[68:71]\n"
  "v_permlane32_swap_b32 v57, v59\n"
  "v_mfma_f32_16x16x32_bf16 v[72:75], v[208:211], a[56:59], v[72:75]\n"
  "buffer_store_dwordx4 v[56:59], v14, s[16:19], s73 offen sc1\n"
  "s_add_u32 s73, s73, 262144\n"
  "v_mfma_f32_16x16x32_bf16 v[72:75], v[212:215], a[60:63], v[72:75]\n"
  "v_max_f32 v144, 0, v144\n"
  "v_max_f32 v145, 0, v145\n"
  "v_mfma_f32_16x16x32_bf16 v[76:79], v[216:219], a[56:59], v[76:79]\n"
  "v_max_f32 v146, 0, v146\n"
  "v_max_f32 v147, 0, v147\n"
  "v_mfma_f32_16x16x32_bf16 v[76:79], v[220:223], a[60:63], v[76:79]\n"
  "v_mul_f32 v144, v144, v148\n"
  "v_mul_f32 v145, v145, v149\n"
  "v_mfma_f32_16x16x32_bf16 v[80:83], v[192:195], a[120:123], v[80:83]\n"
  "v_mul_f32 v146, v146, v150\n"
  "v_mul_f32 v147, v147, v151\n"
  "v_mfma_f32_16x16x32_bf16 v[80:83], v[196:199], a[124:127], v[80:83]\n"
  "v_cvt_pk_bf16_f32 v60, v144, v145\n"
  "v_cvt_pk_bf16_f32 v61, v146, v147\n"
  "v_mfma_f32_16x16x32_bf16 v[84:87], v[200:203], a[120:123], v[84:87]\n"
  "v_max_f32 v152, 0, v152\n"
  "v_max_f32 v153, 0, v153\n"
  "v_mfma_f32_16x16x32_bf16 v[84:87], v[204:207], a[124:127], v[84:87]\n"
  "v_max_f32 v154, 0, v154\n"
  "v_max_f32 v155, 0, v155\n"
  "v_mfma_f32_16x16x32_bf16 v[88:91], v[208:211], a[120:123], v[88:91]\n"
  "v_mul_f32 v152, v152, v156\n"
  "v_mul_f32 v153, v153, v157\n"
  "v_mfma_f32_16x16x32_bf16 v[88:91], v[212:215], a[124:127], v[88:91]\n"
  "v_mul_f32 v154, v154, v158\n"
  "v_mul_f32 v155, v155, v159\n"
  "v_mfma_f32_16x16x32_bf16 v[92:95], v[216:219], a[120:123], v[92:95]\n"
  "v_cvt_pk_bf16_f32 v62, v152, v153\n"
  "v_cvt_pk_bf16_f32 v63, v154, v155\n"
  "v_mfma_f32_16x16x32_bf16 v[92:95], v[220:223], a[124:127], v[92:95]\n"
  "v_permlane32_swap_b32 v60, v62\n"
  "v_mfma_f32_16x16x32_bf16 v[96:99], v[192:195], a[184:187], v[96:99]\n"
  "v_permlane32_swap_b32 v61, v63\n"
  "v_mfma_f32_16x16x32_bf16 v[96:99], v[196:199], a[188:191], v[96:99]\n"
  "buffer_store_dwordx4 v[60:63], v14, s[16:19], s73 offen sc1\n"
  "v_max_f32 v64, 0, v64\n"
  "v_mfma_f32_16x16x32_bf16 v[100:103], v[200:203], a[184:187], v[100:103]\n"
  "v_max_f32 v65, 0, v65\n"
  "v_max_f32 v66, 0, v66\n"
  "v_mfma_f32_16x16x32_bf16 v[100:103], v[204:207], a[188:191], v[100:103]\n"
  "v_max_f32 v67, 0, v67\n"
  "v_mul_f32 v64, v64, v68\n"
  "v_mfma_f32_16x16x32_bf16 v[104:107], v[208:211], a[184:187], v[104:107]\n"
  "v_mul_f32 v65, v65, v69\n"
  "v_mul_f32 v66, v66, v70\n"
  "v_mfma_f32_16x16x32_bf16 v[104:107], v[212:215], a[188:191], v[104:107]\n"
  "v_mul_f32 v67, v67, v71\n"
  "v_cvt_pk_bf16_f32 v224, v64, v65\n"
  "v_mfma_f32_16x16x32_bf16 v[108:111], v[216:219], a[184:187], v[108:111]\n"
  "v_cvt_pk_bf16_f32 v225, v66, v67\n"
  "v_max_f32 v72, 0, v72\n"
  "v_mfma_f32_16x16x32_bf16 v[108:111], v[220:223], a[188:191], v[108:111]\n"
  "v_max_f32 v73, 0, v73\n"
  "v_max_f32 v74, 0, v74\n"
  "v_mfma_f32_16x16x32_bf16 v[112:115], v[192:195], a[248:251], v[112:115]\n"
  "v_max_f32 v75, 0, v75\n"
  "v_mul_f32 v72, v72, v76\n"
  "v_mfma_f32_16x16x32_bf16 v[112:115], v[196:199], a[252:255], v[112:115]\n"
  "v_mul_f32 v73, v73, v77\n"
  "v_mul_f32 v74, v74, v78\n"
  "v_mfma_f32_16x16x32_bf16 v[116:119], v[200:203], a[248:251], v[116:119]\n"
  "v_mul_f32 v75, v75, v79\n"
  "v_cvt_pk_bf16_f32 v226, v72, v73\n"
  "v_mfma_f32_16x16x32_bf16 v[116:119], v[204:207], a[252:255], v[116:119]\n"
  "v_cvt_pk_bf16_f32 v227, v74, v75\n"
  "buffer_store_dwordx4 v[224:227], v1, s[16:19], s72 offen sc1\n"
  "v_mfma_f32_16x16x32_bf16 v[120:123], v[208:211], a[248:251], v[120:123]\n"
  "s_add_u32 s72, s72, 131072\n"
  "v_max_f32 v80, 0, v80\n"
  "v_mfma_f32_16x16x32_bf16 v[120:123], v[212:215], a[252:255], v[120:123]\n"
  "v_max_f32 v81, 0, v81\n"
  "v_max_f32 v82, 0, v82\n"
  "v_mfma_f32_16x16x32_bf16 v[124:127], v[216:219], a[248:251], v[124:127]\n"
  "v_max_f32 v83, 0, v83\n"
  "v_mul_f32 v80, v80, v84\n"
  "v_mfma_f32_16x16x32_bf16 v[124:127], v[220:223], a[252:255], v[124:127]\n"
  "v_mul_f32 v81, v81, v85\n"
  "v_mul_f32 v82, v82, v86\n"
  "v_mul_f32 v83, v83, v87\n"
  "v_cvt_pk_bf16_f32 v228, v80, v81\n"
  "v_cvt_pk_bf16_f32 v229, v82, v83\n"
  "v_max_f32 v88, 0, v88\n"
  "v_max_f32 v89, 0, v89\n"
  "v_max_f32 v90, 0, v90\n"
  "v_max_f32 v91, 0, v91\n"
  "v_mul_f32 v88, v88, v92\n"
  "v_mul_f32 v89, v89, v93\n"
  "v_mul_f32 v90, v90, v94\n"
  "v_mul_f32 v91, v91, v95\n"
  "v_cvt_pk_bf16_f32 v230, v88, v89\n"
  "v_cvt_pk_bf16_f32 v231, v90, v91\n"
  "buffer_store_dwordx4 v[228:231], v1, s[16:19], s72 offen sc1\n"
  "s_add_u32 s72, s72, 131072\n"
  "v_max_f32 v96, 0, v96\n"
  "v_max_f32 v97, 0, v97\n"
  "v_max_f32 v98, 0, v98\n"
  "v_max_f32 v99, 0, v99\n"
  "v_pk_mul_f32 v[96:97], v[96:97], v[100:101]\n"
  "v_pk_mul_f32 v[98:99], v[98:99], v[102:103]\n"
  "v_cvt_pk_bf16_f32 v232, v96, v97\n"
  "v_cvt_pk_bf16_f32 v233, v98, v99\n"
  "v_max_f32 v104, 0, v104\n"
  "v_max_f32 v105, 0, v105\n"
  "v_max_f32 v106, 0, v106\n"
  "v_max_f32 v107, 0, v107\n"
  "v_pk_mul_f32 v[104:105], v[104:105], v[108:109]\n"
  "v_pk_mul_f32 v[106:107], v[106:107], v[110:111]\n"
  "v_cvt_pk_bf16_f32 v234, v104, v105\n"
  "v_cvt_pk_bf16_f32 v235, v106, v107\n"
  "buffer_store_dwordx4 v[232:235], v1, s[16:19], s72 offen sc1\n"
  "s_add_u32 s72, s72, 131072\n"
  "v_max_f32 v112, 0, v112\n"
  "v_max_f32 v113, 0, v113\n"
  "v_max_f32 v114, 0, v114\n"
  "v_max_f32 v115, 0, v115\n"
  "v_pk_mul_f32 v[112:113], v[112:113], v[116:117]\n"
  "v_pk_mul_f32 v[114:115], v[114:115], v[118:119]\n"
  "v_cvt_pk_bf16_f32 v236, v112, v113\n"
  "v_cvt_pk_bf16_f32 v237, v114, v115\n"
  "v_max_f32 v120, 0, v120\n"
  "v_max_f32 v121, 0, v121\n"
  "v_max_f32 v122, 0, v122\n"
  "v_max_f32 v123, 0, v123\n"
  "v_pk_mul_f32 v[120:121], v[120:121], v[124:125]\n"
  "v_pk_mul_f32 v[122:123], v[122:123], v[126:127]\n"
  "v_cvt_pk_bf16_f32 v238, v120, v121\n"
  "v_cvt_pk_bf16_f32 v239, v122, v123\n"
  "buffer_store_dwordx4 v[236:239], v1, s[16:19], s72 offen sc1\n"
  "s_branch .Lend3_42\n"
  ".Lno3_41:\n"
  "s_waitcnt vmcnt(0)\n"
  "s_barrier\n"
  "s_add_u32 m0, s81, 0\n"
  "s_add_u32 s95, s13, s81\n"
  "s_add_u32 s95, s95, 118784\n"
  "buffer_load_dwordx4 v1, s[4:7], s95 offen offset:0 sc1 lds\n"
  "buffer_load_dwordx4 v1, s[4:7], s95 offen offset:1024 sc1 lds\n"
  "buffer_load_dwordx4 v1, s[4:7], s95 offen offset:2048 sc1 lds\n"
  "buffer_load_dwordx4 v1, s[4:7], s95 offen offset:3072 sc1 lds\n"
  "s_add_u32 m0, s81, 16384\n"
  "s_add_u32 s95, s13, s81\n"
  "s_add_u32 s95, s95, 135168\n"
  "buffer_load_dwordx4 v1, s[4:7], s95 offen offset:0 sc1 lds\n"
  "buffer_load_dwordx4 v1, s[4:7], s95 offen offset:1024 sc1 lds\n"
  "buffer_load_dwordx4 v1, s[4:7], s95 offen offset:2048 sc1 lds\n"
  "buffer_load_dwordx4 v1, s[4:7], s95 offen offset:3072 sc1 lds\n"
  "s_add_u32 m0, s81, 32768\n"
  "s_add_u32 s95, s13, s81\n"
  "s_add_u32 s95, s95, 151552\n"
  "buffer_load_dwordx4 v1, s[4:7], s95 offen offset:0 sc1 lds\n"
  "buffer_load_dwordx4 v1, s[4:7], s95 offen offset:1024 sc1 lds\n"
  "buffer_load_dwordx4 v1, s[4:7], s95 offen offset:2048 sc1 lds\n"
  "buffer_load_dwordx4 v1, s[4:7], s95 offen offset:3072 sc1 lds\n"
  "s_lshl_b32 s93, s80, 11\n"
  "s_add_u32 m0, s93, 49152\n"
  "s_add_u32 s95, s93, s13\n"
  "s_add_u32 s95, s95, 167936\n"
  "buffer_load_dwordx4 v1, s[4:7], s95 offen offset:0 sc1 lds\n"
  "buffer_load_dwordx4 v1, s[4:7], s95 offen offset:1024 sc1 lds\n"
  "s_add_u32 m0, s81, 57344\n"
  "s_add_u32 s95, s13, s81\n"
  "buffer_load_dwordx4 v1, s[4:7], s95 offen offset:0 sc1 lds\n"
  "buffer_load_dwordx4 v1, s[4:7], s95 offen offset:1024 sc1 lds\n"
  "buffer_load_dwordx4 v1, s[4:7], s95 offen offset:2048 sc1 lds\n"
  "buffer_load_dwordx4 v1, s[4:7], s95 offen offset:3072 sc1 lds\n"
  "s_add_u32 m0, s81, 73728\n"
  "s_add_u32 s95, s13, s81\n"
  "s_add_u32 s95, s95, 16384\n"
  "buffer_load_dwordx4 v1, s[4:7], s95 offen offset:0 sc1 lds\n"
  "buffer_load_dwordx4 v1, s[4:7], s95 offen offset:1024 sc1 lds\n"
  "buffer_load_dwordx4 v1, s[4:7], s95 offen offset:2048 sc1 lds\n"
  "buffer_load_dwordx4 v1, s[4:7], s95 offen offset:3072 sc1 lds\n"
  "s_add_u32 m0, s81, 90112\n"
  "s_add_u32 s95, s13, s81\n"
  "s_add_u32 s95, s95, 32768\n"
  "buffer_load_dwordx4 v1, s[4:7], s95 offen offset:0 sc1 lds\n"
  "buffer_load_dwordx4 v1, s[4:7], s95 offen offset:1024 sc1 lds\n"
  "buffer_load_dwordx4 v1, s[4:7], s95 offen offset:2048 sc1 lds\n"
  "buffer_load_dwordx4 v1, s[4:7], s95 offen offset:3072 sc1 lds\n"
  "s_add_u32 m0, s81, 122880\n"
  "s_add_u32 s95, s13, s81\n"
  "s_add_u32 s95, s95, 65536\n"
  "buffer_load_dwordx4 v1, s[4:7], s95 offen offset:0 sc1 lds\n"
  "buffer_load_dwordx4 v1, s[4:7], s95 offen offset:1024 sc1 lds\n"
  "buffer_load_dwordx4 v1, s[4:7], s95 offen offset:2048 sc1 lds\n"
  "buffer_load_dwordx4 v1, s[4:7], s95 offen offset:3072 sc1 lds\n"
  "s_waitcnt vmcnt(0)\n"
  "s_barrier\n"
  ".Lend3_42:\n"
  "ds_read_b128 a[72:75], v1 offset:0\n"
  "ds_read_b128 a[76:79], v1 offset:1024\n"
  "ds_read_b128 a[80:83], v1 offset:2048\n"
  "ds_read_b128 a[84:87], v1 offset:3072\n"
  "ds_read_b128 a[88:91], v1 offset:4096\n"
  "ds_read_b128 a[92:95], v1 offset:5120\n"
  "ds_read_b128 a[96:99], v1 offset:6144\n"
  "ds_read_b128 a[100:103], v1 offset:7168\n"
  "ds_read_b128 a[104:107], v1 offset:8192\n"
  "ds_read_b128 a[108:111], v1 offset:9216\n"
  "ds_read_b128 a[112:115], v1 offset:10240\n"
  "ds_read_b128 a[116:119], v1 offset:11264\n"
  "ds_read_b128 a[120:123], v1 offset:12288\n"
  "ds_read_b128 a[124:127], v1 offset:13312\n"
  "ds_read_b128 a[128:131], v1 offset:14336\n"
  "ds_read_b128 a[132:135], v1 offset:15360\n"
  "ds_read_b128 a[136:139], v1 offset:16384\n"
  "ds_read_b128 a[140:143], v1 offset:17408\n"
  "ds_read_b128 a[144:147], v1 offset:18432\n"
  "ds_read_b128 a[148:151], v1 offset:19456\n"
  "v_lshrrev_b32 v3, 4, v2\n"
  "v_lshlrev_b32 v11, 10, v3\n"
  "v_and_b32 v3, 15, v2\n"
  "v_lshl_or_b32 v11, v3, 3, v11\n"
  "s_waitcnt lgkmcnt(0)\n"
  "s_barrier\n"
  "s_add_u32 m0, s81, 106496\n"
  "s_add_u32 s95, s13, s81\n"
  "s_add_u32 s95, s95, 49152\n"
  "buffer_load_dwordx4 v1, s[4:7], s95 offen offset:0 sc1 lds\n"
  "ds_read_b128 a[152:155], v1 offset:20480\n"
  "ds_read_b128 a[156:159], v1 offset:21504\n"
  "ds_read_b128 a[160:163], v1 offset:22528\n"
  "buffer_load_dwordx4 v1, s[4:7], s95 offen offset:1024 sc1 lds\n"
  "ds_read_b128 a[164:167], v1 offset:23552\n"
  "ds_read_b128 a[168:171], v1 offset:24576\n"
  "ds_read_b128 a[172:175], v1 offset:25600\n"
  "s_cmp_lt_u32 s87, 29\n"
  "s_cbranch_scc0 .Lskip_46\n"
  "s_waitcnt vmcnt(2)\n"
  "s_mov_b32 exec_lo, -1\n"
  "s_mov_b32 exec_hi, 0\n"
  "buffer_store_dwordx4 v[4:7], v21, s[16:19], s74 offen sc1\n"
  "s_mov_b64 exec, -1\n"
  ".Lskip_46:\n"
  "buffer_load_dwordx4 v1, s[4:7], s95 offen offset:2048 sc1 lds\n"
  "ds_read_b128 a[176:179], v1 offset:26624\n"
  "ds_read_b128 a[180:183], v1 offset:27648\n"
  "ds_read_b128 a[184:187], v1 offset:28672\n"
  "buffer_load_dwordx4 v1, s[4:7], s95 offen offset:3072 sc1 lds\n"
  "ds_read_b128 a[188:191], v1 offset:29696\n"
  "ds_read_b128 a[192:195], v1 offset:30720\n"
  "ds_read_b128 a[196:199], v1 offset:31744\n"
  "s_add_u32 m0, s81, 139264\n"
  "s_add_u32 s95, s13, s81\n"
  "s_add_u32 s95, s95, 81920\n"
  "buffer_load_dwordx4 v1, s[4:7], s95 offen offset:0 sc1 lds\n"
  "ds_read_b128 a[200:203], v1 offset:32768\n"
  "ds_read_b128 a[204:207], v1 offset:33792\n"
  "ds_read_b128 a[208:211], v1 offset:34816\n"
  "buffer_load_dwordx4 v1, s[4:7], s95 offen offset:1024 sc1 lds\n"
  "ds_read_b128 a[212:215], v1 offset:35840\n"
  "ds_read_b128 a[216:219], v1 offset:36864\n"
  "ds_read_b128 a[220:223], v1 offset:37888\n"
  "buffer_load_dwordx4 v1, s[4:7], s95 offen offset:2048 sc1 lds\n"
  "ds_read_b128 a[224:227], v1 offset:38912\n"
  "ds_read_b128 a[228:231], v1 offset:39936\n"
  "ds_read_b128 a[232:235], v1 offset:40960\n"
  "buffer_load_dwordx4 v1, s[4:7], s95 offen offset:3072 sc1 lds\n"
  "ds_read_b128 a[236:239], v1 offset:41984\n"
  "ds_read_b128 a[240:243], v1 offset:43008\n"
  "ds_read_b128 a[244:247], v1 offset:44032\n"
  "s_add_u32 m0, s81, 0\n"
  "s_add_u32 s95, s13, s81\n"
  "s_add_u32 s95, s95, 98304\n"
  "buffer_load_dwordx4 v1, s[4:7], s95 offen offset:0 sc1 lds\n"
  "ds_read_b128 a[248:251], v1 offset:45056\n"
  "ds_read_b128 a[252:255], v1 offset:46080\n"
  "ds_read_b128 v[156:159], v1 offset:47104\n"
  "buffer_load_dwordx4 v1, s[4:7], s95 offen offset:1024 sc1 lds\n"
  "ds_read_b128 v[160:163], v1 offset:48128\n"
  "ds_read_b128 v[164:167], v1 offset:49152\n"
  "ds_read_b128 v[168:171], v1 offset:50176\n"
  "buffer_load_dwordx4 v1, s[4:7], s95 offen offset:2048 sc1 lds\n"
  "ds_read_b128 v[172:175], v1 offset:51200\n"
  "ds_read_b128 v[176:179], v1 offset:52224\n"
  "ds_read_b128 v[180:183], v1 offset:53248\n"
  "buffer_load_dwordx4 v1, s[4:7], s95 offen offset:3072 sc1 lds\n"
  "ds_read_b128 v[184:187], v1 offset:54272\n"
  "ds_read_b128 v[188:191], v1 offset:55296\n"
  "ds_read_b128 v[192:195], v1 offset:56320\n"
  "s_lshl_b32 s93, s80, 10\n"
  "s_add_u32 m0, s93, 16384\n"
  "s_add_u32 s95, s93, s13\n"
  "s_add_u32 s95, s95, 114688\n"
  "buffer_load_dwordx4 v1, s[4:7], s95 offen offset:0 sc1 lds\n"
  "v_lshrrev_b32 v3, 4, v2\n"
  "v_lshlrev_b32 v22, 4, v3\n"
  "v_add_u32 v22, 156416, v22\n"
  "ds_read_b128 v[140:143], v22 offset:0\n"
  "ds_read_b128 v[144:147], v22 offset:64\n"
  "ds_read_b128 v[148:151], v22 offset:128\n"
  "ds_read_b128 v[152:155], v22 offset:192\n"
  "s_lshl_b32 s36, s80, 11\n"
  "s_add_u32 s36, s36, 24576\n"
  "v_and_b32 v3, 15, v2\n"
  "v_lshlrev_b32 v12, 7, v3\n"
  "v_and_b32 v3, 7, v2\n"
  "v_lshl_or_b32 v12, v3, 4, v12\n"
  "v_add_u32 v12, s36, v12\n"
  "v_lshrrev_b32 v3, 4, v2\n"
  "v_lshlrev_b32 v3, 3, v3\n"
  "v_xor_b32 v12, v3, v12\n"
  "v_xor_b32 v13, 32, v12\n"
  "v_xor_b32 v14, 64, v12\n"
  "v_xor_b32 v17, 96, v12\n"
  "v_lshrrev_b32 v3, 3, v2\n"
  "v_lshlrev_b32 v3, 4, v3\n"
  "v_xor_b32 v22, v3, v1\n"
  "v_add_u32 v22, s36, v22\n"
  "s_add_u32 s36, s83, 8192\n"
  "s_cmp_eq_u32 s80, 0\n"
  "s_cbranch_scc0 .Lskip_47\n"
  "s_mov_b64 exec, 0x1fffffff\n"
  "s_mov_b32 s94, 0x40000\n"
  ".Lpoll_48:\n"
  "s_waitcnt vmcnt(13)\n"
  "v_mov_b64 v[18:19], 0\n"
  "buffer_atomic_or_x2 v[18:19], v10, s[16:19], s76 offen sc0 sc1\n"
  "s_waitcnt vmcnt(0)\n"
  "v_cmp_ne_u64 vcc, s[88:89], v[18:19]\n"
  "s_cbranch_vccz .Ldone_49\n"
  "s_sub_u32 s94, s94, 1\n"
  "s_cbranch_scc0 .Lpoll_48\n"
  ".Ldone_49:\n"
  "s_mov_b64 exec, -1\n"
  ".Lskip_47:\n"
  "s_waitcnt vmcnt(0)\n"
  "s_waitcnt lgkmcnt(0)\n"
  "s_barrier\n"
  "buffer_load_dwordx4 a[0:3], v1, s[16:19], s77 offen sc1\n"
  "ds_read_b128 v[40:43], v15 offset:0\n"
  "ds_read_b128 v[44:47], v15 offset:4096\n"
  "buffer_load_dwordx4 a[4:7], v1, s[16:19], s77 offen offset:1024 sc1\n"
  "ds_read_b128 v[48:51], v15 offset:1024\n"
  "ds_read_b128 v[52:55], v15 offset:5120\n"
  "buffer_load_dwordx4 a[8:11], v1, s[16:19], s77 offen offset:2048 sc1\n"
  "ds_read_b128 v[56:59], v15 offset:2048\n"
  "ds_read_b128 v[60:63], v15 offset:6144\n"
  "buffer_load_dwordx4 a[12:15], v1, s[16:19], s77 offen offset:3072 sc1\n"
  "ds_read_b128 v[64:67], v15 offset:3072\n"
  "ds_read_b128 v[68:71], v15 offset:7168\n"
  "s_addk_i32 s77, 4096\n"
  "buffer_load_dwordx4 a[16:19], v1, s[16:19], s77 offen sc1\n"
  "ds_read_b128 v[72:75], v15 offset:8192\n"
  "ds_read_b128 v[76:79], v15 offset:12288\n"
  "buffer_load_dwordx4 a[20:23], v1, s[16:19], s77 offen offset:1024 sc1\n"
  "ds_read_b128 v[80:83], v15 offset:9216\n"
  "ds_read_b128 v[84:87], v15 offset:13312\n"
  "buffer_load_dwordx4 a[24:27], v1, s[16:19], s77 offen offset:2048 sc1\n"
  "ds_read_b128 v[88:91], v15 offset:10240\n"
  "ds_read_b128 v[92:95], v15 offset:14336\n"
  "buffer_load_dwordx4 a[28:31], v1, s[16:19], s77 offen offset:3072 sc1\n"
  "ds_read_b128 v[96:99], v15 offset:11264\n"
  "ds_read_b128 v[100:103], v15 offset:15360\n"
  "s_addk_i32 s77, 12288\n"
  "buffer_load_dwordx4 a[32:35], v1, s[16:19], s77 offen sc1\n"
  "buffer_load_dwordx4 a[36:39], v1, s[16:19], s77 offen offset:1024 sc1\n"
  "buffer_load_dwordx4 a[40:43], v1, s[16:19], s77 offen offset:2048 sc1\n"
  "buffer_load_dwordx4 a[44:47], v1, s[16:19], s77 offen offset:3072 sc1\n"
  "s_waitcnt vmcnt(9) lgkmcnt(8)\n"
  "v_mfma_f32_16x16x32_bf16 v[24:27], v[40:43], a[0:3], 0\n"
  "ds_read_b128 v[104:107], v15 offset:16384\n"
  "v_mfma_f32_16x16x32_bf16 v[24:27], v[44:47], a[4:7], v[24:27]\n"
  "ds_read_b128 v[108:111], v15 offset:20480\n"
  "s_addk_i32 s77, 4096\n"
  "buffer_load_dwordx4 a[48:51], v1, s[16:19], s77 offen sc1\n"
  "v_mfma_f32_16x16x32_bf16 v[24:27], a[72:75], a[8:11], v[24:27]\n"
  "ds_read_b128 v[112:115], v15 offset:17408\n"
  "v_mfma_f32_16x16x32_bf16 v[28:31], v[48:51], a[0:3], 0\n"
  "ds_read_b128 v[116:119], v15 offset:21504\n"
  "v_mfma_f32_16x16x32_bf16 v[28:31], v[52:55], a[4:7], v[28:31]\n"
  "ds_read_b128 v[120:123], v15 offset:18432\n"
  "v_mfma_f32_16x16x32_bf16 v[28:31], a[76:79], a[8:11], v[28:31]\n"
  "ds_read_b128 v[124:127], v15 offset:22528\n"
  "buffer_load_dwordx4 a[52:55], v1, s[16:19], s77 offen offset:1024 sc1\n"
  "v_mfma_f32_16x16x32_bf16 v[32:35], v[56:59], a[0:3], 0\n"
  "ds_read_b128 v[128:131], v15 offset:19456\n"
  "v_mfma_f32_16x16x32_bf16 v[32:35], v[60:63], a[4:7], v[32:35]\n"
  "ds_read_b128 v[132:135], v15 offset:23552\n"
  "v_mfma_f32_16x16x32_bf16 v[32:35], a[80:83], a[8:11], v[32:35]\n"
  "buffer_load_dwordx2 v[136:137], v11, s[16:19], s78 offen sc1\n"
  "v_mfma_f32_16x16x32_bf16 v[36:39], v[64:67], a[0:3], 0\n"
  "buffer_load_dwordx4 a[56:59], v1, s[16:19], s77 offen offset:2048 sc1\n"
  "v_mfma_f32_16x16x32_bf16 v[36:39], v[68:71], a[4:7], v[36:39]\n"
  "buffer_load_dwordx2 v[138:139], v11, s[16:19], s78 offen offset:512 sc1\n"
  "v_mfma_f32_16x16x32_bf16 v[36:39], a[84:87], a[8:11], v[36:39]\n"
  "s_waitcnt vmcnt(11) lgkmcnt(8)\n"
  "v_mfma_f32_16x16x32_bf16 v[24:27], v[72:75], a[12:15], v[24:27]\n"
  "ds_read_b128 v[40:43], v15 offset:24576\n"
  "v_mfma_f32_16x16x32_bf16 v[24:27], v[76:79], a[16:19], v[24:27]\n"
  "ds_read_b128 v[44:47], v15 offset:28672\n"
  "buffer_load_dwordx4 a[60:63], v1, s[16:19], s77 offen offset:3072 sc1\n"
  "v_mfma_f32_16x16x32_bf16 v[24:27], a[88:91], a[20:23], v[24:27]\n"
  "ds_read_b128 v[48:51], v15 offset:25600\n"
  "v_mfma_f32_16x16x32_bf16 v[28:31], v[80:83], a[12:15], v[28:31]\n"
  "ds_read_b128 v[52:55], v15 offset:29696\n"
  "v_mfma_f32_16x16x32_bf16 v[28:31], v[84:87], a[16:19], v[28:31]\n"
  "ds_read_b128 v[56:59], v15 offset:26624\n"
  "v_mfma_f32_16x16x32_bf16 v[28:31], a[92:95], a[20:23], v[28:31]\n"
  "ds_read_b128 v[60:63], v15 offset:30720\n"
  "s_addk_i32 s77, 12288\n"
  "buffer_load_dwordx4 a[64:67], v1, s[16:19], s77 offen sc1\n"
  "v_mfma_f32_16x16x32_bf16 v[32:35], v[88:91], a[12:15], v[32:35]\n"
  "ds_read_b128 v[64:67], v15 offset:27648\n"
  "v_mfma_f32_16x16x32_bf16 v[32:35], v[92:95], a[16:19], v[32:35]\n"
  "ds_read_b128 v[68:71], v15 offset:31744\n"
  "v_mfma_f32_16x16x32_bf16 v[32:35], a[96:99], a[20:23], v[32:35]\n"
  "v_mfma_f32_16x16x32_bf16 v[36:39], v[96:99], a[12:15], v[36:39]\n"
  "buffer_load_dwordx4 a[68:71], v1, s[16:19], s77 offen offset:1024 sc1\n"
  "v_mfma_f32_16x16x32_bf16 v[36:39], v[100:103], a[16:19], v[36:39]\n"
  "v_mfma_f32_16x16x32_bf16 v[36:39], a[100:103], a[20:23], v[36:39]\n"
  "s_waitcnt vmcnt(11) lgkmcnt(8)\n"
  "v_mfma_f32_16x16x32_bf16 v[24:27], v[104:107], a[24:27], v[24:27]\n"
  "ds_read_b128 v[72:75], v15 offset:32768\n"
  "v_mfma_f32_16x16x32_bf16 v[24:27], v[108:111], a[28:31], v[24:27]\n"
  "ds_read_b128 v[76:79], v15 offset:36864\n"
  "buffer_load_dwordx4 a[0:3], v1, s[16:19], s77 offen offset:2048 sc1\n"
  "v_mfma_f32_16x16x32_bf16 v[24:27], a[104:107], a[32:35], v[24:27]\n"
  "ds_read_b128 v[80:83], v15 offset:33792\n"
  "v_mfma_f32_16x16x32_bf16 v[28:31], v[112:115], a[24:27], v[28:31]\n"
  "ds_read_b128 v[84:87], v15 offset:37888\n"
  "v_mfma_f32_16x16x32_bf16 v[28:31], v[116:119], a[28:31], v[28:31]\n"
  "ds_read_b128 v[88:91], v15 offset:34816\n"
  "v_mfma_f32_16x16x32_bf16 v[28:31], a[108:111], a[32:35], v[28:31]\n"
  "ds_read_b128 v[92:95], v15 offset:38912\n"
  "buffer_load_dwordx4 a[4:7], v1, s[16:19], s77 offen offset:3072 sc1\n"
  "v_mfma_f32_16x16x32_bf16 v[32:35], v[120:123], a[24:27], v[32:35]\n"
  "ds_read_b128 v[96:99], v15 offset:35840\n"
  "v_mfma_f32_16x16x32_bf16 v[32:35], v[124:127], a[28:31], v[32:35]\n"
  "ds_read_b128 v[100:103], v15 offset:39936\n"
  "v_mfma_f32_16x16x32_bf16 v[32:35], a[112:115], a[32:35], v[32:35]\n"
  "v_mfma_f32_16x16x32_bf16 v[36:39], v[128:131], a[24:27], v[36:39]\n"
  "s_addk_i32 s77, 4096\n"
  "buffer_load_dwordx4 a[8:11], v1, s[16:19], s77 offen sc1\n"
  "v_mfma_f32_16x16x32_bf16 v[36:39], v[132:135], a[28:31], v[36:39]\n"
  "v_mfma_f32_16x16x32_bf16 v[36:39], a[116:119], a[32:35], v[36:39]\n"
  "s_waitcnt vmcnt(11) lgkmcnt(8)\n"
  "v_mfma_f32_16x16x32_bf16 v[24:27], v[40:43], a[36:39], v[24:27]\n"
  "ds_read_b128 v[104:107], v15 offset:40960\n"
  "v_mfma_f32_16x16x32_bf16 v[24:27], v[44:47], a[40:43], v[24:27]\n"
  "ds_read_b128 v[108:111], v15 offset:45056\n"
  "buffer_load_dwordx4 a[12:15], v1, s[16:19], s77 offen offset:1024 sc1\n"
  "v_mfma_f32_16x16x32_bf16 v[24:27], a[120:123], a[44:47], v[24:27]\n"
  "ds_read_b128 v[112:115], v15 offset:41984\n"
  "v_mfma_f32_16x16x32_bf16 v[28:31], v[48:51], a[36:39], v[28:31]\n"
  "ds_read_b128 v[116:119], v15 offset:46080\n"
  "v_mfma_f32_16x16x32_bf16 v[28:31], v[52:55], a[40:43], v[28:31]\n"
  "ds_read_b128 v[120:123], v15 offset:43008\n"
  "v_mfma_f32_16x16x32_bf16 v[28:31], a[124:127], a[44:47], v[28:31]\n"
  "ds_read_b128 v[124:127], v15 offset:47104\n"
  "buffer_load_dwordx4 a[16:19], v1, s[16:19], s77 offen offset:2048 sc1\n"
  "v_mfma_f32_16x16x32_bf16 v[32:35], v[56:59], a[36:39], v[32:35]\n"
  "ds_read_b128 v[128:131], v15 offset:44032\n"
  "v_mfma_f32_16x16x32_bf16 v[32:35], v[60:63], a[40:43], v[32:35]\n"
  "ds_read_b128 v[132:135], v15 offset:48128\n"
  "v_mfma_f32_16x16x32_bf16 v[32:35], a[128:131], a[44:47], v[32:35]\n"
  "v_mfma_f32_16x16x32_bf16 v[36:39], v[64:67], a[36:39], v[36:39]\n"
  "buffer_load_dwordx4 a[20:23], v1, s[16:19], s77 offen offset:3072 sc1\n"
  "v_mfma_f32_16x16x32_bf16 v[36:39], v[68:71], a[40:43], v[36:39]\n"
  "v_mfma_f32_16x16x32_bf16 v[36:39], a[132:135], a[44:47], v[36:39]\n"
  "s_waitcnt vmcnt(10) lgkmcnt(8)\n"
  "v_mfma_f32_16x16x32_bf16 v[24:27], v[72:75], a[48:51], v[24:27]\n"
  "ds_read_b128 v[40:43], v15 offset:49152\n"
  "v_mfma_f32_16x16x32_bf16 v[24:27], v[76:79], a[52:55], v[24:27]\n"
  "ds_read_b128 v[44:47], v15 offset:53248\n"
  "s_addk_i32 s77, 12288\n"
  "buffer_load_dwordx4 a[24:27], v1, s[16:19], s77 offen sc1\n"
  "v_mfma_f32_16x16x32_bf16 v[24:27], a[136:139], a[56:59], v[24:27]\n"
  "ds_read_b128 v[48:51], v15 offset:50176\n"
  "v_mfma_f32_16x16x32_bf16 v[28:31], v[80:83], a[48:51], v[28:31]\n"
  "ds_read_b128 v[52:55], v15 offset:54272\n"
  "v_mfma_f32_16x16x32_bf16 v[28:31], v[84:87], a[52:55], v[28:31]\n"
  "ds_read_b128 v[56:59], v15 offset:51200\n"
  "v_mfma_f32_16x16x32_bf16 v[28:31], a[140:143], a[56:59], v[28:31]\n"
  "ds_read_b128 v[60:63], v15 offset:55296\n"
  "buffer_load_dwordx4 a[28:31], v1, s[16:19], s77 offen offset:1024 sc1\n"
  "v_mfma_f32_16x16x32_bf16 v[32:35], v[88:91], a[48:51], v[32:35]\n"
  "ds_read_b128 v[64:67], v15 offset:52224\n"
  "v_mfma_f32_16x16x32_bf16 v[32:35], v[92:95], a[52:55], v[32:35]\n"
  "ds_read_b128 v[68:71], v15 offset:56320\n"
  "v_mfma_f32_16x16x32_bf16 v[32:35], a[144:147], a[56:59], v[32:35]\n"
  "v_mfma_f32_16x16x32_bf16 v[36:39], v[96:99], a[48:51], v[36:39]\n"
  "buffer_load_dwordx4 a[32:35], v1, s[16:19], s77 offen offset:2048 sc1\n"
  "v_mfma_f32_16x16x32_bf16 v[36:39], v[100:103], a[52:55], v[36:39]\n"
  "v_mfma_f32_16x16x32_bf16 v[36:39], a[148:151], a[56:59], v[36:39]\n"
  "s_waitcnt vmcnt(9) lgkmcnt(8)\n"
  "v_mfma_f32_16x16x32_bf16 v[24:27], v[104:107], a[60:63], v[24:27]\n"
  "ds_read_b128 v[72:75], v15 offset:57344\n"
  "v_mfma_f32_16x16x32_bf16 v[24:27], v[108:111], a[64:67], v[24:27]\n"
  "ds_read_b128 v[76:79], v15 offset:61440\n"
  "buffer_load_dwordx4 a[36:39], v1, s[16:19], s77 offen offset:3072 sc1\n"
  "v_mfma_f32_16x16x32_bf16 v[24:27], a[152:155], a[68:71], v[24:27]\n"
  "ds_read_b128 v[80:83], v15 offset:58368\n"
  "v_mfma_f32_16x16x32_bf16 v[28:31], v[112:115], a[60:63], v[28:31]\n"
  "ds_read_b128 v[84:87], v15 offset:62464\n"
  "v_mfma_f32_16x16x32_bf16 v[28:31], v[116:119], a[64:67], v[28:31]\n"
  "ds_read_b128 v[88:91], v15 offset:59392\n"
  "v_mfma_f32_16x16x32_bf16 v[28:31], a[156:159], a[68:71], v[28:31]\n"
  "ds_read_b128 v[92:95], v15 offset:63488\n"
  "s_addk_i32 s77, 4096\n"
  "buffer_load_dwordx4 a[40:43], v1, s[16:19], s77 offen sc1\n"
  "v_mfma_f32_16x16x32_bf16 v[32:35], v[120:123], a[60:63], v[32:35]\n"
  "ds_read_b128 v[96:99], v15 offset:60416\n"
  "v_mfma_f32_16x16x32_bf16 v[32:35], v[124:127], a[64:67], v[32:35]\n"
  "ds_read_b128 v[100:103], v15 offset:64512\n"
  "v_mfma_f32_16x16x32_bf16 v[32:35], a[160:163], a[68:71], v[32:35]\n"
  "v_mfma_f32_16x16x32_bf16 v[36:39], v[128:131], a[60:63], v[36:39]\n"
  "buffer_load_dwordx4 a[44:47], v1, s[16:19], s77 offen offset:1024 sc1\n"
  "v_mfma_f32_16x16x32_bf16 v[36:39], v[132:135], a[64:67], v[36:39]\n"
  "v_mfma_f32_16x16x32_bf16 v[36:39], a[164:167], a[68:71], v[36:39]\n"
  "s_waitcnt vmcnt(9) lgkmcnt(8)\n"
  "v_mfma_f32_16x16x32_bf16 v[24:27], v[40:43], a[0:3], v[24:27]\n"
  "ds_read_b128 v[104:107], v16 offset:0\n"
  "v_mfma_f32_16x16x32_bf16 v[24:27], v[44:47], a[4:7], v[24:27]\n"
  "ds_read_b128 v[108:111], v16 offset:4096\n"
  "buffer_load_dwordx4 a[48:51], v1, s[16:19], s77 offen offset:2048 sc1\n"
  "v_mfma_f32_16x16x32_bf16 v[24:27], a[168:171], a[8:11], v[24:27]\n"
  "ds_read_b128 v[112:115], v16 offset:1024\n"
  "v_mfma_f32_16x16x32_bf16 v[28:31], v[48:51], a[0:3], v[28:31]\n"
  "ds_read_b128 v[116:119], v16 offset:5120\n"
  "v_mfma_f32_16x16x32_bf16 v[28:31], v[52:55], a[4:7], v[28:31]\n"
  "ds_read_b128 v[120:123], v16 offset:2048\n"
  "v_mfma_f32_16x16x32_bf16 v[28:31], a[172:175], a[8:11], v[28:31]\n"
  "ds_read_b128 v[124:127], v16 offset:6144\n"
  "buffer_load_dwordx4 a[52:55], v1, s[16:19], s77 offen offset:3072 sc1\n"
  "v_mfma_f32_16x16x32_bf16 v[32:35], v[56:59], a[0:3], v[32:35]\n"
  "ds_read_b128 v[128:131], v16 offset:3072\n"
  "v_mfma_f32_16x16x32_bf16 v[32:35], v[60:63], a[4:7], v[32:35]\n"
  "ds_read_b128 v[132:135], v16 offset:7168\n"
  "v_mfma_f32_16x16x32_bf16 v[32:35], a[176:179], a[8:11], v[32:35]\n"
  "v_mfma_f32_16x16x32_bf16 v[36:39], v[64:67], a[0:3], v[36:39]\n"
  "s_addk_i32 s77, 12288\n"
  "buffer_load_dwordx4 a[56:59], v1, s[16:19], s77 offen sc1\n"
  "v_mfma_f32_16x16x32_bf16 v[36:39], v[68:71], a[4:7], v[36:39]\n"
  "v_mfma_f32_16x16x32_bf16 v[36:39], a[180:183], a[8:11], v[36:39]\n"
  "s_waitcnt vmcnt(9) lgkmcnt(8)\n"
  "v_mfma_f32_16x16x32_bf16 v[24:27], v[72:75], a[12:15], v[24:27]\n"
  "ds_read_b128 v[40:43], v16 offset:8192\n"
  "v_mfma_f32_16x16x32_bf16 v[24:27], v[76:79], a[16:19], v[24:27]\n"
  "ds_read_b128 v[44:47], v16 offset:12288\n"
  "buffer_load_dwordx4 a[60:63], v1, s[16:19], s77 offen offset:1024 sc1\n"
  "v_mfma_f32_16x16x32_bf16 v[24:27], a[184:187], a[20:23], v[24:27]\n"
  "ds_read_b128 v[48:51], v16 offset:9216\n"
  "v_mfma_f32_16x16x32_bf16 v[28:31], v[80:83], a[12:15], v[28:31]\n"
  "ds_read_b128 v[52:55], v16 offset:13312\n"
  "v_mfma_f32_16x16x32_bf16 v[28:31], v[84:87], a[16:19], v[28:31]\n"
  "ds_read_b128 v[56:59], v16 offset:10240\n"
  "v_mfma_f32_16x16x32_bf16 v[28:31], a[188:191], a[20:23], v[28:31]\n"
  "ds_read_b128 v[60:63], v16 offset:14336\n"
  "buffer_load_dwordx4 a[64:67], v1, s[16:19], s77 offen offset:2048 sc1\n"
  "v_mfma_f32_16x16x32_bf16 v[32:35], v[88:91], a[12:15], v[32:35]\n"
  "ds_read_b128 v[64:67], v16 offset:11264\n"
  "v_mfma_f32_16x16x32_bf16 v[32:35], v[92:95], a[16:19], v[32:35]\n"
  "ds_read_b128 v[68:71], v16 offset:15360\n"
  "v_mfma_f32_16x16x32_bf16 v[32:35], a[192:195], a[20:23], v[32:35]\n"
  "v_mfma_f32_16x16x32_bf16 v[36:39], v[96:99], a[12:15], v[36:39]\n"
  "buffer_load_dwordx4 a[68:71], v1, s[16:19], s77 offen offset:3072 sc1\n"
  "v_mfma_f32_16x16x32_bf16 v[36:39], v[100:103], a[16:19], v[36:39]\n"
  "v_mfma_f32_16x16x32_bf16 v[36:39], a[196:199], a[20:23], v[36:39]\n"
  "s_waitcnt vmcnt(9) lgkmcnt(8)\n"
  "v_mfma_f32_16x16x32_bf16 v[24:27], v[104:107], a[24:27], v[24:27]\n"
  "ds_read_b128 v[72:75], v16 offset:16384\n"
  "v_add_f32 v136, v136, v138\n"
  "v_mfma_f32_16x16x32_bf16 v[24:27], v[108:111], a[28:31], v[24:27]\n"
  "ds_read_b128 v[76:79], v16 offset:20480\n"
  "s_addk_i32 s77, 4096\n"
  "buffer_load_dwordx4 a[0:3], v1, s[16:19], s77 offen sc1\n"
  "v_mfma_f32_16x16x32_bf16 v[24:27], a[200:203], a[32:35], v[24:27]\n"
  "ds_read_b128 v[80:83], v16 offset:17408\n"
  "v_add_f32 v137, v137, v139\n"
  "v_mfma_f32_16x16x32_bf16 v[28:31], v[112:115], a[24:27], v[28:31]\n"
  "ds_read_b128 v[84:87], v16 offset:21504\n"
  "v_mov_b32 v138, v136\n"
  "v_mfma_f32_16x16x32_bf16 v[28:31], v[116:119], a[28:31], v[28:31]\n"
  "ds_read_b128 v[88:91], v16 offset:18432\n"
  "v_mov_b32 v139, v137\n"
  "v_mfma_f32_16x16x32_bf16 v[28:31], a[204:207], a[32:35], v[28:31]\n"
  "ds_read_b128 v[92:95], v16 offset:22528\n"
  "buffer_load_dwordx4 a[4:7], v1, s[16:19], s77 offen offset:1024 sc1\n"
  "v_mfma_f32_16x16x32_bf16 v[32:35], v[120:123], a[24:27], v[32:35]\n"
  "ds_read_b128 v[96:99], v16 offset:19456\n"
  "v_mfma_f32_16x16x32_bf16 v[32:35], v[124:127], a[28:31], v[32:35]\n"
  "ds_read_b128 v[100:103], v16 offset:23552\n"
  "v_mfma_f32_16x16x32_bf16 v[32:35], a[208:211], a[32:35], v[32:35]\n"
  "v_permlane16_swap_b32 v136, v138\n"
  "v_mfma_f32_16x16x32_bf16 v[36:39], v[128:131], a[24:27], v[36:39]\n"
  "buffer_load_dwordx4 a[8:11], v1, s[16:19], s77 offen offset:2048 sc1\n"
  "v_mfma_f32_16x16x32_bf16 v[36:39], v[132:135], a[28:31], v[36:39]\n"
  "v_permlane16_swap_b32 v137, v139\n"
  "v_mfma_f32_16x16x32_bf16 v[36:39], a[212:215], a[32:35], v[36:39]\n"
  "v_add_f32 v136, v136, v138\n"
  "v_add_f32 v137, v137, v139\n"
  "s_waitcnt vmcnt(9) lgkmcnt(8)\n"
  "v_mfma_f32_16x16x32_bf16 v[24:27], v[40:43], a[36:39], v[24:27]\n"
  "ds_read_b128 v[104:107], v16 offset:24576\n"
  "v_mov_b32 v138, v136\n"
  "v_mfma_f32_16x16x32_bf16 v[24:27], v[44:47], a[40:43], v[24:27]\n"
  "ds_read_b128 v[108:111], v16 offset:28672\n"
  "buffer_load_dwordx4 a[12:15], v1, s[16:19], s77 offen offset:3072 sc1\n"
  "v_mfma_f32_16x16x32_bf16 v[24:27], a[216:219], a[44:47], v[24:27]\n"
  "ds_read_b128 v[112:115], v16 offset:25600\n"
  "v_mov_b32 v139, v137\n"
  "v_mfma_f32_16x16x32_bf16 v[28:31], v[48:51], a[36:39], v[28:31]\n"
  "ds_read_b128 v[116:119], v16 offset:29696\n"
  "v_mfma_f32_16x16x32_bf16 v[28:31], v[52:55], a[40:43], v[28:31]\n"
  "ds_read_b128 v[120:123], v16 offset:26624\n"
  "v_mfma_f32_16x16x32_bf16 v[28:31], a[220:223], a[44:47], v[28:31]\n"
  "ds_read_b128 v[124:127], v16 offset:30720\n"
  "s_addk_i32 s77, 12288\n"
  "buffer_load_dwordx4 a[16:19], v1, s[16:19], s77 offen sc1\n"
  "v_mfma_f32_16x16x32_bf16 v[32:35], v[56:59], a[36:39], v[32:35]\n"
  "ds_read_b128 v[128:131], v16 offset:27648\n"
  "v_mfma_f32_16x16x32_bf16 v[32:35], v[60:63], a[40:43], v[32:35]\n"
  "ds_read_b128 v[132:135], v16 offset:31744\n"
  "v_mfma_f32_16x16x32_bf16 v[32:35], a[224:227], a[44:47], v[32:35]\n"
  "v_permlane32_swap_b32 v136, v138\n"
  "v_mfma_f32_16x16x32_bf16 v[36:39], v[64:67], a[36:39], v[36:39]\n"
  "buffer_load_dwordx4 a[20:23], v1, s[16:19], s77 offen offset:1024 sc1\n"
  "v_mfma_f32_16x16x32_bf16 v[36:39], v[68:71], a[40:43], v[36:39]\n"
  "v_permlane32_swap_b32 v137, v139\n"
  "v_mfma_f32_16x16x32_bf16 v[36:39], a[228:231], a[44:47], v[36:39]\n"
  "v_add_f32 v136, v136, v138\n"
  "v_add_f32 v137, v137, v139\n"
  "s_waitcnt vmcnt(9) lgkmcnt(8)\n"
  "v_mfma_f32_16x16x32_bf16 v[24:27], v[72:75], a[48:51], v[24:27]\n"
  "ds_read_b128 v[40:43], v1 offset:0\n"
  "v_mov_b32 v138, 0x3727c5ac\n"
  "v_mfma_f32_16x16x32_bf16 v[24:27], v[76:79], a[52:55], v[24:27]\n"
  "ds_read_b128 v[44:47], v1 offset:4096\n"
  "buffer_load_dwordx4 a[24:27], v1, s[16:19], s77 offen offset:2048 sc1\n"
  "v_mfma_f32_16x16x32_bf16 v[24:27], a[232:235], a[56:59], v[24:27]\n"
  "ds_read_b128 v[48:51], v1 offset:1024\n"
  "v_mul_f32 v136, 0xbb000000, v136\n"
  "v_mfma_f32_16x16x32_bf16 v[28:31], v[80:83], a[48:51], v[28:31]\n"
  "ds_read_b128 v[52:55], v1 offset:5120\n"
  "v_mul_f32 v137, 0x3b000000, v137\n"
  "v_mfma_f32_16x16x32_bf16 v[28:31], v[84:87], a[52:55], v[28:31]\n"
  "ds_read_b128 v[56:59], v1 offset:2048\n"
  "v_fma_f32 v137, -v136, v136, v137\n"
  "v_mfma_f32_16x16x32_bf16 v[28:31], a[236:239], a[56:59], v[28:31]\n"
  "ds_read_b128 v[60:63], v1 offset:6144\n"
  "v_max_f32 v137, 0, v137\n"
  "v_mfma_f32_16x16x32_bf16 v[32:35], v[88:91], a[48:51], v[32:35]\n"
  "ds_read_b128 v[64:67], v1 offset:3072\n"
  "v_add_f32 v138, v138, v137\n"
  "v_mfma_f32_16x16x32_bf16 v[32:35], v[92:95], a[52:55], v[32:35]\n"
  "ds_read_b128 v[68:71], v1 offset:7168\n"
  "v_mfma_f32_16x16x32_bf16 v[32:35], a[240:243], a[56:59], v[32:35]\n"
  "v_rcp_f32 v136, v138\n"
  "v_mfma_f32_16x16x32_bf16 v[36:39], v[96:99], a[48:51], v[36:39]\n"
  "v_mul_f32 v140, v140, v136\n"
  "v_mul_f32 v141, v141, v136\n"
  "v_mfma_f32_16x16x32_bf16 v[36:39], v[100:103], a[52:55], v[36:39]\n"
  "v_mul_f32 v142, v142, v136\n"
  "v_mul_f32 v143, v143, v136\n"
  "v_mfma_f32_16x16x32_bf16 v[36:39], a[244:247], a[56:59], v[36:39]\n"
  "v_mul_f32 v144, v144, v136\n"
  "v_mul_f32 v145, v145, v136\n"
  "s_waitcnt vmcnt(7) lgkmcnt(8)\n"
  "v_mfma_f32_16x16x32_bf16 v[24:27], v[104:107], a[60:63], v[24:27]\n"
  "ds_read_b128 v[72:75], v1 offset:8192\n"
  "v_mul_f32 v146, v146, v136\n"
  "v_mfma_f32_16x16x32_bf16 v[24:27], v[108:111], a[64:67], v[24:27]\n"
  "ds_read_b128 v[76:79], v1 offset:12288\n"
  "v_mul_f32 v147, v147, v136\n"
  "v_mfma_f32_16x16x32_bf16 v[24:27], a[248:251], a[68:71], v[24:27]\n"
  "ds_read_b128 v[80:83], v1 offset:9216\n"
  "v_mul_f32 v148, v148, v136\n"
  "v_mfma_f32_16x16x32_bf16 v[28:31], v[112:115], a[60:63], v[28:31]\n"
  "ds_read_b128 v[84:87], v1 offset:13312\n"
  "v_mul_f32 v149, v149, v136\n"
  "v_mfma_f32_16x16x32_bf16 v[28:31], v[116:119], a[64:67], v[28:31]\n"
  "ds_read_b128 v[88:91], v1 offset:10240\n"
  "v_mul_f32 v150, v150, v136\n"
  "v_mfma_f32_16x16x32_bf16 v[28:31], a[252:255], a[68:71], v[28:31]\n"
  "ds_read_b128 v[92:95], v1 offset:14336\n"
  "v_mul_f32 v151, v151, v136\n"
  "v_mfma_f32_16x16x32_bf16 v[32:35], v[120:123], a[60:63], v[32:35]\n"
  "ds_read_b128 v[96:99], v1 offset:11264\n"
  "v_mul_f32 v152, v152, v136\n"
  "v_mfma_f32_16x16x32_bf16 v[32:35], v[124:127], a[64:67], v[32:35]\n"
  "ds_read_b128 v[100:103], v1 offset:15360\n"
  "v_mul_f32 v153, v153, v136\n"
  "v_mfma_f32_16x16x32_bf16 v[32:35], v[156:159], a[68:71], v[32:35]\n"
  "v_mul_f32 v154, v154, v136\n"
  "v_mul_f32 v155, v155, v136\n"
  "v_mfma_f32_16x16x32_bf16 v[36:39], v[128:131], a[60:63], v[36:39]\n"
  "v_mfma_f32_16x16x32_bf16 v[36:39], v[132:135], a[64:67], v[36:39]\n"
  "v_mfma_f32_16x16x32_bf16 v[36:39], v[160:163], a[68:71], v[36:39]\n"
  "s_waitcnt vmcnt(4) lgkmcnt(8)\n"
  "v_mfma_f32_16x16x32_bf16 v[24:27], v[40:43], a[0:3], v[24:27]\n"
  "ds_read_b128 v[104:107], v1 offset:16384\n"
  "v_mfma_f32_16x16x32_bf16 v[24:27], v[44:47], a[4:7], v[24:27]\n"
  "ds_read_b128 v[112:115], v1 offset:17408\n"
  "v_mfma_f32_16x16x32_bf16 v[24:27], v[164:167], a[8:11], v[24:27]\n"
  "ds_read_b128 v[120:123], v1 offset:18432\n"
  "v_mfma_f32_16x16x32_bf16 v[28:31], v[48:51], a[0:3], v[28:31]\n"
  "ds_read_b128 v[128:131], v1 offset:19456\n"
  "v_mfma_f32_16x16x32_bf16 v[28:31], v[52:55], a[4:7], v[28:31]\n"
  "v_mfma_f32_16x16x32_bf16 v[28:31], v[168:171], a[8:11], v[28:31]\n"
  "v_mfma_f32_16x16x32_bf16 v[32:35], v[56:59], a[0:3], v[32:35]\n"
  "v_mfma_f32_16x16x32_bf16 v[32:35], v[60:63], a[4:7], v[32:35]\n"
  "v_mfma_f32_16x16x32_bf16 v[32:35], v[172:175], a[8:11], v[32:35]\n"
  "v_mfma_f32_16x16x32_bf16 v[36:39], v[64:67], a[0:3], v[36:39]\n"
  "v_mfma_f32_16x16x32_bf16 v[36:39], v[68:71], a[4:7], v[36:39]\n"
  "v_mfma_f32_16x16x32_bf16 v[36:39], v[176:179], a[8:11], v[36:39]\n"
  "s_waitcnt vmcnt(1) lgkmcnt(4)\n"
  "v_mfma_f32_16x16x32_bf16 v[24:27], v[72:75], a[12:15], v[24:27]\n"
  "v_mfma_f32_16x16x32_bf16 v[24:27], v[76:79], a[16:19], v[24:27]\n"
  "v_mfma_f32_16x16x32_bf16 v[24:27], v[180:183], a[20:23], v[24:27]\n"
  "v_mfma_f32_16x16x32_bf16 v[28:31], v[80:83], a[12:15], v[28:31]\n"
  "v_mfma_f32_16x16x32_bf16 v[28:31], v[84:87], a[16:19], v[28:31]\n"
  "v_mfma_f32_16x16x32_bf16 v[28:31], v[184:187], a[20:23], v[28:31]\n"
  "v_mfma_f32_16x16x32_bf16 v[32:35], v[88:91], a[12:15], v[32:35]\n"
  "v_mfma_f32_16x16x32_bf16 v[32:35], v[92:95], a[16:19], v[32:35]\n"
  "v_mfma_f32_16x16x32_bf16 v[32:35], v[188:191], a[20:23], v[32:35]\n"
  "v_mfma_f32_16x16x32_bf16 v[36:39], v[96:99], a[12:15], v[36:39]\n"
  "v_mfma_f32_16x16x32_bf16 v[36:39], v[100:103], a[16:19], v[36:39]\n"
  "v_mfma_f32_16x16x32_bf16 v[36:39], v[192:195], a[20:23], v[36:39]\n"
  "s_waitcnt vmcnt(0) lgkmcnt(0)\n"
  "v_mfma_f32_16x16x32_bf16 v[24:27], v[104:107], a[24:27], v[24:27]\n"
  "v_mfma_f32_16x16x32_bf16 v[28:31], v[112:115], a[24:27], v[28:31]\n"
  "v_mfma_f32_16x16x32_bf16 v[32:35], v[120:123], a[24:27], v[32:35]\n"
  "v_mfma_f32_16x16x32_bf16 v[36:39], v[128:131], a[24:27], v[36:39]\n"
  "s_nop 4\n"
  "v_pk_fma_f32 v[240:241], v[24:25], v[140:141], v[240:241]\n"
  "v_pk_fma_f32 v[242:243], v[26:27], v[142:143], v[242:243]\n"
  "v_cvt_pk_bf16_f32 v200, v240, v241\n"
  "v_cvt_pk_bf16_f32 v201, v242, v243\n"
  "ds_write_b64 v12, v[200:201]\n"
  "v_pk_fma_f32 v[244:245], v[28:29], v[144:145], v[244:245]\n"
  "v_pk_fma_f32 v[246:247], v[30:31], v[146:147], v[246:247]\n"
  "v_cvt_pk_bf16_f32 v202, v244, v245\n"
  "v_cvt_pk_bf16_f32 v203, v246, v247\n"
  "ds_write_b64 v13, v[202:203]\n"
  "v_pk_fma_f32 v[248:249], v[32:33], v[148:149], v[248:249]\n"
  "v_pk_fma_f32 v[250:251], v[34:35], v[150:151], v[250:251]\n"
  "v_cvt_pk_bf16_f32 v204, v248, v249\n"
  "v_cvt_pk_bf16_f32 v205, v250, v251\n"
  "ds_write_b64 v14, v[204:205]\n"
  "v_pk_fma_f32 v[252:253], v[36:37], v[152:153], v[252:253]\n"
  "v_pk_fma_f32 v[254:255], v[38:39], v[154:155], v[254:255]\n"
  "v_cvt_pk_bf16_f32 v206, v252, v253\n"
  "v_cvt_pk_bf16_f32 v207, v254, v255\n"
  "ds_write_b64 v17, v[206:207]\n"
  "ds_read_b128 v[208:211], v22 offset:0\n"
  "ds_read_b128 v[212:215], v22 offset:1024\n"
  "s_waitcnt lgkmcnt(1)\n"
  "buffer_store_dwordx4 v[208:211], v9, s[44:47], s83 offen sc1\n"
  "s_waitcnt lgkmcnt(0)\n"
  "buffer_store_dwordx4 v[212:215], v9, s[44:47], s36 offen sc1\n"
  "s_endpgm\n"
  ".Lend:\n"
  :: "s"(__builtin_amdgcn_kernarg_segment_ptr()), "s"(blockIdx.x), "v"(threadIdx.x), "s"(&tab)
  : "v1", "v2", "v3", "v4", "v5", "v6", "v7", "v8", "v9", "v10", "v11", "v12", "v13", "v14", "v15", "v16", "v17", "v18", "v19", "v20", "v21", "v22", "v23", "v24", "v25", "v26", "v27", "v28", "v29", "v30", "v31", "v32", "v33", "v34", "v35", "v36", "v37", "v38", "v39", "v40", "v41", "v42", "v43", "v44", "v45", "v46", "v47", "v48", "v49", "v50", "v51", "v52", "v53", "v54", "v55", "v56", "v57", "v58", "v59", "v60", "v61", "v62", "v63", "v64", "v65", "v66", "v67", "v68", "v69", "v70", "v71", "v72", "v73", "v74", "v75", "v76", "v77", "v78", "v79", "v80", "v81", "v82", "v83", "v84", "v85", "v86", "v87", "v88", "v89", "v90", "v91", "v92", "v93", "v94", "v95", "v96", "v97", "v98", "v99", "v100", "v101", "v102", "v103", "v104", "v105", "v106", "v107", "v108", "v109", "v110", "v111", "v112", "v113", "v114", "v115", "v116", "v117", "v118", "v119", "v120", "v121", "v122", "v123", "v124", "v125", "v126", "v127", "v128", "v129", "v130", "v131", "v132", "v133", "v134", "v135", "v136", "v137", "v138", "v139", "v140", "v141", "v142", "v143", "v144", "v145", "v146", "v147", "v148", "v149", "v150", "v151", "v152", "v153", "v154", "v155", "v156", "v157", "v158", "v159", "v160", "v161", "v162", "v163", "v164", "v165", "v166", "v167", "v168", "v169", "v170", "v171", "v172", "v173", "v174", "v175", "v176", "v177", "v178", "v179", "v180", "v181", "v182", "v183", "v184", "v185", "v186", "v187", "v188", "v189", "v190", "v191", "v192", "v193", "v194", "v195", "v196", "v197", "v198", "v199", "v200", "v201", "v202", "v203", "v204", "v205", "v206", "v207", "v208", "v209", "v210", "v211", "v212", "v213", "v214", "v215", "v216", "v217", "v218", "v219", "v220", "v221", "v222", "v223", "v224", "v225", "v226", "v227", "v228", "v229", "v230", "v231", "v232", "v233", "v234", "v235", "v236", "v237", "v238", "v239", "v240", "v241", "v242", "v243", "v244", "v245", "v246", "v247", "v248", "v249", "v250", "v251", "v252", "v253", "v254", "v255", "a0", "a1", "a2", "a3", "a4", "a5", "a6", "a7", "a8", "a9", "a10", "a11", "a12", "a13", "a14", "a15", "a16", "a17", "a18", "a19", "a20", "a21", "a22", "a23", "a24", "a25", "a26", "a27", "a28", "a29", "a30", "a31", "a32", "a33", "a34", "a35", "a36", "a37", "a38", "a39", "a40", "a41", "a42", "a43", "a44", "a45", "a46", "a47", "a48", "a49", "a50", "a51", "a52", "a53", "a54", "a55", "a56", "a57", "a58", "a59", "a60", "a61", "a62", "a63", "a64", "a65", "a66", "a67", "a68", "a69", "a70", "a71", "a72", "a73", "a74", "a75", "a76", "a77", "a78", "a79", "a80", "a81", "a82", "a83", "a84", "a85", "a86", "a87", "a88", "a89", "a90", "a91", "a92", "a93", "a94", "a95", "a96", "a97", "a98", "a99", "a100", "a101", "a102", "a103", "a104", "a105", "a106", "a107", "a108", "a109", "a110", "a111", "a112", "a113", "a114", "a115", "a116", "a117", "a118", "a119", "a120", "a121", "a122", "a123", "a124", "a125", "a126", "a127", "a128", "a129", "a130", "a131", "a132", "a133", "a134", "a135", "a136", "a137", "a138", "a139", "a140", "a141", "a142", "a143", "a144", "a145", "a146", "a147", "a148", "a149", "a150", "a151", "a152", "a153", "a154", "a155", "a156", "a157", "a158", "a159", "a160", "a161", "a162", "a163", "a164", "a165", "a166", "a167", "a168", "a169", "a170", "a171", "a172", "a173", "a174", "a175", "a176", "a177", "a178", "a179", "a180", "a181", "a182", "a183", "a184", "a185", "a186", "a187", "a188", "a189", "a190", "a191", "a192", "a193", "a194", "a195", "a196", "a197", "a198", "a199", "a200", "a201", "a202", "a203", "a204", "a205", "a206", "a207", "a208", "a209", "a210", "a211", "a212", "a213", "a214", "a215", "a216", "a217", "a218", "a219", "a220", "a221", "a222", "a223", "a224", "a225", "a226", "a227", "a228", "a229", "a230", "a231", "a232", "a233", "a234", "a235", "a236", "a237", "a238", "a239", "a240", "a241", "a242", "a243", "a244", "a245", "a246", "a247", "a248", "a249", "a250", "a251", "a252", "a253", "a254", "a255", "s3", "s4", "s5", "s6", "s7", "s8", "s9", "s10", "s11", "s12", "s13", "s14", "s15", "s16", "s17", "s18", "s19", "s20", "s21", "s22", "s23", "s24", "s25", "s26", "s27", "s28", "s29", "s30", "s31", "s33", "s34", "s35", "s36", "s37", "s38", "s39", "s40", "s41", "s42", "s43", "s44", "s45", "s46", "s47", "s48", "s49", "s50", "s51", "s52", "s53", "s54", "s55", "s56", "s57", "s58", "s59", "s60", "s61", "s62", "s63", "s64", "s65", "s66", "s67", "s68", "s69", "s70", "s71", "s72", "s73", "s74", "s75", "s76", "s77", "s78", "s79", "s80", "s81", "s82", "s83", "s84", "s85", "s86", "s87", "s88", "s89", "s90", "s91", "s92", "s93", "s94", "s95", "s96", "s97", "vcc", "scc", "memory");
}

extern "C" void kernel_launch(void* const* d_in, const int* in_sizes, int n_in, void* d_out, int out_size, void* d_ws, size_t ws_size, hipStream_t stream) {
  Params p;
  const void* ptr[11] = {d_ws, d_in[8], d_in[9], d_in[0], d_in[1], d_in[2], d_in[3], d_in[5], d_in[6], d_in[7], d_out};
  const unsigned n[11] = {41943040u, 1024u, 3145728u, 2097152u, 1572864u, 16384u, 524288u, 1409024u, 1409024u, 1409024u, 2097152u};
  for (int i = 0; i < 11; ++i) { p.r[i].p = ptr[i]; p.r[i].n = n[i]; p.r[i].f = 0x00020000u; }
  (void)hipFuncSetAttribute((const void*)fused_block, hipFuncAttributeMaxDynamicSharedMemorySize, 163840);
  fused_block<<<256, 256, 163840, stream>>>(p);
}
```
